```python
import torch

def _softmax(x, axis=-1):
    return torch.softmax(x, dim=axis)
def _gelu(x):
    return torch.nn.functional.gelu(x, approximate="tanh")
def _silu(x):
    return torch.nn.functional.silu(x)
def _sigmoid(x):
    return torch.sigmoid(x)
def _layernorm(x, axis=-1, eps=1e-5):
    m = x.mean(dim=axis, keepdim=True)
    v = x.var(dim=axis, keepdim=True, unbiased=False)
    return (x - m) / torch.sqrt(v + eps)
def _scatter_add(tgt, idx, src, axis=0):
    return tgt.clone().index_add_(axis, idx, src)
def _segment_sum(src, seg, n):
    out = torch.zeros((n,) + src.shape[1:], dtype=src.dtype)
    return out.index_add_(0, seg, src)
def _pad(x, pw):
    flat = tuple(v for pair in reversed(pw) for v in pair)
    return torch.nn.functional.pad(x, flat)

kernel_name = 'sequential_preLN__sliding_block__mixer_s_mlp__prompt'

def setup_inputs():

    _w = (lambda a: (lambda t: t.to(torch.bfloat16) if t.is_floating_point() else t)(torch.as_tensor(a)))
    def _A_setup():
        B, S, D = 1, 2048, 640
        return {
            "x": _w((lambda *s: torch.randn(*s, dtype=torch.bfloat16) * 0.05)(B, S, D)),
            "wqkv": _w((lambda *s: torch.randn(*s, dtype=torch.bfloat16) * 0.05)(D, 3 * D)),
            "wo": _w((lambda *s: torch.randn(*s, dtype=torch.bfloat16) * 0.05)(D, D)),
        }
    _A_in = _A_setup()

    def _B_setup():
        B, S, D, F = 1, 2048, 640, 1536
        return {
            "x":  _w((lambda *s: torch.randn(*s, dtype=torch.bfloat16) * 0.05)(B, S, D)),
            "w1": _w((lambda *s: torch.randn(*s, dtype=torch.bfloat16) * 3.2)(D, D) * (D ** -0.5)),
            "w2": _w((lambda *s: torch.randn(*s, dtype=torch.bfloat16) * 3.2)(D, D) * (D ** -0.5)),
            "w3": _w((lambda *s: torch.randn(*s, dtype=torch.bfloat16) * 3.2)(D, F) * (D ** -0.5)),
            "w4": _w((lambda *s: torch.randn(*s, dtype=torch.bfloat16) * 3.2)(F, D) * (F ** -0.5)),
        }
    _B_in = _B_setup()

    return {
        "A_x": _A_in["x"],
        "A_wqkv": _A_in["wqkv"],
        "A_wo": _A_in["wo"],
        "B_x": _B_in["x"],
        "B_w1": _B_in["w1"],
        "B_w2": _B_in["w2"],
        "B_w3": _B_in["w3"],
        "B_w4": _B_in["w4"],
    }

def reference(A_x, A_wqkv, A_wo, B_x, B_w1, B_w2, B_w3, B_w4):
    def _A_ref(x, wqkv, wo):
        B, S, D = x.shape
        H = 32; Dh = D // H; W = 256
        qkv = (lambda x, *d: x.permute(*d))((x @ wqkv).reshape(B, S, 3, H, Dh), 2, 0, 3, 1, 4)
        q, k, v = qkv[0], qkv[1], qkv[2]
        scores = torch.einsum("bhqd,bhkd->bhqk", q, k) * (Dh ** -0.5)
        idx = torch.arange(S)
        mask = (idx[:, None] - idx[None, :] >= 0) & (idx[:, None] - idx[None, :] < W)
        scores = torch.where(mask[None, None], scores, -1e9)
        p = _softmax(scores, axis=-1)
        o = torch.einsum("bhqk,bhkd->bhqd", p, v)
        return (lambda x, *d: x.permute(*d))(o, 0, 2, 1, 3).reshape(B, S, D) @ wo

    def _B_ref(x, w1, w2, w3, w4):
        B, S, D = x.shape
        h = _gelu(_layernorm(x, axis=-1) @ w1)
        c = D // 4
        g0 = _pad(h[:, :, 0*c:1*c], ((0, 0), (1, 0), (0, 0)))[:, :S, :]
        g1 = _pad(h[:, :, 1*c:2*c], ((0, 0), (0, 1), (0, 0)))[:, 1:, :]
        g2 = _pad(h[:, :, 2*c:3*c], ((0, 0), (2, 0), (0, 0)))[:, :S, :]
        g3 = _pad(h[:, :, 3*c:   ], ((0, 0), (0, 2), (0, 0)))[:, 2:, :]
        hs = torch.concatenate([g0, g1, g2, g3], axis=-1) @ w2
        x1 = x + hs
        h2 = _gelu(_layernorm(x1, axis=-1) @ w3) @ w4
        return x1 + h2

    x = A_x
    h = x + _A_ref(_layernorm(x, axis=-1), A_wqkv, A_wo)
    h2 = h + _B_ref(_layernorm(h, axis=-1), B_w1, B_w2, B_w3, B_w4)
    return h2
```

```cpp
#include <hip/hip_runtime.h>
#include <hip/hip_bf16.h>
typedef __hip_bfloat16 bf16;
#define S 2048
#define D 640
#define F 1536
#define H 32
#define DH 20
#define W 256
__device__ __forceinline__ float b2f(bf16 v){ return __bfloat162float(v); }
__device__ __forceinline__ bf16 f2b(float v){ return __float2bfloat16(v); }
__device__ __forceinline__ float rb(float v){ return b2f(f2b(v)); }

__global__ void ln_k(const bf16* x, bf16* y){
  int r = blockIdx.x; int t = threadIdx.x;
  float s=0; for(int i=t;i<D;i+=64) s+=b2f(x[r*D+i]);
  for(int o=32;o;o>>=1) s+=__shfl_xor(s,o);
  float m=s/D; float v=0; for(int i=t;i<D;i+=64){float d=b2f(x[r*D+i])-m; v+=d*d;}
  for(int o=32;o;o>>=1) v+=__shfl_xor(v,o);
  v/=D; float rs=rsqrtf(v+1e-5f);
  for(int i=t;i<D;i+=64) y[r*D+i]=f2b((b2f(x[r*D+i])-m)*rs);
}
__device__ __forceinline__ float gelu(float x){ return 0.5f*x*(1.f+tanhf(0.7978845608f*(x+0.044715f*x*x*x))); }
__global__ void gemm_k(const bf16* A, const bf16* Wt, bf16* C, int N, int K, int mode, const bf16* r1, const bf16* r2, int shift){
  int n = blockIdx.x*64 + threadIdx.x; int m = blockIdx.y;
  if(n>=N) return;
  float acc=0;
  for(int k=0;k<K;++k){
    int mm=m;
    if(shift){ int g=k/(D/4); mm = g==0? m-1 : g==1? m+1 : g==2? m-2 : m+2; }
    float a = (mm>=0 && mm<S)? b2f(A[(long)mm*K+k]) : 0.f;
    acc += a*b2f(Wt[(long)k*N+n]);
  }
  float v = rb(acc);
  if(mode==1) v = gelu(v);
  if(mode==2) v = b2f(r1[m*N+n]) + v;
  if(mode==3) v = b2f(r2[m*N+n]) + rb(b2f(r1[m*N+n]) + v);
  C[(long)m*N+n]=f2b(v);
}
__global__ void attn_k(const bf16* qkv, bf16* o){
  int q = blockIdx.x; int h = threadIdx.x; if(h>=H) return;
  float qv[DH]; for(int d=0;d<DH;++d) qv[d]=b2f(qkv[q*3*D+h*DH+d]);
  int k0 = q-W+1; if(k0<0)k0=0;
  float mx=-1e30f;
  for(int k=k0;k<=q;++k){ float s=0; for(int d=0;d<DH;++d) s+=qv[d]*b2f(qkv[k*3*D+D+h*DH+d]); s*=0.22360679775f; mx=fmaxf(mx,s);}
  float l=0; float ov[DH]; for(int d=0;d<DH;++d) ov[d]=0;
  for(int k=k0;k<=q;++k){ float s=0; for(int d=0;d<DH;++d) s+=qv[d]*b2f(qkv[k*3*D+D+h*DH+d]); s*=0.22360679775f; float p=__expf(s-mx); l+=p; for(int d=0;d<DH;++d) ov[d]+=p*b2f(qkv[k*3*D+2*D+h*DH+d]); }
  for(int d=0;d<DH;++d) o[q*D+h*DH+d]=f2b(ov[d]/l);
}
extern "C" void kernel_launch(void* const* d_in, const int* in_sizes, int n_in, void* d_out, int out_size, void* d_ws, size_t ws_size, hipStream_t stream){
  const bf16* x=(const bf16*)d_in[0]; const bf16* wqkv=(const bf16*)d_in[1]; const bf16* wo=(const bf16*)d_in[2];
  const bf16* w1=(const bf16*)d_in[4]; const bf16* w2=(const bf16*)d_in[5]; const bf16* w3=(const bf16*)d_in[6]; const bf16* w4=(const bf16*)d_in[7];
  bf16* ws=(bf16*)d_ws; bf16* ln1=ws; bf16* qkv=ln1+S*D; bf16* o=qkv+S*3*D; bf16* h=o+S*D; bf16* y=h+S*D; bf16* z=y+S*D; bf16* hh=z+S*D; bf16* x1=hh+S*D; bf16* l3=x1+S*D; bf16* g=l3+S*D;
  bf16* out=(bf16*)d_out;
  ln_k<<<S,64,0,stream>>>(x,ln1);
  gemm_k<<<dim3(3*D/64,S),64,0,stream>>>(ln1,wqkv,qkv,3*D,D,0,0,0,0);
  attn_k<<<S,64,0,stream>>>(qkv,o);
  gemm_k<<<dim3(D/64,S),64,0,stream>>>(o,wo,h,D,D,2,x,0,0);
  ln_k<<<S,64,0,stream>>>(h,y);
  ln_k<<<S,64,0,stream>>>(y,z);
  gemm_k<<<dim3(D/64,S),64,0,stream>>>(z,w1,hh,D,D,1,0,0,0);
  gemm_k<<<dim3(D/64,S),64,0,stream>>>(hh,w2,x1,D,D,2,y,0,1);
  ln_k<<<S,64,0,stream>>>(x1,l3);
  gemm_k<<<dim3(F/64,S),64,0,stream>>>(l3,w3,g,F,D,1,0,0,0);
  gemm_k<<<dim3(D/64,S),64,0,stream>>>(g,w4,out,D,F,3,x1,h,0);
}
```

```cpp
#include <hip/hip_runtime.h>
#include <hip/hip_bf16.h>
typedef __hip_bfloat16 bf16;
#define S 2048
#define D 640
#define F 1536
#define H 32
#define DH 20
#define W 256
__device__ __forceinline__ float b2f(bf16 v){ return __bfloat162float(v); }
__device__ __forceinline__ bf16 f2b(float v){ return __float2bfloat16(v); }
__global__ void cvt_k(const bf16* x, float* y){ int i=blockIdx.x*256+threadIdx.x; y[i]=b2f(x[i]); }
__global__ void cvt2_k(const float* x, bf16* y){ int i=blockIdx.x*256+threadIdx.x; y[i]=f2b(x[i]); }
__global__ void ln_k(const float* x, float* y){
  int r = blockIdx.x; int t = threadIdx.x;
  float s=0; for(int i=t;i<D;i+=64) s+=(x[r*D+i]);
  for(int o=32;o;o>>=1) s+=__shfl_xor(s,o);
  float m=s/D; float v=0; for(int i=t;i<D;i+=64){float d=(x[r*D+i])-m; v+=d*d;}
  for(int o=32;o;o>>=1) v+=__shfl_xor(v,o);
  v/=D; float rs=rsqrtf(v+1e-5f);
  for(int i=t;i<D;i+=64) y[r*D+i]=(((x[r*D+i])-m)*rs);
}
__device__ __forceinline__ float gelu(float x){ return 0.5f*x*(1.f+tanhf(0.7978845608f*(x+0.044715f*x*x*x))); }
__global__ void gemm_k(const float* A, const bf16* Wt, float* C, int N, int K, int mode, const float* r1, const float* r2, int shift){
  int n = blockIdx.x*64 + threadIdx.x; int m = blockIdx.y;
  if(n>=N) return;
  float acc=0;
  for(int k=0;k<K;++k){
    int mm=m;
    if(shift){ int g=k/(D/4); mm = g==0? m-1 : g==1? m+1 : g==2? m-2 : m+2; }
    float a = (mm>=0 && mm<S)? (A[(long)mm*K+k]) : 0.f;
    acc += a*b2f(Wt[(long)k*N+n]);
  }
  float v = (acc);
  if(mode==1) v = gelu(v);
  if(mode==2) v = (r1[m*N+n]) + v;
  if(mode==3) v = (r2[m*N+n]) + ((r1[m*N+n]) + v);
  C[(long)m*N+n]=(v);
}
__global__ void attn_k(const float* qkv, float* o){
  int q = blockIdx.x; int h = threadIdx.x; if(h>=H) return;
  float qv[DH]; for(int d=0;d<DH;++d) qv[d]=(qkv[q*3*D+h*DH+d]);
  int k0 = q-W+1; if(k0<0)k0=0;
  float mx=-1e30f;
  for(int k=k0;k<=q;++k){ float s=0; for(int d=0;d<DH;++d) s+=qv[d]*(qkv[k*3*D+D+h*DH+d]); s*=0.22360679775f; mx=fmaxf(mx,s);}
  float l=0; float ov[DH]; for(int d=0;d<DH;++d) ov[d]=0;
  for(int k=k0;k<=q;++k){ float s=0; for(int d=0;d<DH;++d) s+=qv[d]*(qkv[k*3*D+D+h*DH+d]); s*=0.22360679775f; float p=__expf(s-mx); l+=p; for(int d=0;d<DH;++d) ov[d]+=p*(qkv[k*3*D+2*D+h*DH+d]); }
  for(int d=0;d<DH;++d) o[q*D+h*DH+d]=(ov[d]/l);
}
extern "C" void kernel_launch(void* const* d_in, const int* in_sizes, int n_in, void* d_out, int out_size, void* d_ws, size_t ws_size, hipStream_t stream){
  const bf16* xb=(const bf16*)d_in[0]; const bf16* wqkv=(const bf16*)d_in[1]; const bf16* wo=(const bf16*)d_in[2];
  const bf16* w1=(const bf16*)d_in[4]; const bf16* w2=(const bf16*)d_in[5]; const bf16* w3=(const bf16*)d_in[6]; const bf16* w4=(const bf16*)d_in[7];
  float* ws=(float*)d_ws; float* x=ws; float* ln1=x+S*D; float* qkv=ln1+S*D; float* o=qkv+S*3*D; float* h=o+S*D; float* y=h+S*D; float* z=y+S*D; float* hh=z+S*D; float* x1=hh+S*D; float* l3=x1+S*D; float* g=l3+S*D; float* of=g+S*F;
  bf16* out=(bf16*)d_out;
  cvt_k<<<S*D/256,256,0,stream>>>(xb,x);
  ln_k<<<S,64,0,stream>>>(x,ln1);
  gemm_k<<<dim3(3*D/64,S),64,0,stream>>>(ln1,wqkv,qkv,3*D,D,0,0,0,0);
  attn_k<<<S,64,0,stream>>>(qkv,o);
  gemm_k<<<dim3(D/64,S),64,0,stream>>>(o,wo,h,D,D,2,x,0,0);
  ln_k<<<S,64,0,stream>>>(h,y);
  ln_k<<<S,64,0,stream>>>(y,z);
  gemm_k<<<dim3(D/64,S),64,0,stream>>>(z,w1,hh,D,D,1,0,0,0);
  gemm_k<<<dim3(D/64,S),64,0,stream>>>(hh,w2,x1,D,D,2,y,0,1);
  ln_k<<<S,64,0,stream>>>(x1,l3);
  gemm_k<<<dim3(F/64,S),64,0,stream>>>(l3,w3,g,F,D,1,0,0,0);
  gemm_k<<<dim3(D/64,S),64,0,stream>>>(g,w4,of,D,F,3,x1,h,0);
  cvt2_k<<<S*D/256,256,0,stream>>>(of,out);
}
```

```cpp
#include <hip/hip_runtime.h>
#include <stdint.h>

typedef unsigned short u16;
typedef short s16x4 __attribute__((ext_vector_type(4)));
typedef short s16x8 __attribute__((ext_vector_type(8)));
typedef __bf16 bf16x8 __attribute__((ext_vector_type(8)));
typedef __bf16 bf16x2 __attribute__((ext_vector_type(2)));
typedef float f32x2 __attribute__((ext_vector_type(2)));
typedef float f32x4 __attribute__((ext_vector_type(4)));
typedef unsigned u32x2 __attribute__((ext_vector_type(2)));
typedef unsigned u32x4 __attribute__((ext_vector_type(4)));
typedef __attribute__((address_space(3))) s16x4* lds_s16x4_p;

#define DEVI __device__ __forceinline__

constexpr int SEQ = 2048, DM = 640, FF = 1536, NH = 32;
constexpr float LN_EPS = 1e-5f;

template <int I> struct IC { static constexpr int v = I; };
template <int B, int E, class F> DEVI void sfor(F&& f) {
  if constexpr (B < E) { f(IC<B>{}); sfor<B + 1, E>(f); }
}
template <int N> DEVI void wait_vm() { asm volatile("s_waitcnt vmcnt(%0)" ::"n"(N) : "memory"); }
DEVI void bar() { asm volatile("s_barrier" ::: "memory"); }
DEVI void lgkm_bar() { asm volatile("s_waitcnt lgkmcnt(0)\n\ts_barrier" ::: "memory"); }

DEVI unsigned pack2(float a, float b) {
  f32x2 v = {a, b};
  bf16x2 r = __builtin_convertvector(v, bf16x2);
  return __builtin_bit_cast(unsigned, r);
}
DEVI float bflo(unsigned u) { return __builtin_bit_cast(float, u << 16); }
DEVI float bfhi(unsigned u) { return __builtin_bit_cast(float, u & 0xffff0000u); }
DEVI f32x4 mfma16(bf16x8 a, bf16x8 b, f32x4 c) { return __builtin_amdgcn_mfma_f32_16x16x32_bf16(a, b, c, 0, 0, 0); }
DEVI float gelu_t(float x) {
  float u = x * (0.7978845608028654f + 0.035677408136300125f * x * x);
  float e = __builtin_amdgcn_exp2f(-2.885390081777927f * u);
  return x * __builtin_amdgcn_rcpf(1.0f + e);
}
DEVI void glds16(const char* g, char* l) {
  __builtin_amdgcn_global_load_lds((const unsigned*)g, (unsigned*)l, 16, 0, 0);
}
DEVI bf16x8 tr_frag(const char* p0, const char* p1) {
  s16x4 a = __builtin_amdgcn_ds_read_tr16_b64_v4i16((lds_s16x4_p)p0);
  s16x4 b = __builtin_amdgcn_ds_read_tr16_b64_v4i16((lds_s16x4_p)p1);
  s16x8 f = __builtin_shufflevector(a, b, 0, 1, 2, 3, 4, 5, 6, 7);
  return __builtin_bit_cast(bf16x8, f);
}

template <int MODE> struct Cfg;
template <> struct Cfg<1> { static constexpr int BM = 128, BNL = 128, K = 640, LDB = 1920, WGM = 2, WGN = 2, MB = 4, NB = 4, NS = 5, NCS = 2; };
template <> struct Cfg<3> { static constexpr int BM = 64, BNL = 96, K = 640, LDB = 640, WGM = 4, WGN = 1, MB = 1, NB = 5, NS = 8, NCS = 0; };
template <> struct Cfg<4> { static constexpr int BM = 64, BNL = 96, K = 640, LDB = 640, WGM = 4, WGN = 1, MB = 1, NB = 5, NS = 8, NCS = 2; };
template <> struct Cfg<5> { static constexpr int BM = 64, BNL = 96, K = 640, LDB = 640, WGM = 4, WGN = 1, MB = 1, NB = 5, NS = 8, NCS = 0; };
template <> struct Cfg<6> { static constexpr int BM = 128, BNL = 96, K = 640, LDB = 1536, WGM = 2, WGN = 2, MB = 4, NB = 3, NS = 5, NCS = 2; };
template <> struct Cfg<7> { static constexpr int BM = 64, BNL = 96, K = 1536, LDB = 640, WGM = 4, WGN = 1, MB = 1, NB = 5, NS = 8, NCS = 0; };

template <int MODE>
__global__ __launch_bounds__(512) void gemm_k(const u16* __restrict__ A, const u16* __restrict__ B,
                                               u16* __restrict__ O0, u16* __restrict__ O1, u16* __restrict__ O2,
                                               const u16* __restrict__ R0, const u16* __restrict__ R1,
                                               const float* __restrict__ STI, float* __restrict__ STO) {
  using C = Cfg<MODE>;
  constexpr int BM = C::BM, BNL = C::BNL, K = C::K, LDA = C::K, LDB = C::LDB;
  constexpr int WGM = C::WGM, WGN = C::WGN, MB = C::MB, NB = C::NB, NS = C::NS, NCS = C::NCS;
  constexpr int NST = K / 64;
  constexpr int ABYTES = BM * 128, PITCH = BNL * 2, BBYTES = 64 * PITCH, SLOT = ABYTES + BBYTES;
  constexpr int AI = BM / 8, BI = BNL / 8, CH = BNL / 8, PER = (AI + BI) / 4;
  constexpr bool XCHG = (NCS > 0);
  constexpr int XOFF = ((NST - 2) % NS) * SLOT;
  __shared__ __attribute__((aligned(16))) char lds[NS * SLOT];

  const int tid = threadIdx.x, lane = tid & 63;
  const int wave = __builtin_amdgcn_readfirstlane(tid >> 6);
  const int bid = blockIdx.x, xcd = bid & 7, idx = bid >> 3;

  int rt, ct;
  if constexpr (MODE == 1) { rt = xcd * 2 + idx / 15; ct = idx % 15; }
  else if constexpr (MODE == 6) { rt = xcd * 2 + (idx >> 4); ct = idx & 15; }
  else { rt = xcd * 4 + (idx >> 3); ct = idx & 7; }
  const int row0 = rt * BM;
  int c0, cstart, cboff;
  if constexpr (MODE == 1) { c0 = ct * 128; cstart = c0; cboff = 0; }
  else if constexpr (MODE == 6) { c0 = ct * 96; cstart = c0; cboff = 0; }
  else { c0 = ct * 80; cstart = c0 & ~31; cboff = (c0 - cstart) >> 4; }

  if (wave >= 4) {
    const int lw = wave - 4;
    const char* Ab = (const char*)A + (size_t)row0 * (LDA * 2);
    const char* Bb = (const char*)B + (size_t)cstart * 2;
    const unsigned acx = (unsigned)((lane & 7) ^ (lane >> 3));
    const unsigned aoff = (unsigned)(((lane >> 3) + 8 * lw) * (LDA * 2)) + acx * 16;
    unsigned boff[BI / 4];
#pragma unroll
    for (int i = 0; i < BI / 4; ++i) {
      int li = (lw + 4 * i) * 64 + lane;
      int rho = li / CH, cp = li % CH;
      int k = (rho & ~12) | ((rho & 4) << 1) | ((rho & 8) >> 1);
      int f = (BNL == 128) ? (rho & 7) : ((rho >> 2) & 1);
      int c = (((cp >> 1) ^ f) << 1) | (cp & 1);
      boff[i] = (unsigned)(k * (LDB * 2) + c * 16);
    }
    auto issue = [&](auto SC) {
      constexpr int s = decltype(SC)::v;
      char* sl = lds + (s % NS) * SLOT + lw * 1024;
      unsigned ao = aoff;
      if constexpr (MODE == 5) {
        int col = 64 * s + 8 * (int)acx;
        int sh = col < 160 ? 1 : (col < 320 ? 3 : (col < 480 ? 0 : 4));
        ao += (unsigned)(sh * (LDA * 2));
      }
#pragma unroll
      for (int i = 0; i < AI / 4; ++i) glds16(Ab + (s * 128 + i * 32 * (LDA * 2)) + ao, sl + i * 4096);
#pragma unroll
      for (int i = 0; i < BI / 4; ++i) glds16(Bb + (size_t)s * (64 * LDB * 2) + boff[i], sl + ABYTES + i * 4096);
    };
    sfor<0, (NS < NST ? NS : NST)>([&](auto SC) { issue(SC); });
    sfor<0, NST>([&](auto SC) {
      constexpr int s = decltype(SC)::v;
      constexpr int iss0 = NS + (s > 0 ? s - 1 : 0);
      constexpr int issued = iss0 < NST ? iss0 : NST;
      wait_vm<(issued - (s + 1)) * PER>();
      bar();
      if constexpr (s >= 1 && (s - 1 + NS) < NST) issue(IC<s - 1 + NS>{});
    });
    if constexpr (XCHG) bar();
    return;
  }

  const int wm = (WGN == 1) ? wave : (wave >> 1), wn = (WGN == 1) ? 0 : (wave & 1);
  const int l15 = lane & 15, g = lane >> 4, q = l15 >> 2, p = lane & 3;
  constexpr int WROWS = BM / WGM;

  const int mrot = (MODE == 1) ? 2 * wn : 0;
  const int nrot = (MODE == 1) ? 2 * wm : (MODE == 6) ? 2 * wm : (MODE == 4) ? (2 * wave) % 5 : 0;
  int mblk[MB], nblk[NB];
#pragma unroll
  for (int i = 0; i < MB; ++i) mblk[i] = (i + mrot) % MB;
#pragma unroll
  for (int j = 0; j < NB; ++j) { int v = j + nrot; nblk[j] = v >= NB ? v - NB : v; }

  int aad[2][MB > 2 ? 2 : 1];
  {
    int base = (wm * WROWS + l15) * 128 + ((g ^ (lane & 7)) << 4);
#pragma unroll
    for (int hh = 0; hh < (MB > 2 ? 2 : 1); ++hh) {
      int b = base + mblk[hh * 2] * 2048;
      aad[0][hh] = b; aad[1][hh] = b ^ 64;
    }
  }
  int bad[NB];
  {
    const int g0 = g & 1, g1 = g >> 1;
    const int rho = q + 4 * g0 + 16 * g1;
    const int f = (BNL == 128) ? (rho & 7) : g0;
#pragma unroll
    for (int j = 0; j < NB; ++j) {
      int cb = cboff + wn * NB + nblk[j];
      bad[j] = ABYTES + rho * PITCH + ((cb ^ f) << 5) + p * 8;
    }
  }

  u32x2 r0v[NB], r1v[NB];
  f32x4 stv[MB];
  const int erow = row0 + wm * WROWS + l15;
  if constexpr (MODE == 3 || MODE == 5 || MODE == 7) {
#pragma unroll
    for (int j = 0; j < NB; ++j) r0v[j] = *(const u32x2*)(R0 + (size_t)erow * DM + c0 + 16 * j + 4 * g);
  }
  if constexpr (MODE == 7) {
#pragma unroll
    for (int j = 0; j < NB; ++j) r1v[j] = *(const u32x2*)(R1 + (size_t)erow * DM + c0 + 16 * j + 4 * g);
  }
  if constexpr (MODE == 4 || MODE == 5 || MODE == 6) {
#pragma unroll
    for (int i = 0; i < MB; ++i) stv[i] = *(const f32x4*)(STI + (size_t)(erow + 16 * i) * 16 + 4 * g);
  }

  f32x4 acc[MB][NB];
#pragma unroll
  for (int i = 0; i < MB; ++i)
#pragma unroll
    for (int j = 0; j < NB; ++j) acc[i][j] = f32x4{0.f, 0.f, 0.f, 0.f};
  f32x4 csa[XCHG ? NCS : 1], gra[2], rsa[2];
#pragma unroll
  for (int i = 0; i < (XCHG ? NCS : 1); ++i) csa[i] = f32x4{0.f, 0.f, 0.f, 0.f};
  gra[0] = gra[1] = rsa[0] = rsa[1] = f32x4{0.f, 0.f, 0.f, 0.f};
  const s16x8 ones_s = {0x3F80, 0x3F80, 0x3F80, 0x3F80, 0x3F80, 0x3F80, 0x3F80, 0x3F80};
  const bf16x8 ones = __builtin_bit_cast(bf16x8, ones_s);

  bf16x8 fa[2][MB], fb[2][NB];
  auto readf = [&](auto SC, auto HC) {
    constexpr int s = decltype(SC)::v, h = decltype(HC)::v;
    const char* sl = lds + (s % NS) * SLOT;
#pragma unroll
    for (int i = 0; i < MB; ++i) fa[h][i] = *(const bf16x8*)(sl + aad[h][i >> 1] + (i & 1) * 2048);
#pragma unroll
    for (int j = 0; j < NB; ++j) fb[h][j] = tr_frag(sl + bad[j] + h * 32 * PITCH, sl + bad[j] + (h * 32 + 8) * PITCH);
  };
  auto mainloop = [&](auto SW) {
    constexpr bool swapped = decltype(SW)::v != 0;
    auto mm = [&](auto HC) {
      constexpr int h = decltype(HC)::v;
#pragma unroll
      for (int i = 0; i < MB; ++i)
#pragma unroll
        for (int j = 0; j < NB; ++j)
          acc[i][j] = swapped ? mfma16(fb[h][j], fa[h][i], acc[i][j]) : mfma16(fa[h][i], fb[h][j], acc[i][j]);
      if constexpr (XCHG) {
#pragma unroll
        for (int j = 0; j < NCS; ++j) csa[j] = mfma16(ones, fb[h][j], csa[j]);
      }
      if constexpr (MODE == 1) {
#pragma unroll
        for (int i = 0; i < 2; ++i) {
          gra[i] = mfma16(fa[h][i], fa[h][i], gra[i]);
          rsa[i] = mfma16(ones, fa[h][i], rsa[i]);
        }
      }
    };
    bar();
    readf(IC<0>{}, IC<0>{});
    sfor<0, NST>([&](auto SC) {
      constexpr int s = decltype(SC)::v;
      readf(SC, IC<1>{});
      mm(IC<0>{});
      if constexpr (s + 1 < NST) {
        lgkm_bar();
        readf(IC<s + 1>{}, IC<0>{});
      }
      mm(IC<1>{});
    });
  };
  bool isV = false;
  if constexpr (MODE == 1) {
    isV = ct >= 10;
    if (isV) mainloop(IC<0>{}); else mainloop(IC<1>{});
  } else {
    mainloop(IC<1>{});
  }

  float* xs = (float*)(lds + XOFF);
  if constexpr (XCHG) {
    if (g == 0) {
#pragma unroll
      for (int j = 0; j < NCS; ++j) xs[(wn * NB + nblk[j]) * 16 + l15] = csa[j][0];
    }
    if constexpr (MODE == 1) {
#pragma unroll
      for (int i = 0; i < 2; ++i) {
        int r = (wm * MB + mblk[i]) * 16 + l15;
        if (g == 0) xs[128 + r] = rsa[i][0];
        if (g == q) {
          float v = p == 0 ? gra[i][0] : p == 1 ? gra[i][1] : p == 2 ? gra[i][2] : gra[i][3];
          xs[256 + r] = v;
        }
      }
    }
    lgkm_bar();
  }

  if constexpr (MODE == 1) {
    constexpr float INVK = 1.0f / K;
    if (!isV) {
      const bool isQ = ct < 5;
      u16* dst = isQ ? O0 : O1;
      const float sc = isQ ? 0.22360679774997896f * 1.4426950408889634f : 1.0f;
      const int cbase = c0 - (isQ ? 0 : 640) + wn * 64 + 4 * g;
#pragma unroll
      for (int i = 0; i < MB; ++i) {
        const int rl = (wm * MB + mblk[i]) * 16 + l15;
        const float mean = xs[128 + rl] * INVK;
        const float var = fmaxf(xs[256 + rl] * INVK - mean * mean, 0.f);
        const float rs = __builtin_amdgcn_rsqf(var + LN_EPS) * sc;
        const int rb = (row0 + rl) >> 4;
#pragma unroll
        for (int j = 0; j < NB; ++j) {
          const f32x4 cs = *(const f32x4*)(xs + (wn * NB + nblk[j]) * 16 + 4 * g);
          const int c = cbase + 16 * nblk[j];
          const int head = c / 20, quad = (c - head * 20) >> 2;
          u32x2 o;
          o[0] = pack2((acc[i][j][0] - mean * cs[0]) * rs, (acc[i][j][1] - mean * cs[1]) * rs);
          o[1] = pack2((acc[i][j][2] - mean * cs[2]) * rs, (acc[i][j][3] - mean * cs[3]) * rs);
          *(u32x2*)(dst + (size_t)(((head * 128 + rb) * 5 + quad) * 64 + l15 * 4)) = o;
        }
      }
    } else {
      f32x4 mean[MB], rs[MB];
#pragma unroll
      for (int i = 0; i < MB; ++i) {
        const int rl = (wm * MB + mblk[i]) * 16 + 4 * g;
        const f32x4 sm = *(const f32x4*)(xs + 128 + rl);
        const f32x4 sq = *(const f32x4*)(xs + 256 + rl);
#pragma unroll
        for (int r = 0; r < 4; ++r) {
          mean[i][r] = sm[r] * INVK;
          rs[i][r] = __builtin_amdgcn_rsqf(fmaxf(sq[r] * INVK - mean[i][r] * mean[i][r], 0.f) + LN_EPS);
        }
      }
#pragma unroll
      for (int j = 0; j < NB; ++j) {
        const int cl = (wn * NB + nblk[j]) * 16 + l15;
        const float cs = xs[cl];
        const int c = c0 - 1280 + cl;
        const int head = c / 20, d = c - head * 20;
        const int chunk = g ^ ((d >> 2) & 2);
#pragma unroll
        for (int ip = 0; ip < 2; ++ip) {
          const int i0 = 2 * ip, i1 = 2 * ip + 1;
          const int tile = (row0 + (wm * MB + mblk[i0]) * 16) >> 5;
          u32x4 o;
          o[0] = pack2((acc[i0][j][0] - mean[i0][0] * cs) * rs[i0][0], (acc[i0][j][1] - mean[i0][1] * cs) * rs[i0][1]);
          o[1] = pack2((acc[i0][j][2] - mean[i0][2] * cs) * rs[i0][2], (acc[i0][j][3] - mean[i0][3] * cs) * rs[i0][3]);
          o[2] = pack2((acc[i1][j][0] - mean[i1][0] * cs) * rs[i1][0], (acc[i1][j][1] - mean[i1][1] * cs) * rs[i1][1]);
          o[3] = pack2((acc[i1][j][2] - mean[i1][2] * cs) * rs[i1][2], (acc[i1][j][3] - mean[i1][3] * cs) * rs[i1][3]);
          *(u32x4*)(O2 + (size_t)(((head * 64 + tile) * 20 + d) * 32 + chunk * 8)) = o;
        }
      }
    }
  } else {
    float mean[MB], rstd[MB], var[MB];
    if constexpr (MODE == 4 || MODE == 5 || MODE == 6) {
#pragma unroll
      for (int i = 0; i < MB; ++i) {
        float sm = stv[i][0] + stv[i][2], sq = stv[i][1] + stv[i][3];
        sm += __shfl_xor(sm, 16); sq += __shfl_xor(sq, 16);
        sm += __shfl_xor(sm, 32); sq += __shfl_xor(sq, 32);
        mean[i] = sm * (1.0f / DM);
        var[i] = fmaxf(sq * (1.0f / DM) - mean[i] * mean[i], 0.f);
        rstd[i] = __builtin_amdgcn_rsqf(var[i] + LN_EPS);
      }
    }
    if constexpr (MODE == 3 || MODE == 5) {
      float sm = 0.f, sq = 0.f;
#pragma unroll
      for (int j = 0; j < NB; ++j) {
        float r[4] = {bflo(r0v[j][0]), bfhi(r0v[j][0]), bflo(r0v[j][1]), bfhi(r0v[j][1])};
        float v[4];
#pragma unroll
        for (int e = 0; e < 4; ++e) {
          if constexpr (MODE == 3) v[e] = r[e] + acc[0][j][e];
          else v[e] = (r[e] - mean[0]) * rstd[0] + acc[0][j][e];
        }
        u32x2 o; o[0] = pack2(v[0], v[1]); o[1] = pack2(v[2], v[3]);
        *(u32x2*)(O0 + (size_t)erow * DM + c0 + 16 * j + 4 * g) = o;
        float w[4] = {bflo(o[0]), bfhi(o[0]), bflo(o[1]), bfhi(o[1])};
#pragma unroll
        for (int e = 0; e < 4; ++e) { sm += w[e]; sq += w[e] * w[e]; }
      }
      sm += __shfl_xor(sm, 16); sq += __shfl_xor(sq, 16);
      sm += __shfl_xor(sm, 32); sq += __shfl_xor(sq, 32);
      if (g == 0) *(f32x2*)(STO + (size_t)erow * 16 + ct * 2) = f32x2{sm, sq};
    } else if constexpr (MODE == 4) {
      const float r2 = __builtin_amdgcn_rsqf(var[0] * rstd[0] * rstd[0] + LN_EPS);
      const float rs = rstd[0] * r2;
#pragma unroll
      for (int j = 0; j < NB; ++j) {
        const f32x4 cs = *(const f32x4*)(xs + nblk[j] * 16 + 4 * g);
        float v[4];
#pragma unroll
        for (int e = 0; e < 4; ++e) v[e] = gelu_t((acc[0][j][e] - mean[0] * cs[e]) * rs);
        u32x2 o; o[0] = pack2(v[0], v[1]); o[1] = pack2(v[2], v[3]);
        *(u32x2*)(O0 + (size_t)(erow + 2) * DM + c0 + 16 * nblk[j] + 4 * g) = o;
      }
      if ((rt == 0 || rt == 31) && wave == 0 && lane < 40) {
        const int gr = (rt == 0 ? 0 : SEQ + 2) + (lane >= 20 ? 1 : 0);
        const int cc = c0 + 4 * (lane >= 20 ? lane - 20 : lane);
        *(u32x2*)(O0 + (size_t)gr * DM + cc) = u32x2{0u, 0u};
      }
    } else if constexpr (MODE == 6) {
#pragma unroll
      for (int j = 0; j < NB; ++j) {
        const f32x4 cs = *(const f32x4*)(xs + (wn * NB + nblk[j]) * 16 + 4 * g);
#pragma unroll
        for (int i = 0; i < MB; ++i) {
          float v[4];
#pragma unroll
          for (int e = 0; e < 4; ++e) v[e] = gelu_t((acc[i][j][e] - mean[i] * cs[e]) * rstd[i]);
          u32x2 o; o[0] = pack2(v[0], v[1]); o[1] = pack2(v[2], v[3]);
          *(u32x2*)(O0 + (size_t)(erow + 16 * i) * FF + c0 + (wn * NB + nblk[j]) * 16 + 4 * g) = o;
        }
      }
    } else if constexpr (MODE == 7) {
#pragma unroll
      for (int j = 0; j < NB; ++j) {
        float hv[4] = {bflo(r0v[j][0]), bfhi(r0v[j][0]), bflo(r0v[j][1]), bfhi(r0v[j][1])};
        float xv[4] = {bflo(r1v[j][0]), bfhi(r1v[j][0]), bflo(r1v[j][1]), bfhi(r1v[j][1])};
        float v[4];
#pragma unroll
        for (int e = 0; e < 4; ++e) v[e] = hv[e] + (xv[e] + acc[0][j][e]);
        u32x2 o; o[0] = pack2(v[0], v[1]); o[1] = pack2(v[2], v[3]);
        *(u32x2*)(O0 + (size_t)erow * DM + c0 + 16 * j + 4 * g) = o;
      }
    }
  }
}

__global__ __launch_bounds__(512) void attn_k(const u16* __restrict__ Qg, const u16* __restrict__ Kg,
                                               const u16* __restrict__ Vg, u16* __restrict__ Og) {
  __shared__ __attribute__((aligned(16))) char lds[20480 * 2 + 32];
  const int tid = threadIdx.x, lane = tid & 63;
  const int wave = __builtin_amdgcn_readfirstlane(tid >> 6);
  const int chunk = blockIdx.x & 7, head = blockIdx.x >> 3;
  const int l15 = lane & 15, g = lane >> 4;
  char* Kl = lds;
  char* Vl = lds + 20480;

  const char* Ksrc = (const char*)Kg + (size_t)head * (128 * 640) + (ptrdiff_t)(chunk - 1) * 10240 + lane * 16;
  const char* Vsrc = (const char*)Vg + (size_t)head * (64 * 1280) + (ptrdiff_t)(chunk - 1) * 10240 + lane * 16;
#pragma unroll
  for (int i = 0; i < 3; ++i) {
    const int n = wave + 8 * i;
    if (n < 20 && (chunk > 0 || n >= 10)) glds16(Ksrc + n * 1024, Kl + n * 1024);
  }
  const int qb0 = chunk * 16 + wave * 2;
  u32x2 qa0, qa1, qb0v, qb1v;
  {
    const u16* qp = Qg + (size_t)(head * 128 + qb0) * 320 + l15 * 4;
    qa0 = *(const u32x2*)(qp + g * 64);
    qa1 = *(const u32x2*)(qp + 256);
    qb0v = *(const u32x2*)(qp + 320 + g * 64);
    qb1v = *(const u32x2*)(qp + 320 + 256);
  }
  if (chunk == 0) {
#pragma unroll
    for (int i = 0; i < 2; ++i) {
      const int o = (tid + 512 * i) * 16;
      if (o < 10240) {
        *(u32x4*)(Kl + o) = u32x4{0u, 0u, 0u, 0u};
        *(u32x4*)(Vl + o) = u32x4{0u, 0u, 0u, 0u};
      }
    }
  }
  if (tid < 2) {
    const unsigned v = tid == 0 ? 0x3F803F80u : 0u;
    *(u32x4*)(lds + 40960 + tid * 16) = u32x4{v, v, v, v};
  }
  if (g != 0) { qa1 = u32x2{0u, 0u}; qb1v = u32x2{0u, 0u}; }
  const bf16x8 qA = __builtin_bit_cast(bf16x8, u32x4{qa0[0], qa0[1], qa1[0], qa1[1]});
  const bf16x8 qB = __builtin_bit_cast(bf16x8, u32x4{qb0v[0], qb0v[1], qb1v[0], qb1v[1]});
  asm volatile("s_waitcnt vmcnt(0) lgkmcnt(0)\n\ts_barrier" ::: "memory");

  f32x4 sA[17], sB[17];
  {
    const char* kp = Kl + wave * 1280 + l15 * 8;
    const char* kp0 = kp + g * 128;
    const f32x4 z = {0.f, 0.f, 0.f, 0.f};
#pragma unroll
    for (int i = 0; i < 18; ++i) {
      const u32x2 k0 = *(const u32x2*)(kp0 + i * 640);
      const u32x2 k1 = *(const u32x2*)(kp + 512 + i * 640);
      const bf16x8 kf = __builtin_bit_cast(bf16x8, u32x4{k0[0], k0[1], k1[0], k1[1]});
      if (i < 17) sA[i] = mfma16(kf, qA, z);
      if (i > 0) sB[i - 1] = mfma16(kf, qB, z);
    }
  }
#pragma unroll
  for (int i = 0; i < 3; ++i) {
    const int n = wave + 8 * i;
    if (n < 20 && (chunk > 0 || n >= 10)) glds16(Vsrc + n * 1024, Vl + n * 1024);
  }
  const float NEG = -__builtin_inff();
#pragma unroll
  for (int r = 0; r < 4; ++r) {
    const bool gt = (4 * g + r) > l15;
    sA[0][r] = gt ? sA[0][r] : NEG;
    sB[0][r] = gt ? sB[0][r] : NEG;
    sA[16][r] = gt ? NEG : sA[16][r];
    sB[16][r] = gt ? NEG : sB[16][r];
  }
  if (chunk == 0) {
#pragma unroll
    for (int i = 0; i < 16; ++i) {
      if (qb0 - 16 + i < 0) sA[i] = f32x4{NEG, NEG, NEG, NEG};
      if (qb0 - 15 + i < 0) sB[i] = f32x4{NEG, NEG, NEG, NEG};
    }
  }
  float mA = sA[16][0], mB = sB[16][0];
#pragma unroll
  for (int i = 0; i < 17; ++i) {
    mA = fmaxf(fmaxf(mA, sA[i][0]), sA[i][1]); mA = fmaxf(fmaxf(mA, sA[i][2]), sA[i][3]);
    mB = fmaxf(fmaxf(mB, sB[i][0]), sB[i][1]); mB = fmaxf(fmaxf(mB, sB[i][2]), sB[i][3]);
  }
  mA = fmaxf(mA, __shfl_xor(mA, 16)); mB = fmaxf(mB, __shfl_xor(mB, 16));
  mA = fmaxf(mA, __shfl_xor(mA, 32)); mB = fmaxf(mB, __shfl_xor(mB, 32));
  unsigned pA[18][2], pB[18][2];
#pragma unroll
  for (int i = 0; i < 17; ++i) {
    pA[i][0] = pack2(__builtin_amdgcn_exp2f(sA[i][0] - mA), __builtin_amdgcn_exp2f(sA[i][1] - mA));
    pA[i][1] = pack2(__builtin_amdgcn_exp2f(sA[i][2] - mA), __builtin_amdgcn_exp2f(sA[i][3] - mA));
    pB[i + 1][0] = pack2(__builtin_amdgcn_exp2f(sB[i][0] - mB), __builtin_amdgcn_exp2f(sB[i][1] - mB));
    pB[i + 1][1] = pack2(__builtin_amdgcn_exp2f(sB[i][2] - mB), __builtin_amdgcn_exp2f(sB[i][3] - mB));
  }
  pA[17][0] = pA[17][1] = 0u; pB[0][0] = pB[0][1] = 0u;
  asm volatile("s_waitcnt vmcnt(0) lgkmcnt(0)\n\ts_barrier" ::: "memory");

  f32x4 oA0 = {0.f, 0.f, 0.f, 0.f}, oA1 = oA0, oB0 = oA0, oB1 = oA0;
  {
    const char* vp0 = Vl + wave * 1280 + l15 * 64 + ((g ^ ((l15 >> 2) & 2)) << 4);
    int v1off = (l15 < 4) ? (20480 + wave * 1280 + (16 + l15) * 64 + g * 16) : (l15 == 4 ? 40960 : 40976);
    const int v1str = (l15 < 4) ? 1280 : 0;
#pragma unroll
    for (int t = 0; t < 9; ++t) {
      const bf16x8 v0 = *(const bf16x8*)(vp0 + t * 1280);
      const bf16x8 v1 = *(const bf16x8*)(lds + v1off);
      v1off += v1str;
      const bf16x8 fA = __builtin_bit_cast(bf16x8, u32x4{pA[2 * t][0], pA[2 * t][1], pA[2 * t + 1][0], pA[2 * t + 1][1]});
      const bf16x8 fB = __builtin_bit_cast(bf16x8, u32x4{pB[2 * t][0], pB[2 * t][1], pB[2 * t + 1][0], pB[2 * t + 1][1]});
      oA0 = mfma16(v0, fA, oA0); oA1 = mfma16(v1, fA, oA1);
      oB0 = mfma16(v0, fB, oB0); oB1 = mfma16(v1, fB, oB1);
    }
  }
  const float dA = __builtin_amdgcn_rcpf(__shfl(oA1[0], l15 + 16));
  const float dB = __builtin_amdgcn_rcpf(__shfl(oB1[0], l15 + 16));
  u16* op = Og + (size_t)(qb0 * 16 + l15) * DM + head * 20;
  *(u32x2*)(op + 4 * g) = u32x2{pack2(oA0[0] * dA, oA0[1] * dA), pack2(oA0[2] * dA, oA0[3] * dA)};
  *(u32x2*)(op + 16 * DM + 4 * g) = u32x2{pack2(oB0[0] * dB, oB0[1] * dB), pack2(oB0[2] * dB, oB0[3] * dB)};
  if (g == 0) {
    *(u32x2*)(op + 16) = u32x2{pack2(oA1[0] * dA, oA1[1] * dA), pack2(oA1[2] * dA, oA1[3] * dA)};
    *(u32x2*)(op + 16 * DM + 16) = u32x2{pack2(oB1[0] * dB, oB1[1] * dB), pack2(oB1[2] * dB, oB1[3] * dB)};
  }
}

extern "C" void kernel_launch(void* const* d_in, const int* in_sizes, int n_in, void* d_out, int out_size,
                              void* d_ws, size_t ws_size, hipStream_t stream) {
  const u16* x = (const u16*)d_in[0];
  const u16* wqkv = (const u16*)d_in[1];
  const u16* wo = (const u16*)d_in[2];
  const u16* w1 = (const u16*)d_in[4];
  const u16* w2 = (const u16*)d_in[5];
  const u16* w3 = (const u16*)d_in[6];
  const u16* w4 = (const u16*)d_in[7];
  u16* out = (u16*)d_out;
  char* ws = (char*)d_ws;
  constexpr size_t MB4 = 4u << 20;
  u16* Qb = (u16*)(ws + 0 * MB4);
  u16* Kb = (u16*)(ws + 1 * MB4);
  u16* Vb = (u16*)(ws + 2 * MB4);
  u16* ob = (u16*)(ws + 3 * MB4);
  u16* hb = (u16*)(ws + 4 * MB4);
  u16* hh = (u16*)(ws + 5 * MB4);
  u16* x1 = (u16*)(ws + 6 * MB4);
  u16* gb = (u16*)(ws + 7 * MB4);
  float* sth = (float*)(ws + 9 * MB4);
  float* stx = (float*)(ws + 9 * MB4 + (1u << 20));

  gemm_k<1><<<240, 512, 0, stream>>>(x, wqkv, Qb, Kb, Vb, nullptr, nullptr, nullptr, nullptr);
  attn_k<<<256, 512, 0, stream>>>(Qb, Kb, Vb, ob);
  gemm_k<3><<<256, 512, 0, stream>>>(ob, wo, hb, nullptr, nullptr, x, nullptr, nullptr, sth);
  gemm_k<4><<<256, 512, 0, stream>>>(hb, w1, hh, nullptr, nullptr, nullptr, nullptr, sth, nullptr);
  gemm_k<5><<<256, 512, 0, stream>>>(hh, w2, x1, nullptr, nullptr, hb, nullptr, sth, stx);
  gemm_k<6><<<256, 512, 0, stream>>>(x1, w3, gb, nullptr, nullptr, nullptr, nullptr, stx, nullptr);
  gemm_k<7><<<256, 512, 0, stream>>>(gb, w4, out, nullptr, nullptr, hb, x1, nullptr, nullptr);
}
```

```cpp
#include <hip/hip_runtime.h>
#include <stdint.h>

typedef unsigned short u16;
typedef short s16x4 __attribute__((ext_vector_type(4)));
typedef short s16x8 __attribute__((ext_vector_type(8)));
typedef __bf16 bf16x8 __attribute__((ext_vector_type(8)));
typedef __bf16 bf16x2 __attribute__((ext_vector_type(2)));
typedef float f32x2 __attribute__((ext_vector_type(2)));
typedef float f32x4 __attribute__((ext_vector_type(4)));
typedef unsigned u32x2 __attribute__((ext_vector_type(2)));
typedef unsigned u32x4 __attribute__((ext_vector_type(4)));
typedef __attribute__((address_space(3))) s16x4* lds_s16x4_p;

#define DEVI __device__ __forceinline__
#define STAMP(kid, i)

constexpr int SEQ = 2048, DM = 640, FF = 1536, NH = 32;
constexpr float LN_EPS = 1e-5f;

template <int I> struct IC { static constexpr int v = I; };
template <int B, int E, class F> DEVI void sfor(F&& f) {
  if constexpr (B < E) { f(IC<B>{}); sfor<B + 1, E>(f); }
}
template <int N> DEVI void wait_vm() { asm volatile("s_waitcnt vmcnt(%0)" ::"n"(N) : "memory"); }
DEVI void bar() { asm volatile("s_barrier" ::: "memory"); }
DEVI void lgkm_bar() { asm volatile("s_waitcnt lgkmcnt(0)\n\ts_barrier" ::: "memory"); }

DEVI unsigned pack2(float a, float b) {
  f32x2 v = {a, b};
  bf16x2 r = __builtin_convertvector(v, bf16x2);
  return __builtin_bit_cast(unsigned, r);
}
DEVI float bflo(unsigned u) { return __builtin_bit_cast(float, u << 16); }
DEVI float bfhi(unsigned u) { return __builtin_bit_cast(float, u & 0xffff0000u); }
DEVI f32x4 mfma16(bf16x8 a, bf16x8 b, f32x4 c) { return __builtin_amdgcn_mfma_f32_16x16x32_bf16(a, b, c, 0, 0, 0); }
DEVI float gelu_t(float x) {
  float u = x * (0.7978845608028654f + 0.035677408136300125f * x * x);
  float e = __builtin_amdgcn_exp2f(-2.885390081777927f * u);
  return x * __builtin_amdgcn_rcpf(1.0f + e);
}
DEVI void glds16(const char* g, char* l) {
  __builtin_amdgcn_global_load_lds((const unsigned*)g, (unsigned*)l, 16, 0, 0);
}
DEVI bf16x8 tr_frag(const char* p0, const char* p1) {
  s16x4 a = __builtin_amdgcn_ds_read_tr16_b64_v4i16((lds_s16x4_p)p0);
  s16x4 b = __builtin_amdgcn_ds_read_tr16_b64_v4i16((lds_s16x4_p)p1);
  s16x8 f = __builtin_shufflevector(a, b, 0, 1, 2, 3, 4, 5, 6, 7);
  return __builtin_bit_cast(bf16x8, f);
}

template <int MODE> struct Cfg;
template <> struct Cfg<1> { static constexpr int BM = 128, BNL = 128, K = 640, LDB = 1920, WGM = 2, WGN = 2, MB = 4, NB = 4, NS = 5, NCS = 2; };
template <> struct Cfg<3> { static constexpr int BM = 64, BNL = 96, K = 640, LDB = 640, WGM = 4, WGN = 1, MB = 1, NB = 5, NS = 8, NCS = 0; };
template <> struct Cfg<4> { static constexpr int BM = 64, BNL = 96, K = 640, LDB = 640, WGM = 4, WGN = 1, MB = 1, NB = 5, NS = 8, NCS = 2; };
template <> struct Cfg<5> { static constexpr int BM = 64, BNL = 96, K = 640, LDB = 640, WGM = 4, WGN = 1, MB = 1, NB = 5, NS = 8, NCS = 0; };
template <> struct Cfg<6> { static constexpr int BM = 128, BNL = 96, K = 640, LDB = 1536, WGM = 2, WGN = 2, MB = 4, NB = 3, NS = 5, NCS = 2; };
template <> struct Cfg<7> { static constexpr int BM = 64, BNL = 96, K = 1536, LDB = 640, WGM = 4, WGN = 1, MB = 1, NB = 5, NS = 8, NCS = 0; };

template <int MODE>
__global__ __launch_bounds__(512) void gemm_k(const u16* __restrict__ A, const u16* __restrict__ B,
                                               u16* __restrict__ O0, u16* __restrict__ O1, u16* __restrict__ O2,
                                               const u16* __restrict__ R0, const u16* __restrict__ R1,
                                               const float* __restrict__ STI, float* __restrict__ STO) {
  using C = Cfg<MODE>;
  constexpr int BM = C::BM, BNL = C::BNL, K = C::K, LDA = C::K, LDB = C::LDB;
  constexpr int WGM = C::WGM, WGN = C::WGN, MB = C::MB, NB = C::NB, NS = C::NS, NCS = C::NCS;
  constexpr int NST = K / 64;
  constexpr int ABYTES = BM * 128, PITCH = BNL * 2, BBYTES = 64 * PITCH, SLOT = ABYTES + BBYTES;
  constexpr int AI = BM / 8, BI = BNL / 8, CH = BNL / 8, PER = (AI + BI) / 4;
  constexpr bool XCHG = (NCS > 0);
  constexpr int XOFF = ((NST - 2) % NS) * SLOT;
  __shared__ __attribute__((aligned(16))) char lds[NS * SLOT];

  const int tid = threadIdx.x, lane = tid & 63;
  const int wave = __builtin_amdgcn_readfirstlane(tid >> 6);
  const int bid = blockIdx.x, xcd = bid & 7, idx = bid >> 3;
  STAMP(MODE, 0); STAMP(MODE, 1);

  int rt, ct;
  if constexpr (MODE == 1) { rt = xcd * 2 + idx / 15; ct = idx % 15; }
  else if constexpr (MODE == 6) { rt = xcd * 2 + (idx >> 4); ct = idx & 15; }
  else { rt = xcd * 4 + (idx >> 3); ct = idx & 7; }
  const int row0 = rt * BM;
  int c0, cstart, cboff;
  if constexpr (MODE == 1) { c0 = ct * 128; cstart = c0; cboff = 0; }
  else if constexpr (MODE == 6) { c0 = ct * 96; cstart = c0; cboff = 0; }
  else { c0 = ct * 80; cstart = c0 & ~31; cboff = (c0 - cstart) >> 4; }

  if (wave >= 4) {
    const int lw = wave - 4;
    const char* Ab = (const char*)A + (size_t)row0 * (LDA * 2);
    const char* Bb = (const char*)B + (size_t)cstart * 2;
    const unsigned acx = (unsigned)((lane & 7) ^ (lane >> 3));
    const unsigned aoff = (unsigned)(((lane >> 3) + 8 * lw) * (LDA * 2)) + acx * 16;
    unsigned boff[BI / 4];
#pragma unroll
    for (int i = 0; i < BI / 4; ++i) {
      int li = (lw + 4 * i) * 64 + lane;
      int rho = li / CH, cp = li % CH;
      int k = (rho & ~12) | ((rho & 4) << 1) | ((rho & 8) >> 1);
      int f = (BNL == 128) ? (rho & 7) : ((rho >> 2) & 1);
      int c = (((cp >> 1) ^ f) << 1) | (cp & 1);
      boff[i] = (unsigned)(k * (LDB * 2) + c * 16);
    }
    auto issue = [&](auto SC) {
      constexpr int s = decltype(SC)::v;
      char* sl = lds + (s % NS) * SLOT + lw * 1024;
      unsigned ao = aoff;
      if constexpr (MODE == 5) {
        int col = 64 * s + 8 * (int)acx;
        int sh = col < 160 ? 1 : (col < 320 ? 3 : (col < 480 ? 0 : 4));
        ao += (unsigned)(sh * (LDA * 2));
      }
#pragma unroll
      for (int i = 0; i < AI / 4; ++i) glds16(Ab + (s * 128 + i * 32 * (LDA * 2)) + ao, sl + i * 4096);
#pragma unroll
      for (int i = 0; i < BI / 4; ++i) glds16(Bb + (size_t)s * (64 * LDB * 2) + boff[i], sl + ABYTES + i * 4096);
    };
    STAMP(MODE, 2);
    sfor<0, (NS < NST ? NS : NST)>([&](auto SC) { issue(SC); });
    STAMP(MODE, 3);
    sfor<0, NST>([&](auto SC) {
      constexpr int s = decltype(SC)::v;
      constexpr int iss0 = NS + (s > 0 ? s - 1 : 0);
      constexpr int issued = iss0 < NST ? iss0 : NST;
      wait_vm<(issued - (s + 1)) * PER>();
      bar();
      if constexpr (s == 0) STAMP(MODE, 4);
      if constexpr (s == NST - 1) STAMP(MODE, 5);
      if constexpr (s >= 1 && (s - 1 + NS) < NST) issue(IC<s - 1 + NS>{});
    });
    if constexpr (XCHG) bar();
    return;
  }

  const int wm = (WGN == 1) ? wave : (wave >> 1), wn = (WGN == 1) ? 0 : (wave & 1);
  const int l15 = lane & 15, g = lane >> 4, q = l15 >> 2, p = lane & 3;
  constexpr int WROWS = BM / WGM;

  const int mrot = (MODE == 1) ? 2 * wn : 0;
  const int nrot = (MODE == 1) ? 2 * wm : (MODE == 6) ? 2 * wm : (MODE == 4) ? (2 * wave) % 5 : 0;
  int mblk[MB], nblk[NB];
#pragma unroll
  for (int i = 0; i < MB; ++i) mblk[i] = (i + mrot) % MB;
#pragma unroll
  for (int j = 0; j < NB; ++j) { int v = j + nrot; nblk[j] = v >= NB ? v - NB : v; }

  int aad[2][MB > 2 ? 2 : 1];
  {
    int base = (wm * WROWS + l15) * 128 + ((g ^ (lane & 7)) << 4);
#pragma unroll
    for (int hh = 0; hh < (MB > 2 ? 2 : 1); ++hh) {
      int b = base + mblk[hh * 2] * 2048;
      aad[0][hh] = b; aad[1][hh] = b ^ 64;
    }
  }
  int bad[NB];
  {
    const int g0 = g & 1, g1 = g >> 1;
    const int rho = q + 4 * g0 + 16 * g1;
    const int f = (BNL == 128) ? (rho & 7) : g0;
#pragma unroll
    for (int j = 0; j < NB; ++j) {
      int cb = cboff + wn * NB + nblk[j];
      bad[j] = ABYTES + rho * PITCH + ((cb ^ f) << 5) + p * 8;
    }
  }

  u32x2 r0v[NB], r1v[NB];
  f32x4 stv[MB];
  const int erow = row0 + wm * WROWS + l15;
  if constexpr (MODE == 3 || MODE == 5 || MODE == 7) {
#pragma unroll
    for (int j = 0; j < NB; ++j) r0v[j] = *(const u32x2*)(R0 + (size_t)erow * DM + c0 + 16 * j + 4 * g);
  }
  if constexpr (MODE == 7) {
#pragma unroll
    for (int j = 0; j < NB; ++j) r1v[j] = *(const u32x2*)(R1 + (size_t)erow * DM + c0 + 16 * j + 4 * g);
  }
  if constexpr (MODE == 4 || MODE == 5 || MODE == 6) {
#pragma unroll
    for (int i = 0; i < MB; ++i) stv[i] = *(const f32x4*)(STI + (size_t)(erow + 16 * i) * 16 + 4 * g);
  }

  f32x4 acc[MB][NB];
#pragma unroll
  for (int i = 0; i < MB; ++i)
#pragma unroll
    for (int j = 0; j < NB; ++j) acc[i][j] = f32x4{0.f, 0.f, 0.f, 0.f};
  f32x4 csa[XCHG ? NCS : 1], gra[2], rsa[2];
#pragma unroll
  for (int i = 0; i < (XCHG ? NCS : 1); ++i) csa[i] = f32x4{0.f, 0.f, 0.f, 0.f};
  gra[0] = gra[1] = rsa[0] = rsa[1] = f32x4{0.f, 0.f, 0.f, 0.f};
  const s16x8 ones_s = {0x3F80, 0x3F80, 0x3F80, 0x3F80, 0x3F80, 0x3F80, 0x3F80, 0x3F80};
  const bf16x8 ones = __builtin_bit_cast(bf16x8, ones_s);

  bf16x8 fa[2][MB], fb[2][NB];
  auto readf = [&](auto SC, auto HC) {
    constexpr int s = decltype(SC)::v, h = decltype(HC)::v;
    const char* sl = lds + (s % NS) * SLOT;
#pragma unroll
    for (int i = 0; i < MB; ++i) fa[h][i] = *(const bf16x8*)(sl + aad[h][i >> 1] + (i & 1) * 2048);
#pragma unroll
    for (int j = 0; j < NB; ++j) fb[h][j] = tr_frag(sl + bad[j] + h * 32 * PITCH, sl + bad[j] + (h * 32 + 8) * PITCH);
  };
  auto mainloop = [&](auto SW) {
    constexpr bool swapped = decltype(SW)::v != 0;
    auto mm = [&](auto HC) {
      constexpr int h = decltype(HC)::v;
#pragma unroll
      for (int i = 0; i < MB; ++i)
#pragma unroll
        for (int j = 0; j < NB; ++j)
          acc[i][j] = swapped ? mfma16(fb[h][j], fa[h][i], acc[i][j]) : mfma16(fa[h][i], fb[h][j], acc[i][j]);
      if constexpr (XCHG) {
#pragma unroll
        for (int j = 0; j < NCS; ++j) csa[j] = mfma16(ones, fb[h][j], csa[j]);
      }
      if constexpr (MODE == 1) {
#pragma unroll
        for (int i = 0; i < 2; ++i) {
          gra[i] = mfma16(fa[h][i], fa[h][i], gra[i]);
          rsa[i] = mfma16(ones, fa[h][i], rsa[i]);
        }
      }
    };
    STAMP(MODE, 2);
    bar();
    STAMP(MODE, 3);
    readf(IC<0>{}, IC<0>{});
    sfor<0, NST>([&](auto SC) {
      constexpr int s = decltype(SC)::v;
      readf(SC, IC<1>{});
      mm(IC<0>{});
      if constexpr (s + 1 < NST) {
        lgkm_bar();
        readf(IC<s + 1>{}, IC<0>{});
      }
      mm(IC<1>{});
    });
  };
  bool isV = false;
  if constexpr (MODE == 1) {
    isV = ct >= 10;
    if (isV) mainloop(IC<0>{}); else mainloop(IC<1>{});
  } else {
    mainloop(IC<1>{});
  }

  STAMP(MODE, 4);
  float* xs = (float*)(lds + XOFF);
  if constexpr (XCHG) {
    if (g == 0) {
#pragma unroll
      for (int j = 0; j < NCS; ++j) xs[(wn * NB + nblk[j]) * 16 + l15] = csa[j][0];
    }
    if constexpr (MODE == 1) {
#pragma unroll
      for (int i = 0; i < 2; ++i) {
        int r = (wm * MB + mblk[i]) * 16 + l15;
        if (g == 0) xs[128 + r] = rsa[i][0];
        if (g == q) {
          float v = p == 0 ? gra[i][0] : p == 1 ? gra[i][1] : p == 2 ? gra[i][2] : gra[i][3];
          xs[256 + r] = v;
        }
      }
    }
    lgkm_bar();
  }

  STAMP(MODE, 5);
  if constexpr (MODE == 1) {
    constexpr float INVK = 1.0f / K;
    if (!isV) {
      const bool isQ = ct < 5;
      u16* dst = isQ ? O0 : O1;
      const float sc = isQ ? 0.22360679774997896f * 1.4426950408889634f : 1.0f;
      const int cbase = c0 - (isQ ? 0 : 640) + wn * 64 + 4 * g;
#pragma unroll
      for (int i = 0; i < MB; ++i) {
        const int rl = (wm * MB + mblk[i]) * 16 + l15;
        const float mean = xs[128 + rl] * INVK;
        const float var = fmaxf(xs[256 + rl] * INVK - mean * mean, 0.f);
        const float rs = __builtin_amdgcn_rsqf(var + LN_EPS) * sc;
        const int rb = (row0 + rl) >> 4;
#pragma unroll
        for (int j = 0; j < NB; ++j) {
          const f32x4 cs = *(const f32x4*)(xs + (wn * NB + nblk[j]) * 16 + 4 * g);
          const int c = cbase + 16 * nblk[j];
          const int head = c / 20, quad = (c - head * 20) >> 2;
          u32x2 o;
          o[0] = pack2((acc[i][j][0] - mean * cs[0]) * rs, (acc[i][j][1] - mean * cs[1]) * rs);
          o[1] = pack2((acc[i][j][2] - mean * cs[2]) * rs, (acc[i][j][3] - mean * cs[3]) * rs);
          *(u32x2*)(dst + (size_t)(((head * 128 + rb) * 5 + quad) * 64 + l15 * 4)) = o;
        }
      }
    } else {
      f32x4 mean[MB], rs[MB];
#pragma unroll
      for (int i = 0; i < MB; ++i) {
        const int rl = (wm * MB + mblk[i]) * 16 + 4 * g;
        const f32x4 sm = *(const f32x4*)(xs + 128 + rl);
        const f32x4 sq = *(const f32x4*)(xs + 256 + rl);
#pragma unroll
        for (int r = 0; r < 4; ++r) {
          mean[i][r] = sm[r] * INVK;
          rs[i][r] = __builtin_amdgcn_rsqf(fmaxf(sq[r] * INVK - mean[i][r] * mean[i][r], 0.f) + LN_EPS);
        }
      }
#pragma unroll
      for (int j = 0; j < NB; ++j) {
        const int cl = (wn * NB + nblk[j]) * 16 + l15;
        const float cs = xs[cl];
        const int c = c0 - 1280 + cl;
        const int head = c / 20, d = c - head * 20;
        const int chunk = g ^ ((d >> 2) & 2);
#pragma unroll
        for (int ip = 0; ip < 2; ++ip) {
          const int i0 = 2 * ip, i1 = 2 * ip + 1;
          const int tile = (row0 + (wm * MB + mblk[i0]) * 16) >> 5;
          u32x4 o;
          o[0] = pack2((acc[i0][j][0] - mean[i0][0] * cs) * rs[i0][0], (acc[i0][j][1] - mean[i0][1] * cs) * rs[i0][1]);
          o[1] = pack2((acc[i0][j][2] - mean[i0][2] * cs) * rs[i0][2], (acc[i0][j][3] - mean[i0][3] * cs) * rs[i0][3]);
          o[2] = pack2((acc[i1][j][0] - mean[i1][0] * cs) * rs[i1][0], (acc[i1][j][1] - mean[i1][1] * cs) * rs[i1][1]);
          o[3] = pack2((acc[i1][j][2] - mean[i1][2] * cs) * rs[i1][2], (acc[i1][j][3] - mean[i1][3] * cs) * rs[i1][3]);
          *(u32x4*)(O2 + (size_t)(((head * 64 + tile) * 20 + d) * 32 + chunk * 8)) = o;
        }
      }
    }
  } else {
    float mean[MB], rstd[MB], var[MB];
    if constexpr (MODE == 4 || MODE == 5 || MODE == 6) {
#pragma unroll
      for (int i = 0; i < MB; ++i) {
        float sm = stv[i][0] + stv[i][2], sq = stv[i][1] + stv[i][3];
        sm += __shfl_xor(sm, 16); sq += __shfl_xor(sq, 16);
        sm += __shfl_xor(sm, 32); sq += __shfl_xor(sq, 32);
        mean[i] = sm * (1.0f / DM);
        var[i] = fmaxf(sq * (1.0f / DM) - mean[i] * mean[i], 0.f);
        rstd[i] = __builtin_amdgcn_rsqf(var[i] + LN_EPS);
      }
    }
    if constexpr (MODE == 3 || MODE == 5) {
      float sm = 0.f, sq = 0.f;
#pragma unroll
      for (int j = 0; j < NB; ++j) {
        float r[4] = {bflo(r0v[j][0]), bfhi(r0v[j][0]), bflo(r0v[j][1]), bfhi(r0v[j][1])};
        float v[4];
#pragma unroll
        for (int e = 0; e < 4; ++e) {
          if constexpr (MODE == 3) v[e] = r[e] + acc[0][j][e];
          else v[e] = (r[e] - mean[0]) * rstd[0] + acc[0][j][e];
        }
        u32x2 o; o[0] = pack2(v[0], v[1]); o[1] = pack2(v[2], v[3]);
        *(u32x2*)(O0 + (size_t)erow * DM + c0 + 16 * j + 4 * g) = o;
        float w[4] = {bflo(o[0]), bfhi(o[0]), bflo(o[1]), bfhi(o[1])};
#pragma unroll
        for (int e = 0; e < 4; ++e) { sm += w[e]; sq += w[e] * w[e]; }
      }
      sm += __shfl_xor(sm, 16); sq += __shfl_xor(sq, 16);
      sm += __shfl_xor(sm, 32); sq += __shfl_xor(sq, 32);
      if (g == 0) *(f32x2*)(STO + (size_t)erow * 16 + ct * 2) = f32x2{sm, sq};
    } else if constexpr (MODE == 4) {
      const float r2 = __builtin_amdgcn_rsqf(var[0] * rstd[0] * rstd[0] + LN_EPS);
      const float rs = rstd[0] * r2;
#pragma unroll
      for (int j = 0; j < NB; ++j) {
        const f32x4 cs = *(const f32x4*)(xs + nblk[j] * 16 + 4 * g);
        float v[4];
#pragma unroll
        for (int e = 0; e < 4; ++e) v[e] = gelu_t((acc[0][j][e] - mean[0] * cs[e]) * rs);
        u32x2 o; o[0] = pack2(v[0], v[1]); o[1] = pack2(v[2], v[3]);
        *(u32x2*)(O0 + (size_t)(erow + 2) * DM + c0 + 16 * nblk[j] + 4 * g) = o;
      }
      if ((rt == 0 || rt == 31) && wave == 0 && lane < 40) {
        const int gr = (rt == 0 ? 0 : SEQ + 2) + (lane >= 20 ? 1 : 0);
        const int cc = c0 + 4 * (lane >= 20 ? lane - 20 : lane);
        *(u32x2*)(O0 + (size_t)gr * DM + cc) = u32x2{0u, 0u};
      }
    } else if constexpr (MODE == 6) {
#pragma unroll
      for (int j = 0; j < NB; ++j) {
        const f32x4 cs = *(const f32x4*)(xs + (wn * NB + nblk[j]) * 16 + 4 * g);
#pragma unroll
        for (int i = 0; i < MB; ++i) {
          float v[4];
#pragma unroll
          for (int e = 0; e < 4; ++e) v[e] = gelu_t((acc[i][j][e] - mean[i] * cs[e]) * rstd[i]);
          u32x2 o; o[0] = pack2(v[0], v[1]); o[1] = pack2(v[2], v[3]);
          *(u32x2*)(O0 + (size_t)(erow + 16 * i) * FF + c0 + (wn * NB + nblk[j]) * 16 + 4 * g) = o;
        }
      }
    } else if constexpr (MODE == 7) {
#pragma unroll
      for (int j = 0; j < NB; ++j) {
        float hv[4] = {bflo(r0v[j][0]), bfhi(r0v[j][0]), bflo(r0v[j][1]), bfhi(r0v[j][1])};
        float xv[4] = {bflo(r1v[j][0]), bfhi(r1v[j][0]), bflo(r1v[j][1]), bfhi(r1v[j][1])};
        float v[4];
#pragma unroll
        for (int e = 0; e < 4; ++e) v[e] = hv[e] + (xv[e] + acc[0][j][e]);
        u32x2 o; o[0] = pack2(v[0], v[1]); o[1] = pack2(v[2], v[3]);
        *(u32x2*)(O0 + (size_t)erow * DM + c0 + 16 * j + 4 * g) = o;
      }
    }
  }
  STAMP(MODE, 6);
  STAMP(MODE, 7);
}

__global__ __launch_bounds__(512) void attn_k(const u16* __restrict__ Qg, const u16* __restrict__ Kg,
                                               const u16* __restrict__ Vg, u16* __restrict__ Og) {
  __shared__ __attribute__((aligned(16))) char lds[20480 * 2 + 32];
  const int tid = threadIdx.x, lane = tid & 63;
  const int wave = __builtin_amdgcn_readfirstlane(tid >> 6);
  const int chunk = blockIdx.x & 7, head = blockIdx.x >> 3;
  const int l15 = lane & 15, g = lane >> 4;
  STAMP(2, 0); STAMP(2, 1);
  char* Kl = lds;
  char* Vl = lds + 20480;

  const char* Ksrc = (const char*)Kg + (size_t)head * (128 * 640) + (ptrdiff_t)(chunk - 1) * 10240 + lane * 16;
  const char* Vsrc = (const char*)Vg + (size_t)head * (64 * 1280) + (ptrdiff_t)(chunk - 1) * 10240 + lane * 16;
#pragma unroll
  for (int i = 0; i < 3; ++i) {
    const int n = wave + 8 * i;
    if (n < 20 && (chunk > 0 || n >= 10)) glds16(Ksrc + n * 1024, Kl + n * 1024);
  }
  const int qb0 = chunk * 16 + wave * 2;
  u32x2 qa0, qa1, qb0v, qb1v;
  {
    const u16* qp = Qg + (size_t)(head * 128 + qb0) * 320 + l15 * 4;
    qa0 = *(const u32x2*)(qp + g * 64);
    qa1 = *(const u32x2*)(qp + 256);
    qb0v = *(const u32x2*)(qp + 320 + g * 64);
    qb1v = *(const u32x2*)(qp + 320 + 256);
  }
  if (chunk == 0) {
#pragma unroll
    for (int i = 0; i < 2; ++i) {
      const int o = (tid + 512 * i) * 16;
      if (o < 10240) {
        *(u32x4*)(Kl + o) = u32x4{0u, 0u, 0u, 0u};
        *(u32x4*)(Vl + o) = u32x4{0u, 0u, 0u, 0u};
      }
    }
  }
  if (tid < 2) {
    const unsigned v = tid == 0 ? 0x3F803F80u : 0u;
    *(u32x4*)(lds + 40960 + tid * 16) = u32x4{v, v, v, v};
  }
  if (g != 0) { qa1 = u32x2{0u, 0u}; qb1v = u32x2{0u, 0u}; }
  const bf16x8 qA = __builtin_bit_cast(bf16x8, u32x4{qa0[0], qa0[1], qa1[0], qa1[1]});
  const bf16x8 qB = __builtin_bit_cast(bf16x8, u32x4{qb0v[0], qb0v[1], qb1v[0], qb1v[1]});
  asm volatile("s_waitcnt vmcnt(0) lgkmcnt(0)\n\ts_barrier" ::: "memory");

  STAMP(2, 2);
  f32x4 sA[17], sB[17];
  {
    const char* kp = Kl + wave * 1280 + l15 * 8;
    const char* kp0 = kp + g * 128;
    const f32x4 z = {0.f, 0.f, 0.f, 0.f};
#pragma unroll
    for (int i = 0; i < 18; ++i) {
      const u32x2 k0 = *(const u32x2*)(kp0 + i * 640);
      const u32x2 k1 = *(const u32x2*)(kp + 512 + i * 640);
      const bf16x8 kf = __builtin_bit_cast(bf16x8, u32x4{k0[0], k0[1], k1[0], k1[1]});
      if (i < 17) sA[i] = mfma16(kf, qA, z);
      if (i > 0) sB[i - 1] = mfma16(kf, qB, z);
    }
  }
  STAMP(2, 3);
#pragma unroll
  for (int i = 0; i < 3; ++i) {
    const int n = wave + 8 * i;
    if (n < 20 && (chunk > 0 || n >= 10)) glds16(Vsrc + n * 1024, Vl + n * 1024);
  }
  const float NEG = -__builtin_inff();
#pragma unroll
  for (int r = 0; r < 4; ++r) {
    const bool gt = (4 * g + r) > l15;
    sA[0][r] = gt ? sA[0][r] : NEG;
    sB[0][r] = gt ? sB[0][r] : NEG;
    sA[16][r] = gt ? NEG : sA[16][r];
    sB[16][r] = gt ? NEG : sB[16][r];
  }
  if (chunk == 0) {
#pragma unroll
    for (int i = 0; i < 16; ++i) {
      if (qb0 - 16 + i < 0) sA[i] = f32x4{NEG, NEG, NEG, NEG};
      if (qb0 - 15 + i < 0) sB[i] = f32x4{NEG, NEG, NEG, NEG};
    }
  }
  float mA = sA[16][0], mB = sB[16][0];
#pragma unroll
  for (int i = 0; i < 17; ++i) {
    mA = fmaxf(fmaxf(mA, sA[i][0]), sA[i][1]); mA = fmaxf(fmaxf(mA, sA[i][2]), sA[i][3]);
    mB = fmaxf(fmaxf(mB, sB[i][0]), sB[i][1]); mB = fmaxf(fmaxf(mB, sB[i][2]), sB[i][3]);
  }
  mA = fmaxf(mA, __shfl_xor(mA, 16)); mB = fmaxf(mB, __shfl_xor(mB, 16));
  mA = fmaxf(mA, __shfl_xor(mA, 32)); mB = fmaxf(mB, __shfl_xor(mB, 32));
  unsigned pA[18][2], pB[18][2];
#pragma unroll
  for (int i = 0; i < 17; ++i) {
    pA[i][0] = pack2(__builtin_amdgcn_exp2f(sA[i][0] - mA), __builtin_amdgcn_exp2f(sA[i][1] - mA));
    pA[i][1] = pack2(__builtin_amdgcn_exp2f(sA[i][2] - mA), __builtin_amdgcn_exp2f(sA[i][3] - mA));
    pB[i + 1][0] = pack2(__builtin_amdgcn_exp2f(sB[i][0] - mB), __builtin_amdgcn_exp2f(sB[i][1] - mB));
    pB[i + 1][1] = pack2(__builtin_amdgcn_exp2f(sB[i][2] - mB), __builtin_amdgcn_exp2f(sB[i][3] - mB));
  }
  pA[17][0] = pA[17][1] = 0u; pB[0][0] = pB[0][1] = 0u;
  STAMP(2, 4);
  asm volatile("s_waitcnt vmcnt(0) lgkmcnt(0)\n\ts_barrier" ::: "memory");
  STAMP(2, 5);

  f32x4 oA0 = {0.f, 0.f, 0.f, 0.f}, oA1 = oA0, oB0 = oA0, oB1 = oA0;
  {
    const char* vp0 = Vl + wave * 1280 + l15 * 64 + ((g ^ ((l15 >> 2) & 2)) << 4);
    int v1off = (l15 < 4) ? (20480 + wave * 1280 + (16 + l15) * 64 + g * 16) : (l15 == 4 ? 40960 : 40976);
    const int v1str = (l15 < 4) ? 1280 : 0;
#pragma unroll
    for (int t = 0; t < 9; ++t) {
      const bf16x8 v0 = *(const bf16x8*)(vp0 + t * 1280);
      const bf16x8 v1 = *(const bf16x8*)(lds + v1off);
      v1off += v1str;
      const bf16x8 fA = __builtin_bit_cast(bf16x8, u32x4{pA[2 * t][0], pA[2 * t][1], pA[2 * t + 1][0], pA[2 * t + 1][1]});
      const bf16x8 fB = __builtin_bit_cast(bf16x8, u32x4{pB[2 * t][0], pB[2 * t][1], pB[2 * t + 1][0], pB[2 * t + 1][1]});
      oA0 = mfma16(v0, fA, oA0); oA1 = mfma16(v1, fA, oA1);
      oB0 = mfma16(v0, fB, oB0); oB1 = mfma16(v1, fB, oB1);
    }
  }
  const float dA = __builtin_amdgcn_rcpf(__shfl(oA1[0], l15 + 16));
  const float dB = __builtin_amdgcn_rcpf(__shfl(oB1[0], l15 + 16));
  u16* op = Og + (size_t)(qb0 * 16 + l15) * DM + head * 20;
  *(u32x2*)(op + 4 * g) = u32x2{pack2(oA0[0] * dA, oA0[1] * dA), pack2(oA0[2] * dA, oA0[3] * dA)};
  *(u32x2*)(op + 16 * DM + 4 * g) = u32x2{pack2(oB0[0] * dB, oB0[1] * dB), pack2(oB0[2] * dB, oB0[3] * dB)};
  if (g == 0) {
    *(u32x2*)(op + 16) = u32x2{pack2(oA1[0] * dA, oA1[1] * dA), pack2(oA1[2] * dA, oA1[3] * dA)};
    *(u32x2*)(op + 16 * DM + 16) = u32x2{pack2(oB1[0] * dB, oB1[1] * dB), pack2(oB1[2] * dB, oB1[3] * dB)};
  }
  STAMP(2, 6);
  STAMP(2, 7);
}

extern "C" void kernel_launch(void* const* d_in, const int* in_sizes, int n_in, void* d_out, int out_size,
                              void* d_ws, size_t ws_size, hipStream_t stream) {
  const u16* x = (const u16*)d_in[0];
  const u16* wqkv = (const u16*)d_in[1];
  const u16* wo = (const u16*)d_in[2];
  const u16* w1 = (const u16*)d_in[4];
  const u16* w2 = (const u16*)d_in[5];
  const u16* w3 = (const u16*)d_in[6];
  const u16* w4 = (const u16*)d_in[7];
  u16* out = (u16*)d_out;
  char* ws = (char*)d_ws;
  constexpr size_t MB4 = 4u << 20;
  u16* Qb = (u16*)(ws + 0 * MB4);
  u16* Kb = (u16*)(ws + 1 * MB4);
  u16* Vb = (u16*)(ws + 2 * MB4);
  u16* ob = (u16*)(ws + 3 * MB4);
  u16* hb = (u16*)(ws + 4 * MB4);
  u16* hh = (u16*)(ws + 5 * MB4);
  u16* x1 = (u16*)(ws + 6 * MB4);
  u16* gb = (u16*)(ws + 7 * MB4);
  float* sth = (float*)(ws + 9 * MB4);
  float* stx = (float*)(ws + 9 * MB4 + (1u << 20));

  gemm_k<1><<<240, 512, 0, stream>>>(x, wqkv, Qb, Kb, Vb, nullptr, nullptr, nullptr, nullptr);
  attn_k<<<256, 512, 0, stream>>>(Qb, Kb, Vb, ob);
  gemm_k<3><<<256, 512, 0, stream>>>(ob, wo, hb, nullptr, nullptr, x, nullptr, nullptr, sth);
  gemm_k<4><<<256, 512, 0, stream>>>(hb, w1, hh, nullptr, nullptr, nullptr, nullptr, sth, nullptr);
  gemm_k<5><<<256, 512, 0, stream>>>(hh, w2, x1, nullptr, nullptr, hb, nullptr, sth, stx);
  gemm_k<6><<<256, 512, 0, stream>>>(x1, w3, gb, nullptr, nullptr, nullptr, nullptr, stx, nullptr);
  gemm_k<7><<<256, 512, 0, stream>>>(gb, w4, out, nullptr, nullptr, hb, x1, nullptr, nullptr);
}
```

```cpp
#include <hip/hip_runtime.h>
#include <stdint.h>

typedef unsigned short u16;
typedef short s16x4 __attribute__((ext_vector_type(4)));
typedef short s16x8 __attribute__((ext_vector_type(8)));
typedef __bf16 bf16x8 __attribute__((ext_vector_type(8)));
typedef __bf16 bf16x2 __attribute__((ext_vector_type(2)));
typedef float f32x2 __attribute__((ext_vector_type(2)));
typedef float f32x4 __attribute__((ext_vector_type(4)));
typedef unsigned u32x2 __attribute__((ext_vector_type(2)));
typedef unsigned u32x4 __attribute__((ext_vector_type(4)));
typedef __attribute__((address_space(3))) s16x4* lds_s16x4_p;

#define DEVI __device__ __forceinline__
#define STAMP_DECL
#define STAMP(kid, i)
#define STAMP_FLUSH(kid)

constexpr int SEQ = 2048, DM = 640, FF = 1536, NH = 32;
constexpr float LN_EPS = 1e-5f;
constexpr int CSN = DM + FF;

template <int I> struct IC { static constexpr int v = I; };
template <int B, int E, class F> DEVI void sfor(F&& f) {
  if constexpr (B < E) { f(IC<B>{}); sfor<B + 1, E>(f); }
}
template <int N> DEVI void wait_vm() { asm volatile("s_waitcnt vmcnt(%0)" ::"n"(N) : "memory"); }
DEVI void bar() { asm volatile("s_barrier" ::: "memory"); }
DEVI void lgkm_bar() { asm volatile("s_waitcnt lgkmcnt(0)\n\ts_barrier" ::: "memory"); }

DEVI unsigned pack2(float a, float b) {
  f32x2 v = {a, b};
  bf16x2 r = __builtin_convertvector(v, bf16x2);
  return __builtin_bit_cast(unsigned, r);
}
DEVI float bflo(unsigned u) { return __builtin_bit_cast(float, u << 16); }
DEVI float bfhi32(unsigned u) { return __builtin_bit_cast(float, u); }
DEVI float bfhi(unsigned u) { return __builtin_bit_cast(float, u & 0xffff0000u); }
DEVI f32x4 mfma16(bf16x8 a, bf16x8 b, f32x4 c) { return __builtin_amdgcn_mfma_f32_16x16x32_bf16(a, b, c, 0, 0, 0); }
DEVI float gelu_t(float x) {
  float u = x * (0.7978845608028654f + 0.035677408136300125f * x * x);
  float e = __builtin_amdgcn_exp2f(-2.885390081777927f * u);
  return x * __builtin_amdgcn_rcpf(1.0f + e);
}
DEVI void glds16(const char* g, char* l) {
  __builtin_amdgcn_global_load_lds((const unsigned*)g, (unsigned*)l, 16, 0, 0);
}
DEVI bf16x8 tr_frag(const char* p0, const char* p1) {
  s16x4 a = __builtin_amdgcn_ds_read_tr16_b64_v4i16((lds_s16x4_p)p0);
  s16x4 b = __builtin_amdgcn_ds_read_tr16_b64_v4i16((lds_s16x4_p)p1);
  s16x8 f = __builtin_shufflevector(a, b, 0, 1, 2, 3, 4, 5, 6, 7);
  return __builtin_bit_cast(bf16x8, f);
}

template <int MODE> struct Cfg;
template <> struct Cfg<1> { static constexpr int BM = 128, BNL = 128, K = 640, LDB = 1920, WGM = 2, WGN = 2, MB = 4, NB = 4, NS = 5, NCS = 2; };
template <> struct Cfg<3> { static constexpr int BM = 64, BNL = 96, K = 640, LDB = 640, WGM = 4, WGN = 1, MB = 1, NB = 5, NS = 8, NCS = 0; };
template <> struct Cfg<4> { static constexpr int BM = 64, BNL = 96, K = 640, LDB = 640, WGM = 4, WGN = 1, MB = 1, NB = 5, NS = 8, NCS = 2; };
template <> struct Cfg<5> { static constexpr int BM = 64, BNL = 96, K = 640, LDB = 640, WGM = 4, WGN = 1, MB = 1, NB = 5, NS = 8, NCS = 0; };
template <> struct Cfg<6> { static constexpr int BM = 128, BNL = 96, K = 640, LDB = 1536, WGM = 2, WGN = 2, MB = 4, NB = 3, NS = 5, NCS = 2; };
template <> struct Cfg<7> { static constexpr int BM = 64, BNL = 96, K = 1536, LDB = 640, WGM = 4, WGN = 1, MB = 1, NB = 5, NS = 8, NCS = 0; };

template <int MODE>
__global__ __launch_bounds__(512) void gemm_k(const u16* __restrict__ A, const u16* __restrict__ B,
                                               u16* __restrict__ O0, u16* __restrict__ O1, u16* __restrict__ O2,
                                               const u16* __restrict__ R0, const u16* __restrict__ R1,
                                               const float* __restrict__ STI, float* __restrict__ STO) {
  using C = Cfg<MODE>;
  constexpr int BM = C::BM, BNL = C::BNL, K = C::K, LDA = C::K, LDB = C::LDB;
  constexpr int WGM = C::WGM, WGN = C::WGN, MB = C::MB, NB = C::NB, NS = C::NS, NCS = C::NCS;
  constexpr int NST = K / 64;
  constexpr int ABYTES = BM * 128, PITCH = BNL * 2, BBYTES = 64 * PITCH, SLOT = ABYTES + BBYTES;
  constexpr int AI = BM / 8, BI = BNL / 8, CH = BNL / 8, PER = (AI + BI) / 4;
  constexpr bool XCHG = (NCS > 0);
  constexpr int XOFF = ((NST - 2) % NS) * SLOT;
  __shared__ __attribute__((aligned(16))) char lds[NS * SLOT];

  const int tid = threadIdx.x, lane = tid & 63;
  const int wave = __builtin_amdgcn_readfirstlane(tid >> 6);
  const int bid = blockIdx.x, xcd = bid & 7, idx = bid >> 3;
  STAMP_DECL; STAMP(MODE, 0); STAMP(MODE, 1);

  int rt, ct;
  if constexpr (MODE == 1) { rt = xcd * 2 + idx / 15; ct = idx % 15; }
  else if constexpr (MODE == 6) { rt = xcd * 2 + (idx >> 4); ct = idx & 15; }
  else { rt = xcd * 4 + (idx >> 3); ct = idx & 7; }
  const int row0 = rt * BM;
  int c0, cstart, cboff;
  if constexpr (MODE == 1) { c0 = ct * 128; cstart = c0; cboff = 0; }
  else if constexpr (MODE == 6) { c0 = ct * 96; cstart = c0; cboff = 0; }
  else { c0 = ct * 80; cstart = c0 & ~31; cboff = (c0 - cstart) >> 4; }

  if (wave >= 4) {
    const int lw = wave - 4;
    const char* Ab = (const char*)A + (size_t)row0 * (LDA * 2);
    const char* Bb = (const char*)B + (size_t)cstart * 2;
    const unsigned acx = (unsigned)((lane & 7) ^ (lane >> 3));
    const unsigned aoff = (unsigned)(((lane >> 3) + 8 * lw) * (LDA * 2)) + acx * 16;
    unsigned boff[BI / 4];
#pragma unroll
    for (int i = 0; i < BI / 4; ++i) {
      int li = (lw + 4 * i) * 64 + lane;
      int rho = li / CH, cp = li % CH;
      int k = (rho & ~12) | ((rho & 4) << 1) | ((rho & 8) >> 1);
      int f = (BNL == 128) ? (rho & 7) : ((rho >> 2) & 1);
      int c = (((cp >> 1) ^ f) << 1) | (cp & 1);
      boff[i] = (unsigned)(k * (LDB * 2) + c * 16);
    }
    auto issue = [&](auto SC) {
      constexpr int s = decltype(SC)::v;
      char* sl = lds + (s % NS) * SLOT + lw * 1024;
      unsigned ao = aoff;
      if constexpr (MODE == 5) {
        int col = 64 * s + 8 * (int)acx;
        int sh = col < 160 ? 1 : (col < 320 ? 3 : (col < 480 ? 0 : 4));
        ao += (unsigned)(sh * (LDA * 2));
      }
#pragma unroll
      for (int i = 0; i < AI / 4; ++i) glds16(Ab + (s * 128 + i * 32 * (LDA * 2)) + ao, sl + i * 4096);
#pragma unroll
      for (int i = 0; i < BI / 4; ++i) glds16(Bb + (size_t)s * (64 * LDB * 2) + boff[i], sl + ABYTES + i * 4096);
    };
    STAMP(MODE, 2);
    sfor<0, (NS < NST ? NS : NST)>([&](auto SC) { issue(SC); });
    STAMP(MODE, 3);
    sfor<0, NST>([&](auto SC) {
      constexpr int s = decltype(SC)::v;
      constexpr int iss0 = NS + (s > 0 ? s - 1 : 0);
      constexpr int issued = iss0 < NST ? iss0 : NST;
      wait_vm<(issued - (s + 1)) * PER>();
      bar();
      if constexpr (s == 0) STAMP(MODE, 4);
      if constexpr (s == NST - 1) STAMP(MODE, 5);
      if constexpr (s >= 1 && (s - 1 + NS) < NST) issue(IC<s - 1 + NS>{});
    });
    if constexpr (XCHG) bar();
    STAMP_FLUSH(MODE);
    return;
  }

  const int wm = (WGN == 1) ? wave : (wave >> 1), wn = (WGN == 1) ? 0 : (wave & 1);
  const int l15 = lane & 15, g = lane >> 4, q = l15 >> 2, p = lane & 3;
  constexpr int WROWS = BM / WGM;

  const int mrot = (MODE == 1) ? 2 * wn : 0;
  const int nrot = (MODE == 1) ? 2 * wm : (MODE == 6) ? 2 * wm : (MODE == 4) ? (2 * wave) % 5 : 0;
  int mblk[MB], nblk[NB];
#pragma unroll
  for (int i = 0; i < MB; ++i) mblk[i] = (i + mrot) % MB;
#pragma unroll
  for (int j = 0; j < NB; ++j) { int v = j + nrot; nblk[j] = v >= NB ? v - NB : v; }

  int aad[2][MB > 2 ? 2 : 1];
  {
    int base = (wm * WROWS + l15) * 128 + ((g ^ (lane & 7)) << 4);
#pragma unroll
    for (int hh = 0; hh < (MB > 2 ? 2 : 1); ++hh) {
      int b = base + mblk[hh * 2] * 2048;
      aad[0][hh] = b; aad[1][hh] = b ^ 64;
    }
  }
  int bad[NB];
  {
    const int g0 = g & 1, g1 = g >> 1;
    const int rho = q + 4 * g0 + 16 * g1;
    const int f = (BNL == 128) ? (rho & 7) : g0;
#pragma unroll
    for (int j = 0; j < NB; ++j) {
      int cb = cboff + wn * NB + nblk[j];
      bad[j] = ABYTES + rho * PITCH + ((cb ^ f) << 5) + p * 8;
    }
  }

  u32x2 r0v[NB], r1v[NB];
  f32x4 stv[MB];
  const int erow = row0 + wm * WROWS + l15;
  if constexpr (MODE == 3 || MODE == 5 || MODE == 7) {
#pragma unroll
    for (int j = 0; j < NB; ++j) r0v[j] = *(const u32x2*)(R0 + (size_t)erow * DM + c0 + 16 * j + 4 * g);
  }
  if constexpr (MODE == 7) {
#pragma unroll
    for (int j = 0; j < NB; ++j) r1v[j] = *(const u32x2*)(R1 + (size_t)erow * DM + c0 + 16 * j + 4 * g);
  }
  if constexpr (MODE == 4 || MODE == 5 || MODE == 6) {
#pragma unroll
    for (int i = 0; i < MB; ++i) stv[i] = *(const f32x4*)(STI + (size_t)(erow + 16 * i) * 16 + 4 * g);
  }

  f32x4 acc[MB][NB];
#pragma unroll
  for (int i = 0; i < MB; ++i)
#pragma unroll
    for (int j = 0; j < NB; ++j) acc[i][j] = f32x4{0.f, 0.f, 0.f, 0.f};
  f32x4 csa[XCHG ? NCS : 1], gra[2], rsa[2];
#pragma unroll
  for (int i = 0; i < (XCHG ? NCS : 1); ++i) csa[i] = f32x4{0.f, 0.f, 0.f, 0.f};
  gra[0] = gra[1] = rsa[0] = rsa[1] = f32x4{0.f, 0.f, 0.f, 0.f};
  const s16x8 ones_s = {0x3F80, 0x3F80, 0x3F80, 0x3F80, 0x3F80, 0x3F80, 0x3F80, 0x3F80};
  const bf16x8 ones = __builtin_bit_cast(bf16x8, ones_s);

  bf16x8 fa[2][MB], fb[2][NB];
  auto readf = [&](auto SC, auto HC) {
    constexpr int s = decltype(SC)::v, h = decltype(HC)::v;
    const char* sl = lds + (s % NS) * SLOT;
#pragma unroll
    for (int i = 0; i < MB; ++i) fa[h][i] = *(const bf16x8*)(sl + aad[h][i >> 1] + (i & 1) * 2048);
#pragma unroll
    for (int j = 0; j < NB; ++j) fb[h][j] = tr_frag(sl + bad[j] + h * 32 * PITCH, sl + bad[j] + (h * 32 + 8) * PITCH);
  };
  auto mainloop = [&](auto SW) {
    constexpr bool swapped = decltype(SW)::v != 0;
    auto mm = [&](auto HC) {
      constexpr int h = decltype(HC)::v;
#pragma unroll
      for (int i = 0; i < MB; ++i)
#pragma unroll
        for (int j = 0; j < NB; ++j)
          acc[i][j] = swapped ? mfma16(fb[h][j], fa[h][i], acc[i][j]) : mfma16(fa[h][i], fb[h][j], acc[i][j]);
      if constexpr (XCHG) {
#pragma unroll
        for (int j = 0; j < NCS; ++j) csa[j] = mfma16(ones, fb[h][j], csa[j]);
      }
      if constexpr (MODE == 1) {
#pragma unroll
        for (int i = 0; i < 2; ++i) {
          gra[i] = mfma16(fa[h][i], fa[h][i], gra[i]);
          rsa[i] = mfma16(ones, fa[h][i], rsa[i]);
        }
      }
    };
    STAMP(MODE, 2);
    bar();
    STAMP(MODE, 3);
    readf(IC<0>{}, IC<0>{});
    sfor<0, NST>([&](auto SC) {
      constexpr int s = decltype(SC)::v;
      readf(SC, IC<1>{});
      mm(IC<0>{});
      if constexpr (s + 1 < NST) {
        lgkm_bar();
        readf(IC<s + 1>{}, IC<0>{});
      }
      mm(IC<1>{});
    });
  };
  bool isV = false;
  if constexpr (MODE == 1) {
    isV = ct >= 10;
    if (isV) mainloop(IC<0>{}); else mainloop(IC<1>{});
  } else {
    mainloop(IC<1>{});
  }

  STAMP(MODE, 4);
  float* xs = (float*)(lds + XOFF);
  if constexpr (XCHG) {
    if (g == 0) {
#pragma unroll
      for (int j = 0; j < NCS; ++j) xs[(wn * NB + nblk[j]) * 16 + l15] = csa[j][0];
    }
    if constexpr (MODE == 1) {
#pragma unroll
      for (int i = 0; i < 2; ++i) {
        int r = (wm * MB + mblk[i]) * 16 + l15;
        if (g == 0) xs[128 + r] = rsa[i][0];
        if (g == q) {
          float v = p == 0 ? gra[i][0] : p == 1 ? gra[i][1] : p == 2 ? gra[i][2] : gra[i][3];
          xs[256 + r] = v;
        }
      }
    }
    lgkm_bar();
  }

  STAMP(MODE, 5);
  if constexpr (MODE == 1) {
    constexpr float INVK = 1.0f / K;
    if (!isV) {
      const bool isQ = ct < 5;
      u16* dst = isQ ? O0 : O1;
      const float sc = isQ ? 0.22360679774997896f * 1.4426950408889634f : 1.0f;
      const int cbase = c0 - (isQ ? 0 : 640) + wn * 64 + 4 * g;
#pragma unroll
      for (int i = 0; i < MB; ++i) {
        const int rl = (wm * MB + mblk[i]) * 16 + l15;
        const float mean = xs[128 + rl] * INVK;
        const float var = fmaxf(xs[256 + rl] * INVK - mean * mean, 0.f);
        const float rs = __builtin_amdgcn_rsqf(var + LN_EPS) * sc;
        const int rb = (row0 + rl) >> 4;
#pragma unroll
        for (int j = 0; j < NB; ++j) {
          const f32x4 cs = *(const f32x4*)(xs + (wn * NB + nblk[j]) * 16 + 4 * g);
          const int c = cbase + 16 * nblk[j];
          const int head = c / 20, quad = (c - head * 20) >> 2;
          u32x2 o;
          o[0] = pack2((acc[i][j][0] - mean * cs[0]) * rs, (acc[i][j][1] - mean * cs[1]) * rs);
          o[1] = pack2((acc[i][j][2] - mean * cs[2]) * rs, (acc[i][j][3] - mean * cs[3]) * rs);
          *(u32x2*)(dst + (size_t)(((head * 128 + rb) * 5 + quad) * 64 + l15 * 4)) = o;
        }
      }
    } else {
      f32x4 mean[MB], rs[MB];
#pragma unroll
      for (int i = 0; i < MB; ++i) {
        const int rl = (wm * MB + mblk[i]) * 16 + 4 * g;
        const f32x4 sm = *(const f32x4*)(xs + 128 + rl);
        const f32x4 sq = *(const f32x4*)(xs + 256 + rl);
#pragma unroll
        for (int r = 0; r < 4; ++r) {
          mean[i][r] = sm[r] * INVK;
          rs[i][r] = __builtin_amdgcn_rsqf(fmaxf(sq[r] * INVK - mean[i][r] * mean[i][r], 0.f) + LN_EPS);
        }
      }
#pragma unroll
      for (int j = 0; j < NB; ++j) {
        const int cl = (wn * NB + nblk[j]) * 16 + l15;
        const float cs = xs[cl];
        const int c = c0 - 1280 + cl;
        const int head = c / 20, d = c - head * 20;
        const int chunk = g ^ ((d >> 2) & 2);
#pragma unroll
        for (int ip = 0; ip < 2; ++ip) {
          const int i0 = 2 * ip, i1 = 2 * ip + 1;
          const int tile = (row0 + (wm * MB + mblk[i0]) * 16) >> 5;
          u32x4 o;
          o[0] = pack2((acc[i0][j][0] - mean[i0][0] * cs) * rs[i0][0], (acc[i0][j][1] - mean[i0][1] * cs) * rs[i0][1]);
          o[1] = pack2((acc[i0][j][2] - mean[i0][2] * cs) * rs[i0][2], (acc[i0][j][3] - mean[i0][3] * cs) * rs[i0][3]);
          o[2] = pack2((acc[i1][j][0] - mean[i1][0] * cs) * rs[i1][0], (acc[i1][j][1] - mean[i1][1] * cs) * rs[i1][1]);
          o[3] = pack2((acc[i1][j][2] - mean[i1][2] * cs) * rs[i1][2], (acc[i1][j][3] - mean[i1][3] * cs) * rs[i1][3]);
          *(u32x4*)(O2 + (size_t)(((head * 64 + tile) * 20 + d) * 32 + chunk * 8)) = o;
        }
      }
    }
  } else {
    float mean[MB], rstd[MB], var[MB];
    if constexpr (MODE == 4 || MODE == 5 || MODE == 6) {
#pragma unroll
      for (int i = 0; i < MB; ++i) {
        float sm = stv[i][0] + stv[i][2], sq = stv[i][1] + stv[i][3];
        sm += __shfl_xor(sm, 16); sq += __shfl_xor(sq, 16);
        sm += __shfl_xor(sm, 32); sq += __shfl_xor(sq, 32);
        mean[i] = sm * (1.0f / DM);
        var[i] = fmaxf(sq * (1.0f / DM) - mean[i] * mean[i], 0.f);
        rstd[i] = __builtin_amdgcn_rsqf(var[i] + LN_EPS);
      }
    }
    if constexpr (MODE == 3 || MODE == 5) {
      float sm = 0.f, sq = 0.f;
#pragma unroll
      for (int j = 0; j < NB; ++j) {
        float r[4] = {bflo(r0v[j][0]), bfhi(r0v[j][0]), bflo(r0v[j][1]), bfhi(r0v[j][1])};
        float v[4];
#pragma unroll
        for (int e = 0; e < 4; ++e) {
          if constexpr (MODE == 3) v[e] = r[e] + acc[0][j][e];
          else v[e] = (r[e] - mean[0]) * rstd[0] + acc[0][j][e];
        }
        u32x2 o; o[0] = pack2(v[0], v[1]); o[1] = pack2(v[2], v[3]);
        *(u32x2*)(O0 + (size_t)erow * DM + c0 + 16 * j + 4 * g) = o;
        float w[4] = {bflo(o[0]), bfhi(o[0]), bflo(o[1]), bfhi(o[1])};
#pragma unroll
        for (int e = 0; e < 4; ++e) { sm += w[e]; sq += w[e] * w[e]; }
      }
      sm += __shfl_xor(sm, 16); sq += __shfl_xor(sq, 16);
      sm += __shfl_xor(sm, 32); sq += __shfl_xor(sq, 32);
      if (g == 0) *(f32x2*)(STO + (size_t)erow * 16 + ct * 2) = f32x2{sm, sq};
    } else if constexpr (MODE == 4) {
      const float r2 = __builtin_amdgcn_rsqf(var[0] * rstd[0] * rstd[0] + LN_EPS);
      const float rs = rstd[0] * r2;
#pragma unroll
      for (int j = 0; j < NB; ++j) {
        const f32x4 cs = *(const f32x4*)(xs + nblk[j] * 16 + 4 * g);
        float v[4];
#pragma unroll
        for (int e = 0; e < 4; ++e) v[e] = gelu_t((acc[0][j][e] - mean[0] * cs[e]) * rs);
        u32x2 o; o[0] = pack2(v[0], v[1]); o[1] = pack2(v[2], v[3]);
        *(u32x2*)(O0 + (size_t)(erow + 2) * DM + c0 + 16 * nblk[j] + 4 * g) = o;
      }
      if ((rt == 0 || rt == 31) && wave == 0 && lane < 40) {
        const int gr = (rt == 0 ? 0 : SEQ + 2) + (lane >= 20 ? 1 : 0);
        const int cc = c0 + 4 * (lane >= 20 ? lane - 20 : lane);
        *(u32x2*)(O0 + (size_t)gr * DM + cc) = u32x2{0u, 0u};
      }
    } else if constexpr (MODE == 6) {
#pragma unroll
      for (int j = 0; j < NB; ++j) {
        const f32x4 cs = *(const f32x4*)(xs + (wn * NB + nblk[j]) * 16 + 4 * g);
#pragma unroll
        for (int i = 0; i < MB; ++i) {
          float v[4];
#pragma unroll
          for (int e = 0; e < 4; ++e) v[e] = gelu_t((acc[i][j][e] - mean[i] * cs[e]) * rstd[i]);
          u32x2 o; o[0] = pack2(v[0], v[1]); o[1] = pack2(v[2], v[3]);
          *(u32x2*)(O0 + (size_t)(erow + 16 * i) * FF + c0 + (wn * NB + nblk[j]) * 16 + 4 * g) = o;
        }
      }
    } else if constexpr (MODE == 7) {
#pragma unroll
      for (int j = 0; j < NB; ++j) {
        float hv[4] = {bflo(r0v[j][0]), bfhi(r0v[j][0]), bflo(r0v[j][1]), bfhi(r0v[j][1])};
        float xv[4] = {bflo(r1v[j][0]), bfhi(r1v[j][0]), bflo(r1v[j][1]), bfhi(r1v[j][1])};
        float v[4];
#pragma unroll
        for (int e = 0; e < 4; ++e) v[e] = hv[e] + (xv[e] + acc[0][j][e]);
        u32x2 o; o[0] = pack2(v[0], v[1]); o[1] = pack2(v[2], v[3]);
        *(u32x2*)(O0 + (size_t)erow * DM + c0 + 16 * j + 4 * g) = o;
      }
    }
  }
  STAMP(MODE, 6);
  STAMP(MODE, 7);
  STAMP_FLUSH(MODE);
}

DEVI void repack_unit(const u16* __restrict__ W, u16* __restrict__ P, float* __restrict__ CP, int N, int KS, int NB, int ks, int cq, int lane) {
  const int g = lane >> 4, q = lane & 15;
  const u16* src = W + (size_t)(32 * ks + 8 * g) * N + 64 * cq + 4 * q;
  u32x2 v[8];
#pragma unroll
  for (int e = 0; e < 8; ++e) v[e] = *(const u32x2*)(src + (size_t)e * N);
  const int cb = 4 * cq + (q >> 2);
  const int ct = cb / NB, j = cb - ct * NB;
  u16* dst = P + ((size_t)((ct * KS + ks) * NB + j) * 64 + g * 16 + 4 * (q & 3)) * 8;
#pragma unroll
  for (int c = 0; c < 4; ++c) {
    u32x4 o;
#pragma unroll
    for (int e2 = 0; e2 < 4; ++e2) {
      const unsigned lo = v[2 * e2][c >> 1], hi = v[2 * e2 + 1][c >> 1];
      o[e2] = (c & 1) ? __builtin_amdgcn_perm(hi, lo, 0x07060302u) : __builtin_amdgcn_perm(hi, lo, 0x05040100u);
    }
    *(u32x4*)(dst + c * 8) = o;
  }
  if (CP) {
    f32x4 sm = {0.f, 0.f, 0.f, 0.f};
#pragma unroll
    for (int e = 0; e < 8; ++e) { sm[0] += bflo(v[e][0]); sm[1] += bfhi(v[e][0]); sm[2] += bflo(v[e][1]); sm[3] += bfhi(v[e][1]); }
#pragma unroll
    for (int c = 0; c < 4; ++c) { sm[c] += __shfl_xor(sm[c], 16); sm[c] += __shfl_xor(sm[c], 32); }
    if (g == 0) *(f32x4*)(CP + (size_t)ks * CSN + 64 * cq + 4 * q) = sm;
  }
}
constexpr int NUNITS = 1560 + 320;
DEVI void repack_any(int u, int lane, const u16* __restrict__ x, const u16* __restrict__ wo, const u16* __restrict__ w1, const u16* __restrict__ w2,
                     const u16* __restrict__ w3, const u16* __restrict__ w4, u16* __restrict__ P, u16* __restrict__ XP, float* __restrict__ CP) {
  if (u < 600) {
    const int wi = u / 200, r = u - wi * 200;
    const u16* W = wi == 0 ? wo : (wi == 1 ? w1 : w2);
    repack_unit(W, P + (size_t)wi * (DM * DM), wi == 1 ? CP : nullptr, DM, 20, 5, r / 10, r % 10, lane);
  } else if (u < 1080) {
    const int r = u - 600;
    repack_unit(w3, P + (size_t)3 * (DM * DM), CP + DM, FF, 20, 6, r / 24, r % 24, lane);
  } else if (u < 1560) {
    const int r = u - 1080;
    repack_unit(w4, P + (size_t)3 * (DM * DM) + (size_t)DM * FF, nullptr, DM, 48, 5, r / 10, r % 10, lane);
  } else {
    const int f0 = (u - 1560) * 8;
    u32x4 v[8];
#pragma unroll
    for (int i = 0; i < 8; ++i) {
      const int f = f0 + i, rb = f / 20, ks = f - rb * 20;
      v[i] = *(const u32x4*)(x + (size_t)(rb * 16 + (lane & 15)) * DM + ks * 32 + (lane >> 4) * 8);
    }
#pragma unroll
    for (int i = 0; i < 8; ++i) *(u32x4*)(XP + (size_t)(f0 + i) * 512 + lane * 8) = v[i];
  }
}
__global__ __launch_bounds__(512) void repack_k(const u16* __restrict__ x, const u16* __restrict__ wo, const u16* __restrict__ w1, const u16* __restrict__ w2,
                                                 const u16* __restrict__ w3, const u16* __restrict__ w4, u16* __restrict__ P, u16* __restrict__ XP, float* __restrict__ CP) {
  const int u = blockIdx.x * 8 + (threadIdx.x >> 6);
  if (u < NUNITS) repack_any(u, threadIdx.x & 63, x, wo, w1, w2, w3, w4, P, XP, CP);
}

#define LAHEAD 6
template <int MODE> struct C2 { static constexpr int MB = 1, NB = 5, K = 640, NS = 8; };
template <> struct C2<6> { static constexpr int MB = 2, NB = 6, K = 640, NS = 5; };
template <> struct C2<7> { static constexpr int MB = 1, NB = 5, K = 1536, NS = 8; };

template <int NB> DEVI void glds_chunk(unsigned voff, const char* sbase, unsigned dst) {
  unsigned keep;
  if constexpr (NB == 5)
    asm volatile("s_mov_b32 %0, m0\n\ts_mov_b32 m0, %3\n\ts_nop 0\n\t"
                 "global_load_lds_dwordx4 %1, %2\n\tglobal_load_lds_dwordx4 %1, %2 offset:1024\n\t"
                 "s_mov_b32 exec_hi, 0\n\tglobal_load_lds_dwordx4 %1, %2 offset:2048\n\ts_mov_b32 exec_hi, -1\n\t"
                 "s_mov_b32 m0, %0"
                 : "=&s"(keep) : "v"(voff), "s"(sbase), "s"(dst) : "memory");
  else
    asm volatile("s_mov_b32 %0, m0\n\ts_mov_b32 m0, %3\n\ts_nop 0\n\t"
                 "global_load_lds_dwordx4 %1, %2\n\tglobal_load_lds_dwordx4 %1, %2 offset:1024\n\t"
                 "global_load_lds_dwordx4 %1, %2 offset:2048\n\t"
                 "s_mov_b32 m0, %0"
                 : "=&s"(keep) : "v"(voff), "s"(sbase), "s"(dst) : "memory");
}
template <int OFF> DEVI void gld16(bf16x8& d, unsigned voff, const char* sbase) {
  asm volatile("global_load_dwordx4 %0, %1, %2 offset:%3" : "=v"(d) : "v"(voff), "s"(sbase), "i"(OFF) : "memory");
}
template <int OFF> DEVI void gld8(u32x2& d, unsigned voff, const char* sbase) {
  asm volatile("global_load_dwordx2 %0, %1, %2 offset:%3" : "=v"(d) : "v"(voff), "s"(sbase), "i"(OFF) : "memory");
}
template <int OFF, class T> DEVI void dsr16(T& d, unsigned addr) {
  asm volatile("ds_read_b128 %0, %1 offset:%2" : "=v"(d) : "v"(addr), "i"(OFF) : "memory");
}
template <class T> DEVI void tie(T& x) { asm volatile("" : "+v"(x)); }
template <int N> DEVI void wait_lgkm() { asm volatile("s_waitcnt lgkmcnt(%0)" ::"n"(N) : "memory"); }

DEVI float dot2(unsigned a, unsigned b, float c) {
  return __builtin_amdgcn_fdot2_f32_bf16(__builtin_bit_cast(bf16x2, a), __builtin_bit_cast(bf16x2, b), c, false);
}

DEVI void glds1(unsigned voff, const char* sbase, unsigned dst) {
  unsigned keep;
  asm volatile("s_mov_b32 %0, m0\n\ts_mov_b32 m0, %3\n\ts_nop 0\n\tglobal_load_lds_dwordx4 %1, %2\n\ts_mov_b32 m0, %0"
               : "=&s"(keep) : "v"(voff), "s"(sbase), "s"(dst) : "memory");
}

template <int MODE>
__global__ __launch_bounds__(768, 3) void gemm2_k(const u16* __restrict__ A, const u16* __restrict__ Bp, u16* __restrict__ O,
                                                  const u16* __restrict__ R0, const u16* __restrict__ R1,
                                                  const float* __restrict__ STI, float* __restrict__ STO,
                                                  float* __restrict__ ST2, float* __restrict__ CS) {
  using C = C2<MODE>;
  constexpr int MB = C::MB, NB = C::NB, K = C::K, NS = C::NS;
  constexpr int LA = (LAHEAD < NS - 2) ? LAHEAD : NS - 2;
  constexpr int BM = 64 * MB, BN = 16 * NB, KH = K / 2, NST = KH / 32, KS = K / 32;
  constexpr int AB = 8 * MB * 1024, HB = NB * 1024, SLOT = AB + 2 * HB;
  constexpr int LDA = K, LDO = (MODE == 6) ? FF : DM;
  constexpr int PER = 2 * MB + 3;
  constexpr bool LN = (MODE == 4 || MODE == 6);
  constexpr int XS = 8 * 3 * MB * 1024;
  constexpr int L1 = (NST - 1) % NS, L2 = (NST - 2) % NS;
  constexpr int LO = L1 < L2 ? L1 : L2, HI = L1 < L2 ? L2 : L1;
  constexpr int XOFF = (LO * SLOT >= XS) ? 0 : ((HI - LO - 1) * SLOT >= XS) ? (LO + 1) * SLOT : (HI + 1) * SLOT;
  static_assert(XOFF + XS <= NS * SLOT, "exchange region");
  __shared__ __attribute__((aligned(16))) char lds[NS * SLOT];

  const int tid = threadIdx.x, lane = tid & 63;
  const int wave = __builtin_amdgcn_readfirstlane(tid >> 6);
  const int bid = blockIdx.x, xcd = bid & 7, idx = bid >> 3;
  STAMP_DECL; STAMP(MODE, 0); STAMP(MODE, 1);
  int rt, ct;
  if constexpr (MODE == 6) { rt = xcd * 2 + (idx >> 4); ct = idx & 15; }
  else { rt = xcd * 4 + (idx >> 3); ct = idx & 7; }
  const int row0 = rt * BM, c0 = ct * BN;
  const unsigned lds0 = (unsigned)(size_t)(__attribute__((address_space(3))) char*)lds;
  const unsigned voff = lane * 16;

  if (wave >= 8) {
    const int lw = wave - 8, bh = lw >> 1, bp = lw & 1;
    const char* Bsrc = (const char*)Bp + (size_t)((ct * KS + bh * (KS / 2)) * NB) * 1024 + bp * (NB * 512);
    const unsigned Bdst = lds0 + AB + bh * HB + bp * (NB * 512);
    const char* Asrc = (const char*)(A + (size_t)((row0 >> 4) + lw * MB) * (LDA * 16));
    const unsigned Adst = lds0 + lw * (MB * 2048);
    auto issue = [&](auto SC) {
      constexpr int s = decltype(SC)::v;
#pragma unroll
      for (int i = 0; i < MB; ++i) {
        glds1(voff, Asrc + i * (LDA * 32) + s * 1024, Adst + (s % NS) * SLOT + i * 2048);
        glds1(voff, Asrc + i * (LDA * 32) + KH * 32 + s * 1024, Adst + (s % NS) * SLOT + i * 2048 + 1024);
      }
      glds_chunk<NB>(voff, Bsrc + s * HB, Bdst + (s % NS) * SLOT);
    };
    sfor<0, LA>([&](auto SC) { issue(SC); });
    sfor<0, NST>([&](auto SC) {
      constexpr int s = decltype(SC)::v;
      constexpr int issued = (s + LA < NST) ? s + LA : NST;
      wait_vm<(issued - (s + 1)) * PER>();
      bar();
      if constexpr (s + LA < NST) issue(IC<s + LA>{});
    });
    return;
  }

  const int hf = wave >> 2, rg = wave & 3;
  const int l15 = lane & 15, g = lane >> 4;
  const int erow = row0 + rg * (16 * MB) + l15;
  const int rb0 = (row0 >> 4) + rg * MB;
  const int lpart = (g >> 1) * 128 + l15 * 8 + 4 * (g & 1);

  const int jb = 3 * hf;
  u32x2 r0v[3], r1v[3];
  f32x2 stv;
  f32x4 csv[3];
  float mean[MB], rs[MB];
  if constexpr (MODE == 3 || MODE == 5 || MODE == 7) {
#pragma unroll
    for (int j = 0; j < 3; ++j) {
      const int jj = (jb + j < NB) ? jb + j : NB - 1;
      r0v[j] = *(const u32x2*)(R0 + (size_t)rb0 * (DM * 16) + (ct * NB + jj) * 256 + lpart);
      if constexpr (MODE == 7) r1v[j] = *(const u32x2*)(R1 + (size_t)rb0 * (DM * 16) + (ct * NB + jj) * 256 + lpart);
    }
  }
  if constexpr (MODE == 5) stv = *(const f32x2*)(ST2 + (size_t)erow * 2);
  if constexpr (MODE == 3) {
    const int c = bid * 512 + tid;
    if (c < CSN) {
      float sm = 0.f;
#pragma unroll
      for (int k = 0; k < 20; ++k) sm += STI[k * CSN + c];
      CS[c] = sm;
    }
  }
  if constexpr (LN) {
#pragma unroll
    for (int j = 0; j < 3; ++j) {
      const int jj = (jb + j < NB) ? jb + j : NB - 1;
      csv[j] = *(const f32x4*)(CS + c0 + 16 * jj + 4 * g);
    }
#pragma unroll
    for (int i = 0; i < MB; ++i) {
      const f32x4 p0 = *(const f32x4*)(STI + (size_t)(erow + 16 * i) * 32 + 8 * g);
      const f32x4 p1 = *(const f32x4*)(STI + (size_t)(erow + 16 * i) * 32 + 8 * g + 4);
      float sm = (p0[0] + p0[2]) + (p1[0] + p1[2]), sq = (p0[1] + p0[3]) + (p1[1] + p1[3]);
      sm += __shfl_xor(sm, 16); sq += __shfl_xor(sq, 16);
      sm += __shfl_xor(sm, 32); sq += __shfl_xor(sq, 32);
      mean[i] = sm * (1.0f / DM);
      const float var = fmaxf(sq * (1.0f / DM) - mean[i] * mean[i], 0.f);
      const float rstd = __builtin_amdgcn_rsqf(var + LN_EPS);
      rs[i] = rstd;
      if constexpr (MODE == 4) {
        rs[i] = rstd * __builtin_amdgcn_rsqf(var * rstd * rstd + LN_EPS);
        if (ct == 0 && hf == 0 && g == 0) *(f32x2*)(ST2 + (size_t)erow * 2) = f32x2{mean[i], rstd};
      }
      tie(mean[i]); tie(rs[i]);
    }
  }

  f32x4 acc[MB][NB];
#pragma unroll
  for (int i = 0; i < MB; ++i)
#pragma unroll
    for (int j = 0; j < NB; ++j) acc[i][j] = f32x4{0.f, 0.f, 0.f, 0.f};
  const unsigned ard = lds0 + rg * (MB * 2048) + hf * 1024 + lane * 16;
  const unsigned brd = lds0 + AB + hf * HB + lane * 16;

  bf16x8 fa[2][MB], fb[2][NB];
  auto reads = [&](auto SC) {
    constexpr int s = decltype(SC)::v, b = s & 1;
    const unsigned sa = ard + (s % NS) * SLOT, sb = brd + (s % NS) * SLOT;
    sfor<0, MB>([&](auto JC) { constexpr int i = decltype(JC)::v; dsr16<i * 2048>(fa[b][i], sa); });
    sfor<0, NB>([&](auto JC) { constexpr int j = decltype(JC)::v; dsr16<j * 1024>(fb[b][j], sb); });
  };
  auto mm = [&](auto SC) {
    constexpr int b = decltype(SC)::v & 1;
#pragma unroll
    for (int i = 0; i < MB; ++i) tie(fa[b][i]);
#pragma unroll
    for (int j = 0; j < NB; ++j) tie(fb[b][j]);
#pragma unroll
    for (int j = 0; j < NB; ++j)
#pragma unroll
      for (int i = 0; i < MB; ++i) acc[i][j] = mfma16(fb[b][j], fa[b][i], acc[i][j]);
#pragma unroll
    for (int j = 0; j < NB; ++j)
#pragma unroll
      for (int i = 0; i < MB; ++i) tie(acc[i][j]);
  };
  STAMP(MODE, 2);
  sfor<0, NST>([&](auto SC) {
    constexpr int s = decltype(SC)::v;
    bar();
    if constexpr (s == 0) STAMP(MODE, 3);
    reads(SC);
    if constexpr (s > 0) {
      wait_lgkm<MB + NB>();
      mm(IC<s - 1>{});
    }
  });
  wait_lgkm<0>();
  mm(IC<NST - 1>{});
  STAMP(MODE, 4);

  {
    char* xw = lds + XOFF + wave * (3 * MB * 1024) + lane * 16;
    if (hf == 0) {
#pragma unroll
      for (int j = 3; j < NB; ++j)
#pragma unroll
        for (int i = 0; i < MB; ++i) *(f32x4*)(xw + ((j - 3) * MB + i) * 1024) = acc[i][j];
    } else {
#pragma unroll
      for (int j = 0; j < 3; ++j)
#pragma unroll
        for (int i = 0; i < MB; ++i) *(f32x4*)(xw + (j * MB + i) * 1024) = acc[i][j];
    }
  }
  lgkm_bar();
  STAMP(MODE, 5);
  f32x4 fin[MB][3];
  {
    const char* xr = lds + XOFF + (wave ^ 4) * (3 * MB * 1024) + lane * 16;
#pragma unroll
    for (int j = 0; j < 3; ++j)
#pragma unroll
      for (int i = 0; i < MB; ++i) fin[i][j] = *(const f32x4*)(xr + (j * MB + i) * 1024);
    if (hf == 0) {
#pragma unroll
      for (int j = 0; j < 3; ++j)
#pragma unroll
        for (int i = 0; i < MB; ++i) fin[i][j] += acc[i][j];
    } else {
#pragma unroll
      for (int j = 3; j < NB; ++j)
#pragma unroll
        for (int i = 0; i < MB; ++i) fin[i][j - 3] += acc[i][j];
    }
  }
  const int nj = (hf == 0) ? 3 : NB - 3;
  u16* Op;
  bool zero_out = false;
  constexpr int JSTR = (MODE == 7) ? 16 : 256;
  if constexpr (MODE == 7) Op = O + (size_t)erow * DM + c0 + 16 * jb + 4 * g;
  else if constexpr (MODE == 4) {
    const int dl = (ct < 2) ? 1 : (ct < 4) ? -1 : (ct < 6) ? 2 : -2;
    const int r2 = erow + dl;
    zero_out = (unsigned)r2 >= (unsigned)SEQ;
    const int r3 = r2 & (SEQ - 1);
    Op = O + (size_t)(r3 >> 4) * (DM * 16) + (ct * NB + jb) * 256 + (g >> 1) * 128 + (r3 & 15) * 8 + 4 * (g & 1);
  } else Op = O + (size_t)rb0 * (LDO * 16) + (ct * NB + jb) * 256 + lpart;
  if constexpr (LN) {
#pragma unroll
    for (int j = 0; j < 3; ++j) {
      if (j < nj) {
#pragma unroll
        for (int i = 0; i < MB; ++i) {
          float v[4];
#pragma unroll
          for (int e = 0; e < 4; ++e) v[e] = gelu_t((fin[i][j][e] - mean[i] * csv[j][e]) * rs[i]);
          u32x2 ov = u32x2{pack2(v[0], v[1]), pack2(v[2], v[3])};
          if (MODE == 4 && zero_out) ov = u32x2{0u, 0u};
          *(u32x2*)(Op + (size_t)i * 16 * LDO + JSTR * j) = ov;
        }
      }
    }
  } else {
    float sm = 0.f, sq = 0.f;
#pragma unroll
    for (int j = 0; j < 3; ++j) {
      if (j < nj) {
        float r[4] = {bflo(r0v[j][0]), bfhi(r0v[j][0]), bflo(r0v[j][1]), bfhi(r0v[j][1])};
        float v[4];
#pragma unroll
        for (int e = 0; e < 4; ++e) {
          if constexpr (MODE == 3) v[e] = r[e] + fin[0][j][e];
          else if constexpr (MODE == 5) v[e] = (r[e] - stv[0]) * stv[1] + fin[0][j][e];
          else {
            const float xv = (e & 1) ? bfhi(r1v[j][e >> 1]) : bflo(r1v[j][e >> 1]);
            v[e] = r[e] + (xv + fin[0][j][e]);
          }
          sm += v[e]; sq += v[e] * v[e];
        }
        *(u32x2*)(Op + JSTR * j) = u32x2{pack2(v[0], v[1]), pack2(v[2], v[3])};
      }
    }
    if constexpr (MODE == 3 || MODE == 5) {
      sm += __shfl_xor(sm, 16); sq += __shfl_xor(sq, 16);
      sm += __shfl_xor(sm, 32); sq += __shfl_xor(sq, 32);
      if (g == 0) *(f32x2*)(STO + (size_t)erow * 32 + (ct * 2 + hf) * 2) = f32x2{sm, sq};
    }
  }
  STAMP(MODE, 6);
  STAMP(MODE, 7);
  STAMP_FLUSH(MODE);
}

__global__ __launch_bounds__(512) void attn_k(const u16* __restrict__ Qg, const u16* __restrict__ Kg,
                                               const u16* __restrict__ Vg, u16* __restrict__ Og) {
  __shared__ __attribute__((aligned(16))) char lds[20480 * 2 + 32];
  const int tid = threadIdx.x, lane = tid & 63;
  const int wave = __builtin_amdgcn_readfirstlane(tid >> 6);
  const int chunk = blockIdx.x & 7, head = blockIdx.x >> 3;
  const int l15 = lane & 15, g = lane >> 4;
  STAMP_DECL; STAMP(2, 0); STAMP(2, 1);
  char* Kl = lds;
  char* Vl = lds + 20480;

  const char* Ksrc = (const char*)Kg + (size_t)head * (128 * 640) + (ptrdiff_t)(chunk - 1) * 10240 + lane * 16;
  const char* Vsrc = (const char*)Vg + (size_t)head * (64 * 1280) + (ptrdiff_t)(chunk - 1) * 10240 + lane * 16;
#pragma unroll
  for (int i = 0; i < 3; ++i) {
    const int n = wave + 8 * i;
    if (n < 20 && (chunk > 0 || n >= 10)) glds16(Ksrc + n * 1024, Kl + n * 1024);
  }
  const int qb0 = chunk * 16 + wave * 2;
  u32x2 qa0, qa1, qb0v, qb1v;
  {
    const u16* qp = Qg + (size_t)(head * 128 + qb0) * 320 + l15 * 4;
    qa0 = *(const u32x2*)(qp + g * 64);
    qa1 = *(const u32x2*)(qp + 256);
    qb0v = *(const u32x2*)(qp + 320 + g * 64);
    qb1v = *(const u32x2*)(qp + 320 + 256);
  }
  if (chunk == 0) {
#pragma unroll
    for (int i = 0; i < 2; ++i) {
      const int o = (tid + 512 * i) * 16;
      if (o < 10240) {
        *(u32x4*)(Kl + o) = u32x4{0u, 0u, 0u, 0u};
        *(u32x4*)(Vl + o) = u32x4{0u, 0u, 0u, 0u};
      }
    }
  }
  if (tid < 2) {
    const unsigned v = tid == 0 ? 0x3F803F80u : 0u;
    *(u32x4*)(lds + 40960 + tid * 16) = u32x4{v, v, v, v};
  }
  if (g != 0) { qa1 = u32x2{0u, 0u}; qb1v = u32x2{0u, 0u}; }
  const bf16x8 qA = __builtin_bit_cast(bf16x8, u32x4{qa0[0], qa0[1], qa1[0], qa1[1]});
  const bf16x8 qB = __builtin_bit_cast(bf16x8, u32x4{qb0v[0], qb0v[1], qb1v[0], qb1v[1]});
  asm volatile("s_waitcnt vmcnt(0) lgkmcnt(0)\n\ts_barrier" ::: "memory");

  STAMP(2, 2);
  f32x4 sA[17], sB[17];
  {
    const char* kp = Kl + wave * 1280 + l15 * 8;
    const char* kp0 = kp + g * 128;
    const f32x4 z = {0.f, 0.f, 0.f, 0.f};
#pragma unroll
    for (int i = 0; i < 18; ++i) {
      const u32x2 k0 = *(const u32x2*)(kp0 + i * 640);
      const u32x2 k1 = *(const u32x2*)(kp + 512 + i * 640);
      const bf16x8 kf = __builtin_bit_cast(bf16x8, u32x4{k0[0], k0[1], k1[0], k1[1]});
      if (i < 17) sA[i] = mfma16(kf, qA, z);
      if (i > 0) sB[i - 1] = mfma16(kf, qB, z);
    }
  }
  STAMP(2, 3);
#pragma unroll
  for (int i = 0; i < 3; ++i) {
    const int n = wave + 8 * i;
    if (n < 20 && (chunk > 0 || n >= 10)) glds16(Vsrc + n * 1024, Vl + n * 1024);
  }
  const float NEG = -__builtin_inff();
#pragma unroll
  for (int r = 0; r < 4; ++r) {
    const bool gt = (4 * g + r) > l15;
    sA[0][r] = gt ? sA[0][r] : NEG;
    sB[0][r] = gt ? sB[0][r] : NEG;
    sA[16][r] = gt ? NEG : sA[16][r];
    sB[16][r] = gt ? NEG : sB[16][r];
  }
  if (chunk == 0) {
#pragma unroll
    for (int i = 0; i < 16; ++i) {
      if (qb0 - 16 + i < 0) sA[i] = f32x4{NEG, NEG, NEG, NEG};
      if (qb0 - 15 + i < 0) sB[i] = f32x4{NEG, NEG, NEG, NEG};
    }
  }
  float mA = sA[16][0], mB = sB[16][0];
#pragma unroll
  for (int i = 0; i < 17; ++i) {
    mA = fmaxf(fmaxf(mA, sA[i][0]), sA[i][1]); mA = fmaxf(fmaxf(mA, sA[i][2]), sA[i][3]);
    mB = fmaxf(fmaxf(mB, sB[i][0]), sB[i][1]); mB = fmaxf(fmaxf(mB, sB[i][2]), sB[i][3]);
  }
  mA = fmaxf(mA, __shfl_xor(mA, 16)); mB = fmaxf(mB, __shfl_xor(mB, 16));
  mA = fmaxf(mA, __shfl_xor(mA, 32)); mB = fmaxf(mB, __shfl_xor(mB, 32));
  unsigned pA[18][2], pB[18][2];
#pragma unroll
  for (int i = 0; i < 17; ++i) {
    pA[i][0] = pack2(__builtin_amdgcn_exp2f(sA[i][0] - mA), __builtin_amdgcn_exp2f(sA[i][1] - mA));
    pA[i][1] = pack2(__builtin_amdgcn_exp2f(sA[i][2] - mA), __builtin_amdgcn_exp2f(sA[i][3] - mA));
    pB[i + 1][0] = pack2(__builtin_amdgcn_exp2f(sB[i][0] - mB), __builtin_amdgcn_exp2f(sB[i][1] - mB));
    pB[i + 1][1] = pack2(__builtin_amdgcn_exp2f(sB[i][2] - mB), __builtin_amdgcn_exp2f(sB[i][3] - mB));
  }
  pA[17][0] = pA[17][1] = 0u; pB[0][0] = pB[0][1] = 0u;
  STAMP(2, 4);
  asm volatile("s_waitcnt vmcnt(0) lgkmcnt(0)\n\ts_barrier" ::: "memory");
  STAMP(2, 5);

  f32x4 oA0 = {0.f, 0.f, 0.f, 0.f}, oA1 = oA0, oB0 = oA0, oB1 = oA0;
  {
    const char* vp0 = Vl + wave * 1280 + l15 * 64 + ((g ^ ((l15 >> 2) & 2)) << 4);
    int v1off = (l15 < 4) ? (20480 + wave * 1280 + (16 + l15) * 64 + g * 16) : (l15 == 4 ? 40960 : 40976);
    const int v1str = (l15 < 4) ? 1280 : 0;
#pragma unroll
    for (int t = 0; t < 9; ++t) {
      const bf16x8 v0 = *(const bf16x8*)(vp0 + t * 1280);
      const bf16x8 v1 = *(const bf16x8*)(lds + v1off);
      v1off += v1str;
      const bf16x8 fA = __builtin_bit_cast(bf16x8, u32x4{pA[2 * t][0], pA[2 * t][1], pA[2 * t + 1][0], pA[2 * t + 1][1]});
      const bf16x8 fB = __builtin_bit_cast(bf16x8, u32x4{pB[2 * t][0], pB[2 * t][1], pB[2 * t + 1][0], pB[2 * t + 1][1]});
      oA0 = mfma16(v0, fA, oA0); oA1 = mfma16(v1, fA, oA1);
      oB0 = mfma16(v0, fB, oB0); oB1 = mfma16(v1, fB, oB1);
    }
  }
  const float dA = __builtin_amdgcn_rcpf(__shfl(oA1[0], l15 + 16));
  const float dB = __builtin_amdgcn_rcpf(__shfl(oB1[0], l15 + 16));
  {
    const int ca = head * 20 + 4 * g, cb2 = head * 20 + 16;
    u16* opa = Og + (size_t)qb0 * (DM * 16) + (ca >> 3) * 128 + l15 * 8 + (ca & 7);
    u16* opb = Og + (size_t)qb0 * (DM * 16) + (cb2 >> 3) * 128 + l15 * 8 + (cb2 & 7);
    *(u32x2*)(opa) = u32x2{pack2(oA0[0] * dA, oA0[1] * dA), pack2(oA0[2] * dA, oA0[3] * dA)};
    *(u32x2*)(opa + DM * 16) = u32x2{pack2(oB0[0] * dB, oB0[1] * dB), pack2(oB0[2] * dB, oB0[3] * dB)};
    if (g == 0) {
      *(u32x2*)(opb) = u32x2{pack2(oA1[0] * dA, oA1[1] * dA), pack2(oA1[2] * dA, oA1[3] * dA)};
      *(u32x2*)(opb + DM * 16) = u32x2{pack2(oB1[0] * dB, oB1[1] * dB), pack2(oB1[2] * dB, oB1[3] * dB)};
    }
  }
  STAMP(2, 6);
  STAMP(2, 7);
  STAMP_FLUSH(2);
}

extern "C" void kernel_launch(void* const* d_in, const int* in_sizes, int n_in, void* d_out, int out_size,
                              void* d_ws, size_t ws_size, hipStream_t stream) {
  const u16* x = (const u16*)d_in[0];
  const u16* wqkv = (const u16*)d_in[1];
  const u16* wo = (const u16*)d_in[2];
  const u16* w1 = (const u16*)d_in[4];
  const u16* w2 = (const u16*)d_in[5];
  const u16* w3 = (const u16*)d_in[6];
  const u16* w4 = (const u16*)d_in[7];
  u16* out = (u16*)d_out;
  char* ws = (char*)d_ws;
  constexpr size_t MB4 = 4u << 20;
  u16* Qb = (u16*)(ws + 0 * MB4);
  u16* Kb = (u16*)(ws + 1 * MB4);
  u16* Vb = (u16*)(ws + 2 * MB4);
  u16* ob = (u16*)(ws + 3 * MB4);
  u16* hb = (u16*)(ws + 4 * MB4);
  u16* hh = (u16*)(ws + 5 * MB4);
  u16* x1 = (u16*)(ws + 6 * MB4);
  u16* gb = (u16*)(ws + 7 * MB4);
  float* sth = (float*)(ws + 9 * MB4);
  float* stx = (float*)(ws + 9 * MB4 + (1u << 20));

  gemm_k<1><<<240, 512, 0, stream>>>(x, wqkv, Qb, Kb, Vb, nullptr, nullptr, nullptr, nullptr);
  attn_k<<<256, 512, 0, stream>>>(Qb, Kb, Vb, ob);
  u16* Pk = (u16*)(ws + 10 * MB4);
  u16* xp = (u16*)(ws + 12 * MB4);
  float* cpart = (float*)(ws + 9 * MB4 + (2u << 20));
  float* csum = (float*)(ws + 9 * MB4 + (3u << 20));
  float* st2 = (float*)(ws + 9 * MB4 + (3u << 20) + (1u << 16));
  repack_k<<<(NUNITS + 7) / 8, 512, 0, stream>>>(x, wo, w1, w2, w3, w4, Pk, xp, cpart);
  gemm2_k<3><<<256, 768, 0, stream>>>(ob, Pk, hb, xp, nullptr, cpart, sth, nullptr, csum);
  gemm2_k<4><<<256, 768, 0, stream>>>(hb, Pk + DM * DM, hh, nullptr, nullptr, sth, nullptr, st2, csum);
  gemm2_k<5><<<256, 768, 0, stream>>>(hh, Pk + 2 * DM * DM, x1, hb, nullptr, nullptr, stx, st2, nullptr);
  gemm2_k<6><<<256, 768, 0, stream>>>(x1, Pk + 3 * DM * DM, gb, nullptr, nullptr, stx, nullptr, nullptr, csum + DM);
  gemm2_k<7><<<256, 768, 0, stream>>>(gb, Pk + 3 * DM * DM + DM * FF, out, hb, x1, nullptr, nullptr, nullptr, nullptr);
}
```

```cpp
#define MAP1
#include <hip/hip_runtime.h>
#include <stdint.h>

typedef unsigned short u16;
typedef short s16x4 __attribute__((ext_vector_type(4)));
typedef short s16x8 __attribute__((ext_vector_type(8)));
typedef __bf16 bf16x8 __attribute__((ext_vector_type(8)));
typedef __bf16 bf16x2 __attribute__((ext_vector_type(2)));
typedef float f32x2 __attribute__((ext_vector_type(2)));
typedef float f32x4 __attribute__((ext_vector_type(4)));
typedef unsigned u32x2 __attribute__((ext_vector_type(2)));
typedef unsigned u32x4 __attribute__((ext_vector_type(4)));
typedef __attribute__((address_space(3))) s16x4* lds_s16x4_p;

#define DEVI __device__ __forceinline__
#define STAMP_DECL
#define STAMP(kid, i)
#define STAMP_FLUSH(kid)

constexpr int SEQ = 2048, DM = 640, FF = 1536, NH = 32;
constexpr float LN_EPS = 1e-5f;
constexpr int CSN = DM + FF;

template <int I> struct IC { static constexpr int v = I; };
template <int B, int E, class F> DEVI void sfor(F&& f) {
  if constexpr (B < E) { f(IC<B>{}); sfor<B + 1, E>(f); }
}
template <int N> DEVI void wait_vm() { asm volatile("s_waitcnt vmcnt(%0)" ::"n"(N) : "memory"); }
DEVI void bar() { asm volatile("s_barrier" ::: "memory"); }
DEVI void lgkm_bar() { asm volatile("s_waitcnt lgkmcnt(0)\n\ts_barrier" ::: "memory"); }

DEVI unsigned pack2(float a, float b) {
  f32x2 v = {a, b};
  bf16x2 r = __builtin_convertvector(v, bf16x2);
  return __builtin_bit_cast(unsigned, r);
}
DEVI float bflo(unsigned u) { return __builtin_bit_cast(float, u << 16); }
DEVI float bfhi32(unsigned u) { return __builtin_bit_cast(float, u); }
DEVI float bfhi(unsigned u) { return __builtin_bit_cast(float, u & 0xffff0000u); }
DEVI f32x4 mfma16(bf16x8 a, bf16x8 b, f32x4 c) { return __builtin_amdgcn_mfma_f32_16x16x32_bf16(a, b, c, 0, 0, 0); }
DEVI float gelu_t(float x) {
  float e = __builtin_amdgcn_exp2f(x * __builtin_fmaf(x * x, -0.10294323969f, -2.30220819814f));
  return x * __builtin_amdgcn_rcpf(1.0f + e);
}
DEVI void glds16(const char* g, char* l) {
  __builtin_amdgcn_global_load_lds((const unsigned*)g, (unsigned*)l, 16, 0, 0);
}
DEVI bf16x8 tr_frag(const char* p0, const char* p1) {
  s16x4 a = __builtin_amdgcn_ds_read_tr16_b64_v4i16((lds_s16x4_p)p0);
  s16x4 b = __builtin_amdgcn_ds_read_tr16_b64_v4i16((lds_s16x4_p)p1);
  s16x8 f = __builtin_shufflevector(a, b, 0, 1, 2, 3, 4, 5, 6, 7);
  return __builtin_bit_cast(bf16x8, f);
}

template <class T> DEVI void tie(T& x) { asm volatile("" : "+v"(x)); }
template <int OFF, class T> DEVI void dsr16(T& d, unsigned addr) {
  asm volatile("ds_read_b128 %0, %1 offset:%2" : "=v"(d) : "v"(addr), "i"(OFF) : "memory");
}
template <int OFF> DEVI void dstr(u32x2& d, unsigned addr) {
  asm volatile("ds_read_b64_tr_b16 %0, %1 offset:%2" : "=v"(d) : "v"(addr), "i"(OFF) : "memory");
}
template <int N> DEVI void wait_lgkm() { asm volatile("s_waitcnt lgkmcnt(%0)" ::"n"(N) : "memory"); }

constexpr int k1_issued(int s) {
  int n = 2;
  for (int t = 0; t < s; ++t) {
    int lim = (t == 0 ? 4 : t + 4) + 1;
    if (lim > 10) lim = 10;
    n = n + 2 < lim ? n + 2 : lim;
  }
  return n;
}
__global__ __launch_bounds__(512, 2) void qkv_k(const u16* __restrict__ A, const u16* __restrict__ B, u16* __restrict__ O,
                                                const u16* __restrict__ w1, const u16* __restrict__ w3, float* __restrict__ CS) {
  constexpr int NS = 5, NST = 10, LDA = 640, LDB = 1920, ABYTES = 16384, PITCH = 256, SLOT = 32768;
  constexpr int XOFF = ((NST - 2) % NS) * SLOT;
  __shared__ __attribute__((aligned(16))) char lds[NS * SLOT];

  const int tid = threadIdx.x, lane = tid & 63;
  const int wave = __builtin_amdgcn_readfirstlane(tid >> 6);
  const int bid = blockIdx.x, xcd = bid & 7, idx = bid >> 3;
  STAMP_DECL; STAMP(1, 0); STAMP(1, 1);

  int rt, ct;
  rt = xcd * 2 + idx / 15; ct = idx % 15;
  const int row0 = rt * 128, c0 = ct * 128;

  if (wave < 4 && idx < 30) {
    const int lw = wave;
    const char* Ab = (const char*)A + (size_t)row0 * (LDA * 2);
    const char* Bb = (const char*)B + (size_t)c0 * 2;
    const unsigned acx = (unsigned)((lane & 7) ^ (lane >> 3));
    const unsigned aoff = (unsigned)(((lane >> 3) + 8 * lw) * (LDA * 2)) + acx * 16;
    unsigned boff;
    {
      const int rho = lw * 4 + (lane >> 4), cp = lane & 15;
      const int k = (rho & 3) | ((rho & 4) << 1) | ((rho & 8) >> 1);
      const int c = (((cp >> 1) ^ (rho & 7)) << 1) | (cp & 1);
      boff = (unsigned)(k * (LDB * 2) + c * 16);
    }
    auto issue = [&](auto SC) {
      constexpr int s = decltype(SC)::v;
      char* sl = lds + (s % NS) * SLOT + lw * 1024;
#pragma unroll
      for (int i = 0; i < 4; ++i) {
        glds16(Ab + (s * 128 + i * 32 * (LDA * 2)) + aoff, sl + i * 4096);
        glds16(Bb + (size_t)(s * 64 + i * 16) * (LDB * 2) + boff, sl + ABYTES + i * 4096);
      }
    };
    STAMP(1, 2);
    issue(IC<0>{}); issue(IC<1>{});
    STAMP(1, 3);
    sfor<0, NST>([&](auto SC) {
      constexpr int s = decltype(SC)::v;
      wait_vm<(k1_issued(s) - (s + 1)) * 8>();
      bar();
      if constexpr (s == 0) STAMP(1, 4);
      if constexpr (s == NST - 1) STAMP(1, 5);
      sfor<k1_issued(s), k1_issued(s + 1)>([&](auto TC) { issue(TC); });
    });
    bar();
    STAMP_FLUSH(1);
    return;
  }

  if (idx >= 30) {
    float* red = (float*)lds;
    const int wg = xcd * 2 + (idx - 30);
    const int ch = tid % 17, rg = tid / 17;
    if (tid < 8) ((unsigned*)CS)[16384 + (wg * 8 + tid) * 64] = 0u;
    if (tid < 510) {
      const int c8 = wg * 17 + ch;
      const u16* src = c8 < 80 ? w1 + c8 * 8 : w3 + (c8 - 80) * 8;
      const int ld = c8 < 80 ? DM : FF;
      u32x4 v[22];
#pragma unroll
      for (int i = 0; i < 22; ++i) {
        const int r = rg + 30 * i;
        v[i] = *(const u32x4*)(src + (size_t)(r < DM ? r : rg) * ld);
      }
      float sm[8] = {0.f, 0.f, 0.f, 0.f, 0.f, 0.f, 0.f, 0.f};
#pragma unroll
      for (int i = 0; i < 22; ++i) {
        const bool ok = rg + 30 * i < DM;
#pragma unroll
        for (int e = 0; e < 4; ++e) {
          const unsigned u = ok ? v[i][e] : 0u;
          sm[2 * e] += bflo(u); sm[2 * e + 1] += bfhi(u);
        }
      }
      *(f32x4*)(red + rg * 136 + ch * 8) = f32x4{sm[0], sm[1], sm[2], sm[3]};
      *(f32x4*)(red + rg * 136 + ch * 8 + 4) = f32x4{sm[4], sm[5], sm[6], sm[7]};
    }
    __syncthreads();
    if (tid < 136) {
      float t = 0.f;
#pragma unroll
      for (int i = 0; i < 30; ++i) t += red[i * 136 + tid];
      CS[wg * 136 + tid] = t;
    }
    return;
  }

  const int cw = wave - 4, wm = cw >> 1, wn = cw & 1;
  const int l15 = lane & 15, g = lane >> 4, q = l15 >> 2, p = lane & 3;
  const int mrot = 2 * wn, nrot = 2 * wm;
  const unsigned lds0 = (unsigned)(size_t)(__attribute__((address_space(3))) char*)lds;
  unsigned aad[3][2][2], bad[3][4];
  {
    const unsigned base = lds0 + (wm * 64 + l15) * 128 + ((g ^ (lane & 7)) << 4);
    const int rho = q + 4 * (g & 1) + 16 * (g >> 1);
#pragma unroll
    for (int t = 0; t < 3; ++t) {
#pragma unroll
      for (int hh = 0; hh < 2; ++hh) {
        const unsigned b = base + ((2 * hh + mrot) & 3) * 2048 + t * 65536;
        aad[t][0][hh] = b; aad[t][1][hh] = b ^ 64;
        tie(aad[t][0][hh]); tie(aad[t][1][hh]);
      }
#pragma unroll
      for (int j = 0; j < 4; ++j) {
        const int cb = wn * 4 + ((j + nrot) & 3);
        bad[t][j] = lds0 + ABYTES + rho * PITCH + ((cb ^ (rho & 7)) << 5) + p * 8 + t * 65536;
        tie(bad[t][j]);
      }
    }
  }
  f32x4 acc[4][4], csa[2], gra[2], rsa[2];
  const f32x4 z4 = {0.f, 0.f, 0.f, 0.f};
#pragma unroll
  for (int i = 0; i < 4; ++i)
#pragma unroll
    for (int j = 0; j < 4; ++j) acc[i][j] = z4;
  csa[0] = csa[1] = gra[0] = gra[1] = rsa[0] = rsa[1] = z4;
  const s16x8 ones_s = {0x3F80, 0x3F80, 0x3F80, 0x3F80, 0x3F80, 0x3F80, 0x3F80, 0x3F80};
  bf16x8 ones = __builtin_bit_cast(bf16x8, ones_s);
  tie(ones);

  bf16x8 fa[2][4];
  u32x2 fb[2][4][2];
  auto rd = [&](auto SC, auto HC, auto RC) {
    constexpr int s = decltype(SC)::v, h = decltype(HC)::v, r = decltype(RC)::v;
    constexpr int sl = s % NS, t = sl >> 1, so = (sl & 1) * SLOT;
    if constexpr (r % 3 == 0) { constexpr int i = r / 3; dsr16<so + (i & 1) * 2048>(fa[h][i], aad[t][h][i >> 1]); }
    else { constexpr int j = r / 3, e = r % 3 - 1; dstr<so + h * 32 * PITCH + e * 8 * PITCH>(fb[h][j][e], bad[t][j]); }
  };
  auto phase = [&](auto SC, auto HC) {
    constexpr int s = decltype(SC)::v, h = decltype(HC)::v;
    constexpr int ns = h ? s + 1 : s, nh = 1 - h;
    constexpr bool more = ns < NST;
    wait_lgkm<0>();
    bf16x8 b[4];
#pragma unroll
    for (int j = 0; j < 4; ++j) {
      tie(fa[h][j]); tie(fb[h][j][0]); tie(fb[h][j][1]);
      b[j] = __builtin_bit_cast(bf16x8, u32x4{fb[h][j][0][0], fb[h][j][0][1], fb[h][j][1][0], fb[h][j][1][1]});
    }
    sfor<0, 22>([&](auto MC) {
      constexpr int m = decltype(MC)::v;
      if constexpr (m < 16) {
        constexpr int i = m >> 2, j = m & 3;
        tie(fa[h][i]);
        acc[i][j] = mfma16(b[j], fa[h][i], acc[i][j]); tie(acc[i][j]);
      } else if constexpr (m < 18) {
        tie(ones);
        csa[m - 16] = mfma16(ones, b[m - 16], csa[m - 16]); tie(csa[m - 16]);
      } else if constexpr (m < 20) {
        tie(ones);
        rsa[m - 18] = mfma16(ones, fa[h][m - 18], rsa[m - 18]); tie(rsa[m - 18]);
      } else {
        tie(fa[h][m - 20]);
        gra[m - 20] = mfma16(fa[h][m - 20], fa[h][m - 20], gra[m - 20]); tie(gra[m - 20]);
      }
      constexpr int R0 = h ? 2 : 0;
      if constexpr (more && h == 1 && m == 1) bar();
      if constexpr (more && m >= R0 && m < R0 + 12) rd(IC<ns>{}, IC<nh>{}, IC<m - R0>{});
    });
  };
  STAMP(1, 2);
  bar();
  STAMP(1, 3);
  sfor<0, 12>([&](auto RC) { rd(IC<0>{}, IC<0>{}, RC); });
  sfor<0, NST>([&](auto SC) { phase(SC, IC<0>{}); phase(SC, IC<1>{}); });

  STAMP(1, 4);
  float* xs = (float*)(lds + XOFF);
  if (g == 0) {
#pragma unroll
    for (int j = 0; j < 2; ++j) {
      xs[(wn * 4 + ((j + nrot) & 3)) * 16 + l15] = csa[j][0];
      xs[128 + (wm * 4 + ((j + mrot) & 3)) * 16 + l15] = rsa[j][0];
    }
  }
  if (g == q) {
#pragma unroll
    for (int i = 0; i < 2; ++i)
      xs[256 + (wm * 4 + ((i + mrot) & 3)) * 16 + l15] = p == 0 ? gra[i][0] : p == 1 ? gra[i][1] : p == 2 ? gra[i][2] : gra[i][3];
  }
  lgkm_bar();
  STAMP(1, 5);
  {
    constexpr float INVK = 1.0f / 640;
    f32x4 cs[4];
    unsigned ooff[4];
    float sc[4];
#pragma unroll
    for (int j = 0; j < 4; ++j) {
      const int cl = (wn * 4 + ((j + nrot) & 3)) * 16 + 4 * g;
      cs[j] = *(const f32x4*)(xs + cl);
      const int c = c0 + cl;
      const int hd = c / 20, quad = (c - hd * 20) >> 2;
      ooff[j] = (unsigned)((hd * 640 + quad) * 64 + (l15 ^ ((quad & 2) << 2)) * 4);
      sc[j] = hd < 32 ? 0.22360679774997896f * 1.4426950408889634f : 1.0f;
    }
#pragma unroll
    for (int i = 0; i < 4; ++i) {
      const int rbl = wm * 4 + ((i + mrot) & 3);
      const int rl = rbl * 16 + l15;
      const float mean = xs[128 + rl] * INVK;
      const float var = fmaxf(xs[256 + rl] * INVK - mean * mean, 0.f);
      const float rs = __builtin_amdgcn_rsqf(var + LN_EPS);
      u16* dst = O + (size_t)((rt * 8 + rbl) * 320);
#pragma unroll
      for (int j = 0; j < 4; ++j) {
        const float r2 = rs * sc[j];
        u32x2 o;
        o[0] = pack2((acc[i][j][0] - mean * cs[j][0]) * r2, (acc[i][j][1] - mean * cs[j][1]) * r2);
        o[1] = pack2((acc[i][j][2] - mean * cs[j][2]) * r2, (acc[i][j][3] - mean * cs[j][3]) * r2);
        *(u32x2*)(dst + ooff[j]) = o;
      }
    }
  }
  STAMP(1, 6);
  STAMP(1, 7);
  STAMP_FLUSH(1);
}

DEVI void repack_unit(const u16* __restrict__ W, u16* __restrict__ P, int N, int KSW, int NB, int ks, int cq, int lane) {
  const int g = lane >> 4, q = lane & 15;
  const u16* src = W + (size_t)(32 * ks + 8 * g) * N + 64 * cq + 4 * q;
  u32x2 v[8];
#pragma unroll
  for (int e = 0; e < 8; ++e) v[e] = *(const u32x2*)(src + (size_t)e * N);
  const int cb = 4 * cq + (q >> 2);
  const int ct = cb / NB, j = cb - ct * NB;
  const int kw = ks / KSW, kk = ks - kw * KSW;
  u16* dst = P + ((size_t)(((ct * 4 + kw) * NB + j) * KSW + kk) * 64 + g * 16 + 4 * (q & 3)) * 8;
#pragma unroll
  for (int c = 0; c < 4; ++c) {
    u32x4 o;
#pragma unroll
    for (int e2 = 0; e2 < 4; ++e2) {
      const unsigned lo = v[2 * e2][c >> 1], hi = v[2 * e2 + 1][c >> 1];
      o[e2] = (c & 1) ? __builtin_amdgcn_perm(hi, lo, 0x07060302u) : __builtin_amdgcn_perm(hi, lo, 0x05040100u);
    }
    *(u32x4*)(dst + c * 8) = o;
  }
}
constexpr int NUNITS = 1560;
DEVI void repack_any(int u, int lane, const u16* __restrict__ wo, const u16* __restrict__ w1, const u16* __restrict__ w2,
                     const u16* __restrict__ w3, const u16* __restrict__ w4, u16* __restrict__ P) {
  if (u < 600) {
    const int wi = u / 200, r = u - wi * 200;
    const u16* W = wi == 0 ? wo : (wi == 1 ? w1 : w2);
    repack_unit(W, P + (size_t)wi * (DM * DM), DM, 5, 5, r / 10, r % 10, lane);
  } else if (u < 1080) {
    const int r = u - 600;
    repack_unit(w3, P + (size_t)3 * (DM * DM), FF, 5, 12, r / 24, r % 24, lane);
  } else {
    const int r = u - 1080;
    repack_unit(w4, P + (size_t)3 * (DM * DM) + (size_t)DM * FF, DM, 12, 5, r / 10, r % 10, lane);
  }
}
#define THR 4
template <int OFF, class T> DEVI void gld16(T& d, unsigned voff, const char* sbase) {
  asm volatile("global_load_dwordx4 %0, %1, %2 offset:%3" : "=v"(d) : "v"(voff), "s"(sbase), "i"(OFF) : "memory");
}
template <int OFF, class T> DEVI void gld16s(T& d, unsigned voff, const char* sbase) {
  asm volatile("global_load_dwordx4 %0, %1, %2 offset:%3 sc1" : "=v"(d) : "v"(voff), "s"(sbase), "i"(OFF) : "memory");
}
DEVI void gst16s(unsigned voff, u32x4 d, char* sbase) {
  asm volatile("global_store_dwordx4 %0, %1, %2 sc1" : : "v"(voff), "v"(d), "s"(sbase) : "memory");
}
DEVI void gst8s(unsigned voff, u32x2 d, char* sbase) {
  asm volatile("global_store_dwordx2 %0, %1, %2 sc1" : : "v"(voff), "v"(d), "s"(sbase) : "memory");
}
DEVI void lds_set(unsigned addr, unsigned val) {
  asm volatile("s_mov_b64 exec, 1\n\tds_write_b32 %0, %1\n\ts_mov_b64 exec, -1" : : "v"(addr), "v"(val) : "memory");
}
DEVI unsigned lds_get(unsigned addr) {
  unsigned v;
  asm volatile("ds_read_b32 %0, %1\n\ts_waitcnt lgkmcnt(0)" : "=v"(v) : "v"(addr) : "memory");
  return __builtin_amdgcn_readfirstlane(v);
}
template <int N> DEVI unsigned lds_get_vm(unsigned addr) {
  unsigned v;
  asm volatile("ds_read_b32 %0, %1\n\ts_waitcnt vmcnt(%2) lgkmcnt(0)" : "=v"(v) : "v"(addr), "n"(N) : "memory");
  return __builtin_amdgcn_readfirstlane(v);
}
constexpr int LSPIN = 1 << 22, GSPIN = 1 << 16;
DEVI void lds_wait1(unsigned addr, unsigned tag) {
  for (int i = 0; i < LSPIN; ++i)
    if (lds_get(addr) >= tag) break;
}
DEVI void lds_wait4(unsigned addr, unsigned tag) {
  for (int i = 0; i < LSPIN; ++i) {
    u32x4 f;
    asm volatile("ds_read_b128 %0, %1\n\ts_waitcnt lgkmcnt(0)" : "=v"(f) : "v"(addr) : "memory");
    const unsigned a = f[0] < f[1] ? f[0] : f[1], b = f[2] < f[3] ? f[2] : f[3];
    if (__builtin_amdgcn_readfirstlane(a < b ? a : b) >= tag) break;
  }
}
DEVI float gsum(float v) {
  const unsigned u = __builtin_bit_cast(unsigned, v);
  const auto a = __builtin_amdgcn_permlane32_swap(u, u, false, false);
  const float s = bfhi32(a[0]) + bfhi32(a[1]);
  const unsigned t = __builtin_bit_cast(unsigned, s);
  const auto b = __builtin_amdgcn_permlane16_swap(t, t, false, false);
  return bfhi32(b[0]) + bfhi32(b[1]);
}

constexpr size_t MB1 = 1u << 20;
constexpr size_t W_QKV = 0, W_ACT = 12 * MB1  , W_G = 28 * MB1, W_STH = 36 * MB1, W_STX = 37 * MB1,
                 W_CS = 39 * MB1, W_CNT = 39 * MB1 + 65536, W_PK = 40 * MB1;
constexpr int NSLOT = 36;
constexpr int L_CS = NSLOT * 4096;
constexpr int L_ST = L_CS + 1152;
constexpr int L_IF = L_ST + 2048;
constexpr int L_P1 = L_IF + 19 * 16;
constexpr int L_GO = L_P1 + 16;
constexpr int L_END = L_GO + 16;
constexpr int NFLW = (L_END - L_IF) / 4;

constexpr int p7_kind(int p) { return p < 20 ? 0 : p < 47 ? ((p - 20) % 9 < 5 ? 1 : 0) : p < 67 ? 1 : 0; }
constexpr int p7_ks(int p) { return p < 20 ? p / 4 : p < 47 ? 5 + (p - 20) / 9 : p < 67 ? 8 + (p - 47) / 5 : 8 + (p - 67) % 4; }
constexpr int p7_ix(int p) { return p < 20 ? p % 4 : p < 47 ? ((p - 20) % 9 < 5 ? (p - 20) % 9 : (p - 20) % 9 - 5) : p < 67 ? (p - 47) % 5 : (p - 67) / 4; }
constexpr int p7_need(int s) { return s < 5 ? 4 * (s + 1) : s < 8 ? 20 + 9 * (s - 4) : 67 + 4 * (s - 7); }
#define P7I 8
constexpr int p7_iss(int s) { return P7I + 9 * s < 83 ? P7I + 9 * s : 83; }

#define MST(i)
#define MST_FLUSH()

__global__ __launch_bounds__(512, 2) void mlp_k(char* __restrict__ ws, const u16* __restrict__ X, u16* __restrict__ OUT) {
  __shared__ __attribute__((aligned(16))) char lds[L_END];
  const int tid = threadIdx.x, lane = tid & 63;
  const int wave = __builtin_amdgcn_readfirstlane(tid >> 6);
  const int bid = blockIdx.x, xcd = bid & 7, idx = bid >> 3;
  const int rt = xcd * 4 + (idx >> 3), ct = idx & 7;
  const unsigned lds0 = (unsigned)(size_t)(__attribute__((address_space(3))) char*)lds;
  MST(0);

  if (wave < 4) {
    const int w = wave;
    const unsigned voff = lane * 16;
    const char* Pk = ws + W_PK;
    const char* B6 = Pk + (size_t)3 * (DM * DM * 2) + (ct * 4 + w) * 61440;
    const char* B7 = Pk + (size_t)3 * (DM * DM * 2) + (size_t)DM * FF * 2 + (ct * 4 + w) * 61440;
    char* xw = lds + w * 1024 + lane * 16;
    const unsigned fl = lds0 + L_IF + w * 4, goa = lds0 + L_GO;
    const f32x4 z = {0.f, 0.f, 0.f, 0.f};
    bf16x8 fa[4][5], fb[5][5];
    {
      const char* Bb = Pk + (ct * 4 + w) * 25600;
      sfor<0, 25>([&](auto TC) { constexpr int t = decltype(TC)::v; gld16<(t & 3) * 1024>(fb[t / 5][t % 5], voff, Bb + (t >> 2) * 4096); });
    }
    bar();
#pragma clang loop unroll(disable)
    for (int it = 0; it < 4; ++it) {
      const char* Ab = ws + W_ACT + (size_t)it * (4 * MB1) + (size_t)(rt * 4) * (DM * 32) + w * 5120;
      const char* Bn = it < 2 ? Pk + (size_t)(it + 1) * (DM * DM * 2) + (ct * 4 + w) * 25600 : B6 + (it - 2) * 25600;
      const unsigned tag = it + 1;
      MST(1 + it * 4);
      sfor<0, 20>([&](auto TC) {
        constexpr int rb = decltype(TC)::v / 5, ks = decltype(TC)::v % 5;
        gld16s<(ks & 3) * 1024>(fa[rb][ks], voff, Ab + rb * (DM * 32) + (ks >> 2) * 4096);
      });
      sfor<0, 4>([&](auto RC) {
        constexpr int rb = decltype(RC)::v;
        wait_vm<15 - 5 * rb>();
        if constexpr (rb == 0) MST(2 + it * 4);
#pragma unroll
        for (int ks = 0; ks < 5; ++ks) tie(fa[rb][ks]);
        if constexpr (rb == 0) {
#pragma unroll
          for (int t = 0; t < 25; ++t) tie(fb[t / 5][t % 5]);
        }
        f32x4 c0 = z, c1 = z, c2 = z, c3 = z, c4 = z;
#pragma unroll
        for (int ks = 0; ks < 5; ++ks) {
          c0 = mfma16(fb[0][ks], fa[rb][ks], c0); tie(c0);
          c1 = mfma16(fb[1][ks], fa[rb][ks], c1); tie(c1);
        }
#pragma unroll
        for (int ks = 0; ks < 5; ++ks) {
          c2 = mfma16(fb[2][ks], fa[rb][ks], c2); tie(c2);
          c3 = mfma16(fb[3][ks], fa[rb][ks], c3); tie(c3);
          c4 = mfma16(fb[4][ks], fa[rb][ks], c4); tie(c4);
          if (ks == 0) {
            *(f32x4*)(xw + (rb * 5 + 0) * 4096) = c0;
            *(f32x4*)(xw + (rb * 5 + 1) * 4096) = c1;
            lds_set(fl + (rb * 3) * 16, tag);
          }
          if constexpr (rb == 3) {
            if (ks == 1) {
              sfor<0, THR>([&](auto TC) { constexpr int t = decltype(TC)::v; gld16<(t & 3) * 1024>(fb[t / 5][t % 5], voff, Bn + (t >> 2) * 4096); });
            }
          }
        }
        *(f32x4*)(xw + (rb * 5 + 4) * 4096) = c4;
        lds_set(fl + (rb * 3 + 2) * 16, tag);
        *(f32x4*)(xw + (rb * 5 + 2) * 4096) = c2;
        *(f32x4*)(xw + (rb * 5 + 3) * 4096) = c3;
        lds_set(fl + (rb * 3 + 1) * 16, tag);
      });
      MST(3 + it * 4);
      const unsigned gt = it < 3 ? tag : 0u;
      bool go = it == 3;
      sfor<THR, 25>([&](auto TC) {
        constexpr int t = decltype(TC)::v;
        if (!go) go = lds_get_vm<THR - 1>(goa) >= gt;
        gld16<(t & 3) * 1024>(fb[t / 5][t % 5], voff, Bn + (t >> 2) * 4096);
      });
      MST(4 + it * 4);
      if (!go) lds_wait1(goa, gt);
    }
    bf16x8 g6[2][5], b7[5][12];
    sfor<0, 5>([&](auto TC) { constexpr int t = 50 + decltype(TC)::v; gld16<(t & 3) * 1024>(g6[0][t - 50], voff, B6 + (t >> 2) * 4096); });
    MST(17);
    sfor<5, 12>([&](auto JC) {
      constexpr int jj = decltype(JC)::v;
      f32x4 c[4] = {z, z, z, z};
      sfor<0, 5>([&](auto KC) {
        constexpr int ks = decltype(KC)::v, n = (jj - 5) * 5 + ks;
        constexpr int iss = 30 + n < 60 ? 30 + n : 60;
        wait_vm<iss - n - 1>();
        bf16x8& b = jj < 10 ? fb[(jj - 5) % 5][ks] : g6[(jj - 10) & 1][ks];
        tie(b);
#pragma unroll
        for (int rb = 0; rb < 4; ++rb) { c[rb] = mfma16(b, fa[rb][ks], c[rb]); tie(c[rb]); }
        if constexpr (n < 5) { constexpr int t = 55 + n; gld16<(t & 3) * 1024>(g6[1][n], voff, B6 + (t >> 2) * 4096); }
        else if constexpr (n < 30) {
          constexpr int k7 = (n - 5) / 5, j7 = (n - 5) % 5, t = j7 * 12 + k7;
          gld16<(t & 3) * 1024>(b7[j7][k7], voff, B7 + (t >> 2) * 4096);
        }
      });
      if constexpr (jj == 9) lds_wait4(lds0 + L_P1, 1);
#pragma unroll
      for (int rb = 0; rb < 4; ++rb) {
        const int n = (jj - 5) * 4 + rb, sl = n < 16 ? 20 + n : n - 16;
        *(f32x4*)(xw + sl * 4096) = c[rb];
      }
      lds_set(fl + (12 + jj - 5) * 16, 1);
    });
    MST(18);
    lds_wait1(goa, 4);
    MST(19);
    {
      const char* Ab = ws + W_G + (size_t)(rt * 4) * (FF * 32) + w * 12288;
      bf16x8 a7[4][12];
      f32x4 acc[5][4];
      auto issue = [&](auto PC) {
        constexpr int p = decltype(PC)::v, ks = p7_ks(p), ix = p7_ix(p);
        if constexpr (p7_kind(p) == 0) gld16s<(ks & 3) * 1024>(a7[ix][ks], voff, Ab + ix * (FF * 32) + (ks >> 2) * 4096);
        else { constexpr int t = ix * 12 + ks; gld16<(t & 3) * 1024>(b7[ix][ks], voff, B7 + (t >> 2) * 4096); }
      };
      sfor<0, P7I>(issue);
      sfor<0, 8>([&](auto SC) {
        constexpr int s = decltype(SC)::v;
        wait_vm<p7_iss(s) - p7_need(s)>();
        if constexpr (s == 0) MST(20);
#pragma unroll
        for (int rb = 0; rb < 4; ++rb) tie(a7[rb][s]);
        sfor<0, 5>([&](auto JC) {
          constexpr int j = decltype(JC)::v;
          tie(b7[j][s]);
#pragma unroll
          for (int rb = 0; rb < 4; ++rb) { acc[j][rb] = mfma16(b7[j][s], a7[rb][s], s == 0 ? z : acc[j][rb]); tie(acc[j][rb]); }
          if constexpr (p7_iss(s) + j < p7_iss(s + 1)) issue(IC<p7_iss(s) + j>{});
        });
        sfor<(p7_iss(s) + 5 < p7_iss(s + 1) ? p7_iss(s) + 5 : p7_iss(s + 1)), p7_iss(s + 1)>(issue);
      });
      MST(21);
      sfor<0, 4>([&](auto RC) {
        constexpr int rb = decltype(RC)::v;
        wait_vm<p7_iss(rb == 0 ? 8 : 9) - p7_need(8 + rb)>();
#pragma unroll
        for (int ks = 8; ks < 12; ++ks) tie(a7[rb][ks]);
        if constexpr (rb == 0) {
#pragma unroll
          for (int t = 0; t < 20; ++t) tie(b7[t % 5][8 + t / 5]);
        }
#pragma unroll
        for (int ks = 8; ks < 12; ++ks) {
          acc[0][rb] = mfma16(b7[0][ks], a7[rb][ks], acc[0][rb]); tie(acc[0][rb]);
          acc[1][rb] = mfma16(b7[1][ks], a7[rb][ks], acc[1][rb]); tie(acc[1][rb]);
        }
#pragma unroll
        for (int ks = 8; ks < 12; ++ks) {
          acc[2][rb] = mfma16(b7[2][ks], a7[rb][ks], acc[2][rb]); tie(acc[2][rb]);
          acc[3][rb] = mfma16(b7[3][ks], a7[rb][ks], acc[3][rb]); tie(acc[3][rb]);
          acc[4][rb] = mfma16(b7[4][ks], a7[rb][ks], acc[4][rb]); tie(acc[4][rb]);
          if (ks == 8) {
            *(f32x4*)(xw + (rb * 5 + 0) * 4096) = acc[0][rb];
            *(f32x4*)(xw + (rb * 5 + 1) * 4096) = acc[1][rb];
            lds_set(fl + (rb * 3) * 16, 5);
          }
        }
        if constexpr (rb == 0) sfor<p7_iss(8), p7_iss(9)>(issue);
        *(f32x4*)(xw + (rb * 5 + 2) * 4096) = acc[2][rb];
        *(f32x4*)(xw + (rb * 5 + 3) * 4096) = acc[3][rb];
        lds_set(fl + (rb * 3 + 1) * 16, 5);
        *(f32x4*)(xw + (rb * 5 + 4) * 4096) = acc[4][rb];
        lds_set(fl + (rb * 3 + 2) * 16, 5);
      });
    }
    MST(22);
    MST_FLUSH();
    return;
  }

  const int e = wave - 4;
  const int l15 = lane & 15, g = lane >> 4;
  const int erow = rt * 64 + e * 16 + l15;
  u32x4 xa, xb;
  u32x2 xc;
  {
    const u16* xp = X + (size_t)erow * DM + ct * 80;
    xa = *(const u32x4*)(xp + 8 * g);
    xb = *(const u32x4*)(xp + 32 + 8 * g);
    xc = *(const u32x2*)(xp + 64 + 4 * g);
    const float* cs = (const float*)(ws + W_CS);
    const int i = e * 64 + lane;
    float cv = 0.f, cv2 = 0.f;
    if (i < 80) cv = cs[ct * 80 + i];
    if (i < 192) cv2 = cs[DM + ct * 192 + i];
    if (i < NFLW) *(unsigned*)(lds + L_IF + i * 4) = 0u;
    lgkm_bar();
    if (i < 80) *(float*)(lds + L_CS + i * 4) = cv;
    if (i < 192) *(float*)(lds + L_CS + 320 + i * 4) = cv2;
  }
  const unsigned flb = lds0 + L_IF;
  const int psrc = (g >> 1) * 4096 + (g & 1) * 512 + l15 * 16;
  auto rd8 = [&](int off, float* f) {
    const char* s = lds + off;
    f32x4 a = *(const f32x4*)(s), b = *(const f32x4*)(s + 256);
    f32x4 a1 = *(const f32x4*)(s + 1024), b1 = *(const f32x4*)(s + 1280);
    f32x4 a2 = *(const f32x4*)(s + 2048), b2 = *(const f32x4*)(s + 2304);
    f32x4 a3 = *(const f32x4*)(s + 3072), b3 = *(const f32x4*)(s + 3328);
    a = (a + a1) + (a2 + a3); b = (b + b1) + (b2 + b3);
#pragma unroll
    for (int c = 0; c < 4; ++c) { f[c] = a[c]; f[4 + c] = b[c]; }
  };
  auto rd4 = [&](int off, float* f) {
    const char* s = lds + off;
    f32x4 a = *(const f32x4*)(s), a1 = *(const f32x4*)(s + 1024), a2 = *(const f32x4*)(s + 2048), a3 = *(const f32x4*)(s + 3072);
    a = (a + a1) + (a2 + a3);
#pragma unroll
    for (int c = 0; c < 4; ++c) f[c] = a[c];
  };
  auto gate = [&](int k, bool stats) {
    wait_vm<0>();
    MST(3 + k * 6);
    unsigned old;
    asm volatile("s_mov_b64 exec, 1\n\tds_add_rtn_u32 %0, %1, %2\n\ts_mov_b64 exec, -1\n\ts_waitcnt lgkmcnt(0)"
                 : "=v"(old) : "v"(lds0 + L_GO + 8), "v"(1u) : "memory");
    if (__builtin_amdgcn_readfirstlane(old) != (unsigned)(4 * k + 3)) return;
    unsigned* cnt = (unsigned*)(ws + W_CNT) + k * (32 * 64);
    const unsigned target = k == 1 ? 16u : 8u;
    unsigned v = 0;
    if (lane == 0) {
      if (k == 1) __hip_atomic_fetch_add(cnt + ((rt + ((ct & 2) ? 31 : 1)) & 31) * 64, 1u, __ATOMIC_RELAXED, __HIP_MEMORY_SCOPE_AGENT);
      v = __hip_atomic_fetch_add(cnt + rt * 64, 1u, __ATOMIC_RELAXED, __HIP_MEMORY_SCOPE_AGENT) + 1u;
      for (int i = 0; i < GSPIN && v < target; ++i) v = __hip_atomic_load(cnt + rt * 64, __ATOMIC_RELAXED, __HIP_MEMORY_SCOPE_AGENT);
    }
    asm volatile("" ::: "memory");
    MST(4 + k * 6);
    if (stats) {
      const char* sp = ws + (k == 0 ? W_STH : W_STX) + (size_t)rt * 4096;
      f32x4 s[4];
      const unsigned vo = l15 * 64 + g * 16;
      gld16s<0>(s[0], vo, sp); gld16s<1024>(s[1], vo, sp); gld16s<2048>(s[2], vo, sp); gld16s<3072>(s[3], vo, sp);
      lds_set(lds0 + L_GO, k + 1);
      wait_vm<0>();
      float s1 = 0.f, s2 = 0.f;
#pragma unroll
      for (int q = 0; q < 4; ++q) {
        tie(s[q]);
        const float a = gsum(s[q][0] + s[q][2]), b = gsum(s[q][1] + s[q][3]);
        if (g == q) { s1 = a; s2 = b; }
      }
      const float mean = s1 * (1.0f / DM);
      const float var = fmaxf(s2 * (1.0f / DM) - mean * mean, 0.f);
      const float rs = __builtin_amdgcn_rsqf(var + LN_EPS);
      const float rs2 = rs * __builtin_amdgcn_rsqf(var * rs * rs + LN_EPS);
      *(f32x4*)(lds + L_ST + (k >> 1) * 1024 + lane * 16) = f32x4{mean, k == 0 ? rs2 : rs, rs, 0.f};
      lds_set(lds0 + L_GO + 4, k + 1);
      MST(5 + k * 6);
    } else lds_set(lds0 + L_GO, k + 1);
  };
  auto res_phase = [&](auto MC, const float* r, u32x4& ka, u32x4& kb, u32x2& kc) {
    constexpr int MODE = decltype(MC)::v;
    constexpr unsigned tag = MODE - 2;
    char* ob = MODE == 7 ? (char*)OUT : ws + W_ACT + (MODE == 3 ? 1 : 3) * (4 * MB1);
    const unsigned oa = MODE == 7 ? (unsigned)(erow * DM + ct * 80 + 8 * g) * 2 : (unsigned)((rt * 4 + e) * (DM * 16) + (ct * 10 + g) * 128 + l15 * 8) * 2;
    const unsigned oc = MODE == 7 ? (unsigned)(erow * DM + ct * 80 + 64 + 4 * g) * 2
                                  : (unsigned)((rt * 4 + e) * (DM * 16) + (ct * 10 + 8 + (g >> 1)) * 128 + l15 * 8 + 4 * (g & 1)) * 2;
    float sm = 0.f, sq = 0.f;
#pragma unroll
    for (int t = 0; t < 3; ++t) {
      lds_wait4(flb + (e * 3 + t) * 16, tag);
      if (t == 0) MST(1 + (MODE - 3) * 6);
      float f[8];
      if (t < 2) rd8(e * 20480 + t * 8192 + psrc, f); else rd4(e * 20480 + 16384 + lane * 16, f);
#pragma unroll
      for (int c = 0; c < (t < 2 ? 8 : 4); ++c) {
        f[c] += r[t * 8 + c];
        if constexpr (MODE == 7) {
          const unsigned hv = t == 0 ? xa[c >> 1] : t == 1 ? xb[c >> 1] : xc[(c >> 1) & 1];
          f[c] += (c & 1) ? bfhi(hv) : bflo(hv);
        } else { sm += f[c]; sq += f[c] * f[c]; }
      }
      if (t < 2) {
        const u32x4 o = {pack2(f[0], f[1]), pack2(f[2], f[3]), pack2(f[4], f[5]), pack2(f[6], f[7])};
        if constexpr (MODE == 7) *(u32x4*)(ob + oa + t * 64) = o;
        else { gst16s(oa + t * 1024, o, ob); if (t == 0) ka = o; else kb = o; }
      } else {
        const u32x2 o = {pack2(f[0], f[1]), pack2(f[2], f[3])};
        if constexpr (MODE == 7) *(u32x2*)(ob + oc) = o;
        else { gst8s(oc, o, ob); kc = o; }
      }
    }
    if constexpr (MODE != 7) {
      sm = gsum(sm); sq = gsum(sq);
      if (g == 0) gst8s((unsigned)(erow * 64 + ct * 8), __builtin_bit_cast(u32x2, f32x2{sm, sq}), ws + (MODE == 3 ? W_STH : W_STX));
    }
    MST(2 + (MODE - 3) * 6);
  };
  auto unpack = [&](float* r, const u32x4& a, const u32x4& b, const u32x2& c) {
#pragma unroll
    for (int i = 0; i < 4; ++i) { r[2 * i] = bflo(a[i]); r[2 * i + 1] = bfhi(a[i]); r[8 + 2 * i] = bflo(b[i]); r[9 + 2 * i] = bfhi(b[i]); }
    r[16] = bflo(c[0]); r[17] = bfhi(c[0]); r[18] = bflo(c[1]); r[19] = bfhi(c[1]);
  };
  auto gelu8 = [&](int src, int csoff, int stoff, unsigned oa, char* ob, bool zero) {
    float f[8];
    rd8(src, f);
    const f32x4 c0 = *(const f32x4*)(lds + L_CS + csoff), c1 = *(const f32x4*)(lds + L_CS + csoff + 16);
    const f32x2 st = *(const f32x2*)(lds + L_ST + stoff);
#pragma unroll
    for (int c = 0; c < 8; ++c) f[c] = gelu_t((f[c] - st[0] * (c < 4 ? c0[c & 3] : c1[c & 3])) * st[1]);
    u32x4 o = {pack2(f[0], f[1]), pack2(f[2], f[3]), pack2(f[4], f[5]), pack2(f[6], f[7])};
    if (zero) o = u32x4{0u, 0u, 0u, 0u};
    gst16s(oa, o, ob);
  };
  auto gelu_phase = [&](int ph  ) {
    const unsigned tag = 2 + 2 * ph;
    char* ob = ph ? ws + W_G : ws + W_ACT + 2 * (4 * MB1);
    const int ld16 = ph ? FF * 16 : DM * 16, cg0 = ph ? ct * 24 : ct * 10;
    const int dl = ph ? 0 : ((ct < 2) ? 1 : (ct < 4) ? -1 : (ct < 6) ? 2 : -2);
    lds_wait1(lds0 + L_GO + 4, 1 + 2 * ph);
#pragma clang loop unroll(disable)
    for (int rb = 0; rb < 4; ++rb) {
      const int t = (e - rb) & 3;
      if (t == 3) continue;
      const int r2 = rt * 64 + rb * 16 + l15 + dl;
      const bool zero = (unsigned)r2 >= (unsigned)SEQ;
      const int r3 = r2 & (SEQ - 1);
      const int obase = (r3 >> 4) * ld16 + (r3 & 15) * 8;
      const int stoff = ph * 1024 + (rb * 16 + l15) * 16;
      lds_wait4(flb + (rb * 3 + t) * 16, tag);
      if (t < 2) gelu8(rb * 20480 + t * 8192 + psrc, ph * 320 + (32 * t + 8 * g) * 4, stoff, (unsigned)(obase + (cg0 + 4 * t + g) * 128) * 2, ob, zero);
      else {
        float f[4];
        rd4(rb * 20480 + 16384 + lane * 16, f);
        const f32x4 c0 = *(const f32x4*)(lds + L_CS + ph * 320 + (64 + 4 * g) * 4);
        const f32x2 st = *(const f32x2*)(lds + L_ST + stoff);
#pragma unroll
        for (int c = 0; c < 4; ++c) f[c] = gelu_t((f[c] - st[0] * c0[c]) * st[1]);
        u32x2 o = {pack2(f[0], f[1]), pack2(f[2], f[3])};
        if (zero) o = u32x2{0u, 0u};
        gst8s((unsigned)(obase + (cg0 + 8 + (g >> 1)) * 128 + 4 * (g & 1)) * 2, o, ob);
      }
    }
  };

  u32x4 ya, yb;
  u32x2 yc;
  float r[20];
  unpack(r, xa, xb, xc);
  res_phase(IC<3>{}, r, xa, xb, xc);
  gate(0, true);
  gelu_phase(0);
  MST(8);
  gate(1, false);
  unpack(r, xa, xb, xc);
  {
    const f32x4 st = *(const f32x4*)(lds + L_ST + (e * 16 + l15) * 16);
#pragma unroll
    for (int i = 0; i < 20; ++i) r[i] = (r[i] - st[0]) * st[2];
  }
  res_phase(IC<5>{}, r, ya, yb, yc);
  gate(2, true);
  gelu_phase(1);
  asm volatile("s_waitcnt lgkmcnt(0)" ::: "memory");
  lds_set(lds0 + L_P1 + e * 4, 1);
  MST(19);
  {
    const int rb = (e & 2) + (g >> 1);
    const int obase = (rt * 4 + rb) * (FF * 16) + (ct * 24 + (g & 1)) * 128 + l15 * 8;
#pragma clang loop unroll(disable)
    for (int jj = 5 + (e & 1); jj < 12; jj += 2) {
      const int n = (jj - 5) * 4 + rb, sl = n < 16 ? 20 + n : n - 16;
      lds_wait4(flb + (12 + jj - 5) * 16, 1);
      gelu8(sl * 4096 + (g & 1) * 512 + l15 * 16, 320 + (jj * 16 + 8 * (g & 1)) * 4, 1024 + (rb * 16 + l15) * 16, (unsigned)(obase + jj * 256) * 2, ws + W_G, false);
    }
  }
  MST(20);
  gate(3, false);
  unpack(r, ya, yb, yc);
  res_phase(IC<7>{}, r, ya, yb, yc);
  MST(27);
  MST_FLUSH();
}

DEVI float gmax(float v) {
  const unsigned u = __builtin_bit_cast(unsigned, v);
  const auto a = __builtin_amdgcn_permlane32_swap(u, u, false, false);
  const float s = fmaxf(bfhi32(a[0]), bfhi32(a[1]));
  const unsigned t = __builtin_bit_cast(unsigned, s);
  const auto b = __builtin_amdgcn_permlane16_swap(t, t, false, false);
  return fmaxf(bfhi32(b[0]), bfhi32(b[1]));
}
DEVI float max4(float m, f32x4 s) { return fmaxf(fmaxf(fmaxf(m, s[0]), s[1]), fmaxf(s[2], s[3])); }
DEVI u32x2 exp_pack(f32x4 s) {
  return u32x2{pack2(__builtin_amdgcn_exp2f(s[0]), __builtin_amdgcn_exp2f(s[1])),
               pack2(__builtin_amdgcn_exp2f(s[2]), __builtin_amdgcn_exp2f(s[3]))};
}
__global__ __launch_bounds__(768, 3) void attn_k(const u16* __restrict__ QKV, u16* __restrict__ Og,
                                                  const u16* __restrict__ wo, const u16* __restrict__ w1,
                                                  const u16* __restrict__ w2, const u16* __restrict__ w3, const u16* __restrict__ w4,
                                                  u16* __restrict__ P) {
  constexpr int C0 = 40960 + 128;
  __shared__ __attribute__((aligned(16))) char lds[C0 + 34 * 640];
  const int tid = threadIdx.x, lane = tid & 63;
  const int wave = __builtin_amdgcn_readfirstlane(tid >> 6);
  const int chunk = blockIdx.x & 7, head = blockIdx.x >> 3;
  const int l15 = lane & 15, g = lane >> 4;
  STAMP_DECL; STAMP(2, 0); STAMP(2, 1);
  if (wave >= 8) {
    bar();
    const int u0 = blockIdx.x * 4 + (wave - 8);
    repack_any(u0, lane, wo, w1, w2, w3, w4, P);
    if (u0 + 1024 < NUNITS) repack_any(u0 + 1024, lane, wo, w1, w2, w3, w4, P);
    return;
  }
  {
    const char* src = (const char*)QKV + (size_t)(32 + head) * (128 * 640) + chunk * 10240 + lane * 16;
    const int back = chunk > 0 ? 10240 : 0;
#pragma unroll
    for (int i = 0; i < 5; ++i) {
      const int n = wave + 8 * i;
      const int v = n >= 20 ? 1 : 0, m = n - 20 * v;
      glds16(src + (size_t)v * (32 * 128 * 640) + (m < 10 ? m * 1024 - back : (m - 10) * 1024), lds + n * 1024);
    }
  }
  const int qb0 = chunk * 16 + wave * 2;
  const int rsw = l15 ^ ((g & 2) << 2);
  u32x2 qa0, qa1, qb0v, qb1v;
  {
    const u16* qp = QKV + (size_t)(head * 128 + qb0) * 320;
    qa0 = *(const u32x2*)(qp + g * 64 + rsw * 4);
    qa1 = *(const u32x2*)(qp + 256 + l15 * 4);
    qb0v = *(const u32x2*)(qp + 320 + g * 64 + rsw * 4);
    qb1v = *(const u32x2*)(qp + 320 + 256 + l15 * 4);
  }
  if (tid < 34) {
    *(u32x4*)(lds + C0 + tid * 640) = u32x4{0x3F80u, 0u, 0u, 0u};
    *(u32x2*)(lds + C0 + tid * 640 + 16) = u32x2{(chunk == 0 && tid < 16) ? 0x3F80u : 0u, 0u};
  }
  if (g != 0) { qa1 = u32x2{g == 1 ? 0xFF00u : 0u, 0u}; qb1v = qa1; }
  const bf16x8 qA = __builtin_bit_cast(bf16x8, u32x4{qa0[0], qa0[1], qa1[0], qa1[1]});
  const bf16x8 qB = __builtin_bit_cast(bf16x8, u32x4{qb0v[0], qb0v[1], qb1v[0], qb1v[1]});
  const float NEG = -__builtin_inff();
  f32x4 cm0, cm16;
#pragma unroll
  for (int r = 0; r < 4; ++r) {
    const bool gt = (4 * g + r) > l15;
    cm0[r] = gt ? 0.f : NEG;
    cm16[r] = gt ? NEG : 0.f;
  }
  asm volatile("s_waitcnt vmcnt(0) lgkmcnt(0)\n\ts_barrier" ::: "memory");
  STAMP(2, 2);

  bf16x8 kf[18];
  float mA = NEG, mB = NEG;
  {
    const char* kp0 = lds + wave * 1280 + g * 128 + rsw * 8;
    const char* kp1 = lds + wave * 1280 + (g == 1 ? C0 + 16 : 512 + l15 * 8);
    const f32x4 z = {0.f, 0.f, 0.f, 0.f};
#pragma unroll
    for (int i = 0; i < 18; ++i) {
      const u32x2 k0 = *(const u32x2*)(kp0 + i * 640);
      const u32x2 k1 = *(const u32x2*)(kp1 + i * 640);
      kf[i] = __builtin_bit_cast(bf16x8, u32x4{k0[0], k0[1], k1[0], k1[1]});
      if (i < 17) mA = max4(mA, mfma16(kf[i], qA, i == 0 ? cm0 : i == 16 ? cm16 : z));
      if (i > 0) mB = max4(mB, mfma16(kf[i], qB, i == 1 ? cm0 : i == 17 ? cm16 : z));
    }
  }
  mA = -gmax(mA); mB = -gmax(mB);
  STAMP(2, 3);
  const f32x4 cA = {mA, mA, mA, mA}, cB = {mB, mB, mB, mB};
  const f32x4 cA0 = cm0 + cA, cA16 = cm16 + cA, cB0 = cm0 + cB, cB16 = cm16 + cB;

  f32x4 oA0 = {0.f, 0.f, 0.f, 0.f}, oA1 = oA0, oB0 = oA0, oB1 = oA0;
  {
    const int q = l15 >> 2, p = l15 & 3;
    const char* vp0 = lds + 20480 + wave * 1280 + p * 128 + (((4 * g + q) ^ ((p & 2) << 2)) << 3);
    const char* vp1 = lds + (p == 0 ? 20480 + wave * 1280 + 512 + (4 * g + q) * 8 : C0 + (p == 1 ? 0 : 8));
#pragma unroll
    for (int t = 0; t < 9; ++t) {
      const bf16x8 v0 = tr_frag(vp0 + t * 1280, vp0 + t * 1280 + 640);
      const bf16x8 v1 = tr_frag(vp1 + t * 1280, vp1 + t * 1280 + 640);
      const u32x2 a0 = exp_pack(mfma16(kf[2 * t], qA, t == 0 ? cA0 : t == 8 ? cA16 : cA));
      const u32x2 a1 = t < 8 ? exp_pack(mfma16(kf[2 * t + 1], qA, cA)) : u32x2{0u, 0u};
      const u32x2 b0 = t > 0 ? exp_pack(mfma16(kf[2 * t], qB, cB)) : u32x2{0u, 0u};
      const u32x2 b1 = exp_pack(mfma16(kf[2 * t + 1], qB, t == 0 ? cB0 : t == 8 ? cB16 : cB));
      const bf16x8 fA = __builtin_bit_cast(bf16x8, u32x4{a0[0], a0[1], a1[0], a1[1]});
      const bf16x8 fB = __builtin_bit_cast(bf16x8, u32x4{b0[0], b0[1], b1[0], b1[1]});
      oA0 = mfma16(v0, fA, oA0); oA1 = mfma16(v1, fA, oA1);
      oB0 = mfma16(v0, fB, oB0); oB1 = mfma16(v1, fB, oB1);
    }
  }
  STAMP(2, 5);
  const float dA = __builtin_amdgcn_rcpf(__shfl(oA1[0], l15 + 16));
  const float dB = __builtin_amdgcn_rcpf(__shfl(oB1[0], l15 + 16));
  {
    const int ca = head * 20 + 4 * g, cb2 = head * 20 + 16;
    u16* opa = Og + (size_t)qb0 * (DM * 16) + (ca >> 3) * 128 + l15 * 8 + (ca & 7);
    u16* opb = Og + (size_t)qb0 * (DM * 16) + (cb2 >> 3) * 128 + l15 * 8 + (cb2 & 7);
    *(u32x2*)(opa) = u32x2{pack2(oA0[0] * dA, oA0[1] * dA), pack2(oA0[2] * dA, oA0[3] * dA)};
    *(u32x2*)(opa + DM * 16) = u32x2{pack2(oB0[0] * dB, oB0[1] * dB), pack2(oB0[2] * dB, oB0[3] * dB)};
    if (g == 0) {
      *(u32x2*)(opb) = u32x2{pack2(oA1[0] * dA, oA1[1] * dA), pack2(oA1[2] * dA, oA1[3] * dA)};
      *(u32x2*)(opb + DM * 16) = u32x2{pack2(oB1[0] * dB, oB1[1] * dB), pack2(oB1[2] * dB, oB1[3] * dB)};
    }
  }
  STAMP(2, 6);
  STAMP(2, 7);
  STAMP_FLUSH(2);
}

extern "C" void kernel_launch(void* const* d_in, const int* in_sizes, int n_in, void* d_out, int out_size,
                              void* d_ws, size_t ws_size, hipStream_t stream) {
  const u16* x = (const u16*)d_in[0];
  const u16* wqkv = (const u16*)d_in[1];
  const u16* wo = (const u16*)d_in[2];
  const u16* w1 = (const u16*)d_in[4];
  const u16* w2 = (const u16*)d_in[5];
  const u16* w3 = (const u16*)d_in[6];
  const u16* w4 = (const u16*)d_in[7];
  u16* out = (u16*)d_out;
  char* ws = (char*)d_ws;
  u16* Qb = (u16*)(ws + W_QKV);
  u16* ob = (u16*)(ws + W_ACT);
  u16* Pk = (u16*)(ws + W_PK);
  float* csum = (float*)(ws + W_CS);
  qkv_k<<<256, 512, 0, stream>>>(x, wqkv, Qb, w1, w3, csum);
  attn_k<<<256, 768, 0, stream>>>(Qb, ob, wo, w1, w2, w3, w4, Pk);
  mlp_k<<<256, 512, 0, stream>>>(ws, x, out);
}
```

```cpp
#define MAP1
#define CW 1
#define K1PL 0
#include <hip/hip_runtime.h>
#include <stdint.h>

typedef unsigned short u16;
typedef short s16x4 __attribute__((ext_vector_type(4)));
typedef short s16x8 __attribute__((ext_vector_type(8)));
typedef __bf16 bf16x8 __attribute__((ext_vector_type(8)));
typedef __bf16 bf16x2 __attribute__((ext_vector_type(2)));
typedef float f32x2 __attribute__((ext_vector_type(2)));
typedef float f32x4 __attribute__((ext_vector_type(4)));
typedef unsigned u32x2 __attribute__((ext_vector_type(2)));
typedef unsigned u32x4 __attribute__((ext_vector_type(4)));
typedef __attribute__((address_space(3))) s16x4* lds_s16x4_p;

#define DEVI __device__ __forceinline__
#define STAMP_DECL
#define STAMP(kid, i)
#define STAMP_FLUSH(kid)

constexpr int SEQ = 2048, DM = 640, FF = 1536, NH = 32;
constexpr float LN_EPS = 1e-5f;
constexpr int CSN = DM + FF;

constexpr size_t MB1 = 1u << 20, ACTB = (size_t)SEQ * DM * 2, GUARD = 16 * DM * 2;
constexpr size_t W_QKV = 0, W_O = 3 * ACTB, W_HH = W_O + ACTB + GUARD, W_H = W_HH + ACTB + GUARD, W_X1 = W_H + ACTB, W_G = W_X1 + ACTB,
                 W_CS = W_G + (size_t)SEQ * FF * 2, W_PK = W_CS + 20 * CSN * 4;
constexpr size_t W_F0 = W_O;
static_assert(960 * 8 * 1024 == 3 * ACTB, "fill region");
#define HFILL 0
#define QST 1
#define CST 0
#define OST 0
#define NOFB 0
#define CWF 2
#define ROLL 0
#define PREC 1
#define FS0 1
#define FSTEP 2
#define UL 5
#define UT 0
#define NSP 1
static_assert(UL >= 5 && UL <= 7, "behind the last stage");

template <int I> struct IC { static constexpr int v = I; };
template <int B, int E, class F> DEVI void sfor(F&& f) {
  if constexpr (B < E) { f(IC<B>{}); sfor<B + 1, E>(f); }
}
template <int N> DEVI void wait_vm() { asm volatile("s_waitcnt vmcnt(%0)" ::"n"(N) : "memory"); }
DEVI void bar() { asm volatile("s_barrier" ::: "memory"); }
DEVI void lgkm_bar() { asm volatile("s_waitcnt lgkmcnt(0)\n\ts_barrier" ::: "memory"); }

DEVI unsigned pack2(float a, float b) {
  f32x2 v = {a, b};
  bf16x2 r = __builtin_convertvector(v, bf16x2);
  return __builtin_bit_cast(unsigned, r);
}
DEVI float bflo(unsigned u) { return __builtin_bit_cast(float, u << 16); }
DEVI float bfhi32(unsigned u) { return __builtin_bit_cast(float, u); }
DEVI float bfhi(unsigned u) { return __builtin_bit_cast(float, u & 0xffff0000u); }
DEVI f32x4 mfma16(bf16x8 a, bf16x8 b, f32x4 c) { return __builtin_amdgcn_mfma_f32_16x16x32_bf16(a, b, c, 0, 0, 0); }
DEVI float gelu_t(float x) {
  float e = __builtin_amdgcn_exp2f(x * __builtin_fmaf(x * x, -0.10294323969f, -2.30220819814f));
  return x * __builtin_amdgcn_rcpf(1.0f + e);
}
DEVI void glds16(const char* g, char* l) {
  __builtin_amdgcn_global_load_lds((const unsigned*)g, (unsigned*)l, 16, 0, 0);
}
DEVI void glds16a(unsigned voff, const char* sbase, unsigned l) {
  asm volatile("s_mov_b32 m0, %2\n\ts_nop 0\n\tglobal_load_lds_dwordx4 %0, %1" : : "v"(voff), "s"(sbase), "s"(l) : "memory", "m0");
}
DEVI void gld8a(u32x2& d, unsigned voff, const char* sbase) {
  asm volatile("global_load_dwordx2 %0, %1, %2" : "=v"(d) : "v"(voff), "s"(sbase) : "memory");
}
DEVI bf16x8 tr_frag(const char* p0, const char* p1) {
  s16x4 a = __builtin_amdgcn_ds_read_tr16_b64_v4i16((lds_s16x4_p)p0);
  s16x4 b = __builtin_amdgcn_ds_read_tr16_b64_v4i16((lds_s16x4_p)p1);
  s16x8 f = __builtin_shufflevector(a, b, 0, 1, 2, 3, 4, 5, 6, 7);
  return __builtin_bit_cast(bf16x8, f);
}

#define CODE_MARK(name) asm volatile(#name ":")
#define CODE_WARM(dst, here, b, e, e2, first) do { \
    unsigned long long pc_; int sz_, s2_, dl_; \
    asm volatile("s_getpc_b64 %0\n" #here ":\n\ts_mov_b32 %2, " #b "-" #here "\n\ts_mov_b32 %1, " #e "-" #b "\n\ts_mov_b32 %3, " #e2 "-" #b \
                 : "=s"(pc_), "=s"(sz_), "=s"(dl_), "=s"(s2_)); \
    pc_ += (long long)dl_; \
    sz_ = sz_ > s2_ ? sz_ : s2_; \
    sz_ = sz_ > 4 ? sz_ : 4; \
    unsigned off_ = (unsigned)((first) + 32 * lane) * 128u; \
    off_ = off_ < (unsigned)sz_ - 4 ? off_ : (unsigned)sz_ - 4; \
    asm volatile("global_load_dword %0, %1, %2" : "=v"(dst) : "v"(off_ & ~3u), "s"(pc_) : "memory"); \
  } while (0)
template <class T> DEVI void tie(T& x) { asm volatile("" : "+v"(x)); }
template <int OFF, class T> DEVI void dsr16(T& d, unsigned addr) {
  asm volatile("ds_read_b128 %0, %1 offset:%2" : "=v"(d) : "v"(addr), "i"(OFF) : "memory");
}
template <int OFF> DEVI void dstr(u32x2& d, unsigned addr) {
  asm volatile("ds_read_b64_tr_b16 %0, %1 offset:%2" : "=v"(d) : "v"(addr), "i"(OFF) : "memory");
}
template <int N> DEVI void wait_lgkm() { asm volatile("s_waitcnt lgkmcnt(%0)" ::"n"(N) : "memory"); }

DEVI void gst16s(unsigned voff, u32x4 d, char* sbase) {
  asm volatile("global_store_dwordx4 %0, %1, %2 sc1\n\ts_nop 1" : : "v"(voff), "v"(d), "s"(sbase) : "memory");
}
DEVI void gst16o(unsigned voff, u32x4 d, char* sbase) {
  *(u32x4*)(sbase + voff) = d;
}
DEVI void gst8s(unsigned voff, u32x2 d, char* sbase) {
  asm volatile("global_store_dwordx2 %0, %1, %2 sc1\n\ts_nop 1" : : "v"(voff), "v"(d), "s"(sbase) : "memory");
}
DEVI float gsum(float v) {
  const unsigned u = __builtin_bit_cast(unsigned, v);
  const auto a = __builtin_amdgcn_permlane32_swap(u, u, false, false);
  const float s = bfhi32(a[0]) + bfhi32(a[1]);
  const unsigned t = __builtin_bit_cast(unsigned, s);
  const auto b = __builtin_amdgcn_permlane16_swap(t, t, false, false);
  return bfhi32(b[0]) + bfhi32(b[1]);
}
constexpr int NUNITS = 1560;
DEVI void unit_geo(int u, int& m, int& ks, int& cq) {
  m = u < 600 ? (u * 328) >> 16 : (u < 1080 ? 3 : 4);
  const int r = u - (m < 4 ? m * 200 : 1080);
  ks = (r * (m == 3 ? 2731 : 6554)) >> 16;
  cq = r - ks * (m == 3 ? 24 : 10);
}
DEVI unsigned frag_off(int u, int i) {
  int m, ks, cq;
  unit_geo(u, m, ks, cq);
  const int cb = 4 * cq + i;
  const int ct = (cb * (m == 3 ? 5462 : 13108)) >> 16, j = cb - ct * (m == 3 ? 12 : 5);
  const int kw = (ks * (m == 4 ? 5462 : 13108)) >> 16, kk = ks - kw * (m == 4 ? 12 : 5);
  const int k6 = kk >= 6 ? 1 : 0;
  const int s = m == 4 ? ((kk - 6 * k6) >> 1) * 4 + k6 * 2 + (kk & 1) - 8 : kk;
  const int pa = (j >> 1) * 10 + kk * 2 + (j & 1);
  const int pb = s < 0 ? (s + 8) * 5 + j : (m == 4 ? 40 : 0) + (j < 2 ? s * 2 + j : (m == 4 ? 6 : 8) + s * 3 + j);
  return (unsigned)((m < 4 ? m * (DM * DM * 2) : 3 * (DM * DM * 2) + DM * FF * 2) + ((ct * 4 + kw) * (m < 3 ? 25 : 60) + (m == 3 ? pa : pb)) * 1024);
}
DEVI const char* unit_src(int u, int g, const u16* __restrict__ wo, const u16* __restrict__ w1, const u16* __restrict__ w2,
                          const u16* __restrict__ w3, const u16* __restrict__ w4, unsigned& off, int& N2, bool& rem) {
  int m, ks, cq;
  unit_geo(u, m, ks, cq);
  const u16* W = m == 0 ? wo : m == 1 ? w1 : m == 2 ? w2 : m == 3 ? w3 : w4;
  N2 = m == 3 ? FF * 2 : DM * 2;
  const int kw = (ks * 13108) >> 16, kk = ks - kw * 5;
  rem = m == 0 && kk == 4;
  const int row = m != 0 ? 32 * ks + 8 * g : kw * 160 + (rem ? 40 * g + 16 : 40 * kk + 20 * (g >> 1) + 8 * (g & 1));
  off = (unsigned)(row * N2 + 128 * cq);
  return (const char*)W;
}
DEVI void unit_load(int u, int lane, const u16* __restrict__ wo, const u16* __restrict__ w1, const u16* __restrict__ w2,
                    const u16* __restrict__ w3, const u16* __restrict__ w4, u32x2* v) {
  unsigned off; int N2; bool rem;
  const char* src = unit_src(u, lane >> 4, wo, w1, w2, w3, w4, off, N2, rem);
  off += 8 * (lane & 15);
  const int up = rem ? 16 * N2 : 0;
#pragma unroll
  for (int e = 0; e < 8; ++e)
    asm volatile("global_load_dwordx2 %0, %1, %2" : "=v"(v[e]) : "v"(off + (unsigned)(e * N2 + (e >= 4 ? up : 0))), "s"(src) : "memory");
}
DEVI void unit_store(int u, int lane, char* __restrict__ ws, const u32x2* v, char* lp, unsigned fo, int l0) {
  const int g = lane >> 4, q = lane & 15;
#pragma unroll
  for (int c = 0; c < 4; ++c) {
    u32x4 o;
#pragma unroll
    for (int e2 = 0; e2 < 4; ++e2) {
      const unsigned lo = v[2 * e2][c >> 1], hi = v[2 * e2 + 1][c >> 1];
      o[e2] = (c & 1) ? __builtin_amdgcn_perm(hi, lo, 0x07060302u) : __builtin_amdgcn_perm(hi, lo, 0x05040100u);
    }
    *(u32x4*)(lp + (q >> 2) * 1024 + (g * 16 + 4 * (q & 3) + c) * 16) = o;
  }
  asm volatile("s_waitcnt lgkmcnt(0)" ::: "memory");
  u32x4 o[4];
#pragma unroll
  for (int i = 0; i < 4; ++i) o[i] = *(const u32x4*)(lp + i * 1024 + lane * 16);
#pragma unroll
  for (int i = 0; i < 4; ++i) {
    char* dst = ws + W_PK + (unsigned)__builtin_amdgcn_readlane(fo, l0 + i);
    gst16s((unsigned)lane * 16, o[i], dst);
  }
  int m, ks, cq;
  unit_geo(u, m, ks, cq);
  if (m & 1) {
    float s[4] = {0.f, 0.f, 0.f, 0.f};
#pragma unroll
    for (int e = 0; e < 8; ++e) {
      s[0] += bflo(v[e][0]); s[1] += bfhi(v[e][0]); s[2] += bflo(v[e][1]); s[3] += bfhi(v[e][1]);
    }
#pragma unroll
    for (int c = 0; c < 4; ++c) s[c] = gsum(s[c]);
    if (g == 0) *(f32x4*)(ws + W_CS + (size_t)(ks * CSN + (m == 1 ? 0 : DM) + 64 * cq + 4 * q) * 4) = f32x4{s[0], s[1], s[2], s[3]};
  }
}

constexpr int k1_issued(int s) {
  int n = 2;
  for (int t = 0; t < s; ++t) {
    int lim = (t == 0 ? 4 : t + 4) + 1;
    if (lim > 10) lim = 10;
    n = n + 2 < lim ? n + 2 : lim;
  }
  return n;
}
__global__ __launch_bounds__(512, 2) void qkv_k(const u16* __restrict__ A, const u16* __restrict__ B, char* __restrict__ ws,
                                                const u16* __restrict__ wo, const u16* __restrict__ w1, const u16* __restrict__ w2,
                                                const u16* __restrict__ w3, const u16* __restrict__ w4) {
  constexpr int NS = 5, NST = 10, LDA = 640, LDB = 1920, ABYTES = 16384, PITCH = 256, SLOT = 32768;
  constexpr int XOFF = ((NST - 2) % NS) * SLOT;
  __shared__ __attribute__((aligned(16))) char lds[NS * SLOT];

  const int tid = threadIdx.x, lane = tid & 63;
  const int wave = __builtin_amdgcn_readfirstlane(tid >> 6);
  const int bid = blockIdx.x, xcd = bid & 7, idx = bid >> 3;
  STAMP_DECL; STAMP(1, 0); STAMP(1, 1);

  int rt, ct;
  rt = xcd * 2 + idx / 15; ct = idx % 15;
  const int row0 = rt * 128, c0 = ct * 128;

  if (wave < 4 && idx < 30) {
    const int lw = wave;
    const char* Ab = (const char*)A + (size_t)row0 * (LDA * 2);
    const char* Bb = (const char*)B + (size_t)c0 * 2;
    const unsigned acx = (unsigned)((lane & 7) ^ (lane >> 3));
    const unsigned aoff = (unsigned)(((lane >> 3) + 8 * lw) * (LDA * 2)) + acx * 16;
    unsigned boff;
    {
      const int rho = lw * 4 + (lane >> 4), cp = lane & 15;
      const int k = (rho & 3) | ((rho & 4) << 1) | ((rho & 8) >> 1);
      const int c = (((cp >> 1) ^ (rho & 7)) << 1) | (cp & 1);
      boff = (unsigned)(k * (LDB * 2) + c * 16);
    }
    auto issue = [&](auto SC) {
      constexpr int s = decltype(SC)::v;
      char* sl = lds + (s % NS) * SLOT + lw * 1024;
#pragma unroll
      for (int i = 0; i < 4; ++i) {
        glds16(Ab + (s * 128 + i * 32 * (LDA * 2)) + aoff, sl + i * 4096);
        glds16(Bb + (size_t)(s * 64 + i * 16) * (LDB * 2) + boff, sl + ABYTES + i * 4096);
      }
    };
    STAMP(1, 2);
    issue(IC<0>{}); issue(IC<1>{});
    unsigned pfv;
    {
      const char* pa;
      if (lw == K1PL) pa = (const char*)B + (size_t)(3840 + lane * 240 + xcd * 30 + idx) * 128;
      else {
        int j = (((lw - K1PL - 1) & 3) * 64 + lane) * 30 + idx;
        j = j < 2048 ? j : 2047;
        pa = (const char*)A + (size_t)(xcd * 256 + (j & 255)) * 1280 + (2 + (j >> 8)) * 128;
      }
      asm volatile("global_load_dword %0, %1, off" : "=v"(pfv) : "v"(pa) : "memory");
    }
    constexpr int PFX = 1;
    STAMP(1, 3);
    u32x2 ua[8], ub[8];
    const int u0 = lw * 240 + xcd * 30 + idx, u1 = u0 + 960 < NUNITS - 128 * NSP ? u0 + 960 : u0;
    unsigned fo = frag_off((lane & 4) ? u1 : u0, lane & 3);
    tie(fo);
    sfor<0, NST>([&](auto SC) {
      constexpr int s = decltype(SC)::v;
      if constexpr (s < NST - 1) wait_vm<(k1_issued(s) - (s + 1)) * 8 + (s < 2 ? PFX : 0) + (s > UL ? 16 : 0)>();
      bar();
      if constexpr (s == 0) STAMP(1, 4);
      if constexpr (s == NST - 1) STAMP(1, 5);
      sfor<k1_issued(s), k1_issued(s + 1)>([&](auto TC) { issue(TC); });
      if constexpr (s == UL) {
        unit_load(u0, lane, wo, w1, w2, w3, w4, ua);
        unit_load(u1, lane, wo, w1, w2, w3, w4, ub);
      }
      if constexpr (s == NST - 2) {
        wait_vm<8>();
        tie(pfv);
#pragma unroll
        for (int e = 0; e < 8; ++e) tie(ua[e]);
        unit_store(u0, lane, ws, ua, lds + lw * 8192, fo, 0);
      }
    });
    wait_vm<4>();
#pragma unroll
    for (int e = 0; e < 8; ++e) tie(ub[e]);
    if (u1 != u0) unit_store(u1, lane, ws, ub, lds + lw * 8192 + 4096, fo, 4);
    bar();
    STAMP(1, 6);
    STAMP(1, 7);
    STAMP_FLUSH(1);
    return;
  }

  if (idx >= 30) {
    const int wg = xcd * 2 + (idx - 30);
    unsigned pfv[5];
    {
      const int t = (idx - 30) * 512 + tid;
#pragma unroll
      for (int i = 0; i < 2; ++i) {
        int L = i * 1024 + t;
        const char* pb = (const char*)B + (size_t)(3840 + (L < 1920 ? L : 1919) * 8 + xcd) * 128;
        const char* pa = (const char*)A + (size_t)(xcd * 256 + (L & 255)) * 1280 + (2 + (L >> 8)) * 128;
        asm volatile("global_load_dword %0, %1, off" : "=v"(pfv[2 * i]) : "v"(pb) : "memory");
        asm volatile("global_load_dword %0, %1, off" : "=v"(pfv[2 * i + 1]) : "v"(pa) : "memory");
      }
      CODE_WARM(pfv[4], Lq_h2, Lf_b, Lf_e, Lf_e2, wg * 2 + (wave & 1));
    }
    if (wg * 8 + wave < 40) {
      const int z = wg * 8 + wave;
      gst16s((unsigned)(z * 1024 + lane * 16), u32x4{0u, 0u, 0u, 0u}, ws + W_HH - GUARD + (z >= 20 ? ACTB : 0));
    }
    u32x2 uv[NSP + 1][8];
    const int ub = NUNITS - 128 * NSP + wg * 8 + wave;
#pragma unroll
    for (int i = 0; i < NSP; ++i) unit_load(ub + 128 * i, lane, wo, w1, w2, w3, w4, uv[i]);
    const int li = (lane >> 2) & 7;
    unsigned fo = frag_off(ub + 128 * (li < NSP ? li : 0), lane & 3);
    tie(fo);
    wait_vm<0>();
#pragma unroll
    for (int i = 0; i < 5; ++i) tie(pfv[i]);
#pragma unroll
    for (int i = 0; i < NSP; ++i) {
#pragma unroll
      for (int e = 0; e < 8; ++e) tie(uv[i][e]);
      unit_store(ub + 128 * i, lane, ws, uv[i], lds + (wave * 5 + i) * 4096, fo, 4 * i);
    }
    return;
  }

  const int cw = wave - 4, wm = cw >> 1, wn = cw & 1;
  const int l15 = lane & 15, g = lane >> 4, q = l15 >> 2, p = lane & 3;
  const int mrot = 2 * wn, nrot = 2 * wm;
  const unsigned lds0 = (unsigned)(size_t)(__attribute__((address_space(3))) char*)lds;
  unsigned aad[3][2][2], bad[3][4];
  {
    const unsigned base = lds0 + (wm * 64 + l15) * 128 + ((g ^ (lane & 7)) << 4);
    const int rho = q + 4 * (g & 1) + 16 * (g >> 1);
#pragma unroll
    for (int t = 0; t < 3; ++t) {
#pragma unroll
      for (int hh = 0; hh < 2; ++hh) {
        const unsigned b = base + ((2 * hh + mrot) & 3) * 2048 + t * 65536;
        aad[t][0][hh] = b; aad[t][1][hh] = b ^ 64;
        tie(aad[t][0][hh]); tie(aad[t][1][hh]);
      }
#pragma unroll
      for (int j = 0; j < 4; ++j) {
        const int cb = wn * 4 + ((j + nrot) & 3);
        bad[t][j] = lds0 + ABYTES + rho * PITCH + ((cb ^ (rho & 7)) << 5) + p * 8 + t * 65536;
        tie(bad[t][j]);
      }
    }
  }
  f32x4 acc[4][4], csa[2], gra[2], rsa[2];
  const f32x4 z4 = {0.f, 0.f, 0.f, 0.f};
#pragma unroll
  for (int i = 0; i < 4; ++i)
#pragma unroll
    for (int j = 0; j < 4; ++j) acc[i][j] = z4;
  csa[0] = csa[1] = gra[0] = gra[1] = rsa[0] = rsa[1] = z4;
  const s16x8 ones_s = {0x3F80, 0x3F80, 0x3F80, 0x3F80, 0x3F80, 0x3F80, 0x3F80, 0x3F80};
  bf16x8 ones = __builtin_bit_cast(bf16x8, ones_s);
  tie(ones);

  const int fidx = (xcd * 30 + idx) * 4 + cw;
  char* fillp = ws + W_F0 + (size_t)fidx * 8192 + (fidx >= 640 ? 2 * GUARD : fidx >= 320 ? GUARD : 0);
  u32x4 fones = {~0u, ~0u, ~0u, ~0u};
  tie(fones);
  bf16x8 fa[2][4];
  u32x2 fb[2][4][2];
  const char* utp;
  {
    const int u0 = cw * 240 + xcd * 30 + idx, u1 = u0 + 960 < NUNITS - 128 * NSP ? u0 + 960 : u0;
    unsigned off; int N2; bool rem;
    utp = unit_src(lane < 32 ? u0 : u1, (lane >> 3) & 3, wo, w1, w2, w3, w4, off, N2, rem);
    utp += off + (unsigned)(((lane & 7) + ((rem && (lane & 4)) ? 16 : 0)) * N2);
  }
  unsigned utv = 0;
  auto rd = [&](auto SC, auto HC, auto RC) {
    constexpr int s = decltype(SC)::v, h = decltype(HC)::v, r = decltype(RC)::v;
    constexpr int sl = s % NS, t = sl >> 1, so = (sl & 1) * SLOT;
    if constexpr (r % 3 == 0) { constexpr int i = r / 3; dsr16<so + (i & 1) * 2048>(fa[h][i], aad[t][h][i >> 1]); }
    else { constexpr int j = r / 3, e = r % 3 - 1; dstr<so + h * 32 * PITCH + e * 8 * PITCH>(fb[h][j][e], bad[t][j]); }
  };
  int rit = 0;
  auto phase = [&](auto SC, auto HC) {
    constexpr int s = decltype(SC)::v, h = decltype(HC)::v;
    constexpr int ns = h ? s + 1 : s, nh = 1 - h;
    constexpr bool more = ROLL || ns < NST;
    wait_lgkm<0>();
    bf16x8 b[4];
#pragma unroll
    for (int j = 0; j < 4; ++j) {
      tie(fa[h][j]); tie(fb[h][j][0]); tie(fb[h][j][1]);
      b[j] = __builtin_bit_cast(bf16x8, u32x4{fb[h][j][0][0], fb[h][j][0][1], fb[h][j][1][0], fb[h][j][1][1]});
    }
    sfor<0, 22>([&](auto MC) {
      constexpr int m = decltype(MC)::v;
      if constexpr (m < 16) {
        constexpr int i = m >> 2, j = m & 3;
        tie(fa[h][i]);
        acc[i][j] = mfma16(b[j], fa[h][i], acc[i][j]); tie(acc[i][j]);
      } else if constexpr (m < 18) {
        tie(ones);
        csa[m - 16] = mfma16(ones, b[m - 16], csa[m - 16]); tie(csa[m - 16]);
      } else if constexpr (m < 20) {
        tie(ones);
        rsa[m - 18] = mfma16(ones, fa[h][m - 18], rsa[m - 18]); tie(rsa[m - 18]);
      } else {
        tie(fa[h][m - 20]);
        gra[m - 20] = mfma16(fa[h][m - 20], fa[h][m - 20], gra[m - 20]); tie(gra[m - 20]);
      }
      if constexpr (m == 18 && 2 * s + h == UT) asm volatile("global_load_dword %0, %1, off" : "=v"(utv) : "v"(utp) : "memory");
      constexpr int fi = 2 * s + h - FS0;
      if constexpr (FS0 >= 0 && m == 16 && fi >= 0 && fi % FSTEP == 0 && fi / FSTEP < 8) gst16s((unsigned)(fi / FSTEP * 1024 + lane * 16), fones, fillp);
      constexpr int R0 = h ? 2 : 0;
      if constexpr (more && h == 1 && m == 1) bar();
      if constexpr (more && m >= R0 && m < R0 + 12) rd(IC<ns>{}, IC<nh>{}, IC<m - R0>{});
    });
  };
  unsigned cwv;
  CODE_WARM(cwv, Lq_h, Lq_h, Lq_e, Lf_b, (idx * 4 + cw) & 31);
  STAMP(1, 2);
  bar();
  STAMP(1, 3);
  sfor<0, 12>([&](auto RC) { rd(IC<0>{}, IC<0>{}, RC); });
  unsigned ooff[4];
  float sc[4];
#pragma unroll
  for (int j = 0; j < 4; ++j) {
    const int c = c0 + (wn * 4 + ((j + nrot) & 3)) * 16 + 4 * g;
    const int hd = c / 20, quad = (c - hd * 20) >> 2;
    ooff[j] = (unsigned)((hd * 640 + quad) * 64 + (l15 ^ ((quad & 2) << 2)) * 4 + (rt * 8 + wm * 4) * 320) * 2;
    sc[j] = hd < 32 ? 0.22360679774997896f * 1.4426950408889634f : 1.0f;
    tie(ooff[j]); tie(sc[j]);
  }
  sfor<0, NST>([&](auto SC) { phase(SC, IC<0>{}); phase(SC, IC<1>{}); });

  wait_vm<0>(); tie(cwv); tie(utv);
  STAMP(1, 4);
  float* xs = (float*)(lds + XOFF);
  if (g == 0) {
#pragma unroll
    for (int j = 0; j < 2; ++j) {
      xs[(wn * 4 + ((j + nrot) & 3)) * 16 + l15] = csa[j][0];
      xs[128 + (wm * 4 + ((j + mrot) & 3)) * 16 + l15] = rsa[j][0];
    }
  }
  if (g == q) {
#pragma unroll
    for (int i = 0; i < 2; ++i)
      xs[256 + (wm * 4 + ((i + mrot) & 3)) * 16 + l15] = p == 0 ? gra[i][0] : p == 1 ? gra[i][1] : p == 2 ? gra[i][2] : gra[i][3];
  }
  lgkm_bar();
  STAMP(1, 5);
  {
    constexpr float INVK = 1.0f / 640;
    f32x4 cs[4];
    float r1[4], r2s[4];
#pragma unroll
    for (int i = 0; i < 4; ++i) {
      const int rl = (wm * 4 + ((i + mrot) & 3)) * 16 + l15;
      r1[i] = xs[128 + rl]; r2s[i] = xs[256 + rl];
    }
#pragma unroll
    for (int j = 0; j < 4; ++j) cs[j] = *(const f32x4*)(xs + (wn * 4 + ((j + nrot) & 3)) * 16 + 4 * g);
#pragma unroll
    for (int i = 0; i < 4; ++i) {
      const float mean = r1[i] * INVK;
      const float var = fmaxf(r2s[i] * INVK - mean * mean, 0.f);
      const float rs = __builtin_amdgcn_rsqf(var + LN_EPS);
      const char* dst = ws + W_QKV + __builtin_amdgcn_readfirstlane(((i + mrot) & 3) * 640);
#pragma unroll
      for (int j = 0; j < 4; ++j) {
        const float r2 = rs * sc[j];
        u32x2 o;
        o[0] = pack2((acc[i][j][0] - mean * cs[j][0]) * r2, (acc[i][j][1] - mean * cs[j][1]) * r2);
        o[1] = pack2((acc[i][j][2] - mean * cs[j][2]) * r2, (acc[i][j][3] - mean * cs[j][3]) * r2);
        asm volatile("global_store_dwordx2 %0, %1, %2 sc1" : : "v"(ooff[j]), "v"(o), "s"(dst) : "memory");
      }
    }
  }
  STAMP(1, 6);
  STAMP(1, 7);
  STAMP_FLUSH(1);
  CODE_MARK(Lq_e);
}

constexpr int LSPIN = 1 << 20;
DEVI bool flag_ok(u32x4 f, unsigned tag) {
  const unsigned a = f[0] < f[1] ? f[0] : f[1], b = f[2] < f[3] ? f[2] : f[3];
  return __builtin_amdgcn_readfirstlane(a < b ? a : b) >= tag;
}
DEVI void lds_wait4(unsigned addr, unsigned tag) {
  for (int i = 0; i < LSPIN; ++i) {
    u32x4 f;
    asm volatile("ds_read_b128 %0, %1\n\ts_waitcnt lgkmcnt(0)" : "=v"(f) : "v"(addr) : "memory");
    if (flag_ok(f, tag)) break;
  }
}
DEVI void lds_wait1(unsigned addr, unsigned tag) {
  for (int i = 0; i < LSPIN; ++i) {
    unsigned f;
    asm volatile("ds_read_b32 %0, %1\n\ts_waitcnt lgkmcnt(0)" : "=v"(f) : "v"(addr) : "memory");
    if (__builtin_amdgcn_readfirstlane(f) >= tag) break;
  }
}
DEVI void lds_done(unsigned addr) {
  asm volatile("s_waitcnt lgkmcnt(0)\n\ts_mov_b64 exec, 1\n\tds_add_u32 %0, %1\n\ts_mov_b64 exec, -1" : : "v"(addr), "v"(1u) : "memory");
}
template <bool PAIR, bool ST> DEVI void item_read(unsigned fa, unsigned tag, unsigned da, unsigned sa, float* f, float& s1, float& s2) {
  f32x4 d[8], s[2];
  for (int it = 0; it < 2; ++it) {
    u32x4 fl;
    asm volatile("ds_read_b128 %0, %1" : "=v"(fl) : "v"(fa) : "memory");
    if constexpr (ST) { dsr16<0>(s[0], sa); dsr16<16>(s[1], sa); }
    dsr16<0>(d[0], da); dsr16<1024>(d[1], da); dsr16<2048>(d[2], da); dsr16<3072>(d[3], da);
    if constexpr (PAIR) { dsr16<256>(d[4], da); dsr16<1280>(d[5], da); dsr16<2304>(d[6], da); dsr16<3328>(d[7], da); }
    wait_lgkm<0>();
    tie(fl);
    if (flag_ok(fl, tag)) break;
    lds_wait4(fa, tag);
  }
#pragma unroll
  for (int i = 0; i < (PAIR ? 8 : 4); ++i) tie(d[i]);
  if constexpr (ST) {
    tie(s[0]); tie(s[1]);
    s1 = (s[0][0] + s[0][2]) + (s[1][0] + s[1][2]);
    s2 = (s[0][1] + s[0][3]) + (s[1][1] + s[1][3]);
  }
  const f32x4 a = (d[0] + d[1]) + (d[2] + d[3]);
#pragma unroll
  for (int c = 0; c < 4; ++c) f[c] = a[c];
  if constexpr (PAIR) {
    const f32x4 b = (d[4] + d[5]) + (d[6] + d[7]);
#pragma unroll
    for (int c = 0; c < 4; ++c) f[4 + c] = b[c];
  }
}
DEVI unsigned nosen(unsigned u) { return u < 0xfffffffeu ? u : 0xfffffffeu; }
DEVI u32x4 widen(u32x2 o) {
  const auto a = __builtin_amdgcn_permlane16_swap(o[0], o[0], false, false);
  const auto b = __builtin_amdgcn_permlane16_swap(o[1], o[1], false, false);
  return u32x4{a[0], b[0], a[1], b[1]};
}
DEVI float gmax(float v) {
  const unsigned u = __builtin_bit_cast(unsigned, v);
  const auto a = __builtin_amdgcn_permlane32_swap(u, u, false, false);
  const float s = fmaxf(bfhi32(a[0]), bfhi32(a[1]));
  const unsigned t = __builtin_bit_cast(unsigned, s);
  const auto b = __builtin_amdgcn_permlane16_swap(t, t, false, false);
  return fmaxf(bfhi32(b[0]), bfhi32(b[1]));
}
template <int OFF> DEVI void dsr8x2(u32x2& d0, u32x2& d1, unsigned a0, unsigned a1) {
  asm volatile("ds_read_b64 %0, %2 offset:%4\n\tds_read_b64 %1, %3 offset:%4" : "=&v"(d0), "=&v"(d1) : "v"(a0), "v"(a1), "i"(OFF) : "memory");
}
DEVI float max4(float m, f32x4 s) { return fmaxf(fmaxf(fmaxf(m, s[0]), s[1]), fmaxf(s[2], s[3])); }
DEVI u32x2 exp_pack(f32x4 s) {
  return u32x2{pack2(__builtin_amdgcn_exp2f(s[0]), __builtin_amdgcn_exp2f(s[1])),
               pack2(__builtin_amdgcn_exp2f(s[2]), __builtin_amdgcn_exp2f(s[3]))};
}
DEVI u32x2 pack4s(f32x4 o, float d) { return u32x2{nosen(pack2(o[0] * d, o[1] * d)), nosen(pack2(o[2] * d, o[3] * d))}; }

#define P1ASM 1
#define EPRIO 2
#define PREPOS 0
#define PRET 5
#define PF0 1
#define FILLA 0
#define NPF 1
#define PT2 9
#define PT3 9
#define PT4 9
#define PT5 9
#define PT6 9
#define PFA 9
#define PFB 9
#define TOUCH_AT(n) do { if (PFA == n) touch2(NPF); if (PFB == n) touch2(NPF + 1); if (NPF <= 2 && PT2 == n) touch(2); if (PT3 == n) touch(3); if (PT4 == n) touch(4); if (PT5 == n) touch(5); if (PT6 == n) touch(6); } while (0)
constexpr int NSLOT = 36;
constexpr int L_CS = NSLOT * 4096;
constexpr int L_ST = L_CS + 1152;
constexpr int L_IF = L_ST + 2048;
constexpr int L_DN = L_IF + 192;
constexpr int L_END = L_DN + 64;
constexpr int NFLW = (L_END - L_IF) / 4;
constexpr int A_V = 51200;
constexpr int A_C = 2 * A_V + 128;

#define MST(i)
#define MST_FLUSH()

__global__ __launch_bounds__(512, 2) void fused_k(char* __restrict__ ws, const u16* __restrict__ X, u16* __restrict__ OUT) {
  __shared__ __attribute__((aligned(16))) char lds[L_END];
  CODE_MARK(Lf_b);
  const int tid = threadIdx.x, lane = tid & 63;
  const int wave = __builtin_amdgcn_readfirstlane(tid >> 6);
  const int bid = blockIdx.x, xcd = bid & 7, idx = bid >> 3;
  const int rt = xcd * 4 + (idx >> 3), ct = idx & 7;
  const unsigned lds0 = (unsigned)(size_t)(__attribute__((address_space(3))) char*)lds;
  const int l15 = lane & 15, g = lane >> 4;
  MST(0);
  STAMP_DECL; STAMP(2, 0); STAMP(2, 1);
  const int e = wave - 4;
  u32x4 xa, xb;
  u32x2 xc;
  unsigned pfv = 0;
  typedef unsigned u32x16 __attribute__((ext_vector_type(16)));
  const char* bB = ws + W_PK + (ct * 4 + (wave & 3)) * 25600;
  constexpr int NP1 = 3;
  u32x16 pre0;
  u32x16 pre1;
  u32x16 pre2;
  u32x16 pre3;
  u32x16 pre4;
  u32x16 pre5;
  auto touch = [&](int i) {
    const unsigned pfo = (unsigned)((idx >> 3) * 128 + lane * 512 + (i >= 3 && !(i & 1) ? 32768 : 0));
    const unsigned lim = i < 3 ? 199 * 128 : 479 * 128;
    const char* b = ws + W_PK + (i < 3 ? i * (DM * DM * 2) + (ct * 4 + (wave & 3)) * 25600
                                       : 3 * (DM * DM * 2) + (i >= 5 ? DM * FF * 2 : 0) + (ct * 4 + (wave & 3)) * 61440);
    asm volatile("global_load_dword %0, %1, %2" : "+v"(pfv) : "v"(pfo < lim ? pfo : lim), "s"(b) : "memory");
  };
  auto touch2 = [&](int i) {
    asm volatile("global_load_dword %0, %1, %2" : "+v"(pfv) : "v"((unsigned)(PF0 * 6400 + i * 8192 + (idx * 4 + (wave & 3)) * 64 + lane) * 128u), "s"(ws + W_PK) : "memory");
  };
  auto load_x = [&]() {
    const char* xp = (const char*)X + (size_t)(rt * 64) * (DM * 2) + ct * 160;
    asm volatile("global_load_dwordx4 %0, %1, %2" : "=v"(xa) : "v"((unsigned)((e * 16 + l15) * (DM * 2) + 16 * g)), "s"(xp) : "memory");
    asm volatile("global_load_dwordx4 %0, %1, %2" : "=v"(xb) : "v"((unsigned)((((e - 1) & 3) * 16 + l15) * (DM * 2) + 64 + 16 * g)), "s"(xp) : "memory");
    gld8a(xc, (unsigned)((((e - 2) & 3) * 16 + l15) * (DM * 2) + 128 + 8 * g), xp);
  };
  const u32x4 ones = {~0u, ~0u, ~0u, ~0u};
  auto fill_g = [&](int c) {
#pragma unroll
    for (int q = 0; q < 2; ++q)
      gst16s((unsigned)((rt * 4 + (q ? 2 + (e >> 1) : 1 - (e >> 1))) * (FF * 32) + (ct * 24 + c * 8 + (e & 1) * 4) * 256 + lane * 16), ones, ws + W_G);
  };
  auto fill_x1 = [&]() {
#pragma unroll
    for (int t = 0; t < 3; ++t)
      gst16s((unsigned)((rt * 4 + ((e - t) & 3)) * (DM * 32) + (ct * 10 + 4 * t) * 256 + (t < 2 ? lane : lane & 31) * 16), ones, ws + W_X1);
  };

  {
    const int hl = wave & 3, half = wave >> 2;
    const int head = ct * 4 + hl, qb0 = rt * 4 + half * 2;
    const int rsw = l15 ^ ((g & 2) << 2);
    u32x2 qa0, qa1, qb0v, qb1v;
    unsigned cwv = 0;
    {
      const char* qp = ws + W_QKV + (size_t)(head * 128 + qb0) * 640;
      gld8a(qa0, (unsigned)(g * 128 + rsw * 8), qp);
      gld8a(qa1, (unsigned)(512 + l15 * 8), qp);
      gld8a(qb0v, (unsigned)(640 + g * 128 + rsw * 8), qp);
      gld8a(qb1v, (unsigned)(640 + 512 + l15 * 8), qp);
      const char* kb = ws + W_QKV + (size_t)(32 + ct * 4) * 81920;
      const int back = (rt * 4 - 16) * 640;
      unsigned vo[7];
#pragma unroll
      for (int i = 0; i < 7; ++i) {
        const unsigned o = (unsigned)((wave + 8 * i) * 1024 + lane * 16);
        const unsigned hd = (o * 5243u) >> 26;
        const int r = (int)(o - hd * 12800u) + back;
        vo[i] = hd * 81920u + (unsigned)(r > 0 ? r : 0);
        if (i < 6 || wave < 2) glds16a(vo[i], kb, lds0 + (wave + 8 * i) * 1024);
      }
#pragma unroll
      for (int i = 0; i < 7; ++i)
        if (i < 6 || wave < 2) glds16a(vo[i], kb + (size_t)32 * 81920, lds0 + A_V + (wave + 8 * i) * 1024);
      if (wave == 7) CODE_WARM(cwv, Lf_h, Lf_h, Lf_e, Lf_e2, idx);
    }
    if (tid < 20) {
      *(u32x4*)(lds + A_C + tid * 640) = u32x4{0x3F80u, 0u, 0u, 0u};
      *(u32x2*)(lds + A_C + tid * 640 + 16) = u32x2{(rt * 4 - 16 + tid < 0) ? 0x3F80u : 0u, 0u};
    }
    if (tid >= 64 && tid < 64 + NFLW) *(unsigned*)(lds + L_IF + (tid - 64) * 4) = 0u;
    const float NEG = -__builtin_inff();
    f32x4 cm0, cm16;
#pragma unroll
    for (int r = 0; r < 4; ++r) {
      const bool gt = (4 * g + r) > l15;
      cm0[r] = gt ? 0.f : NEG;
      cm16[r] = gt ? NEG : 0.f;
    }
    if (wave < 2 || wave == 7) wait_vm<7>(); else wait_vm<6>();
    lgkm_bar();
    STAMP(2, 2);
    asm volatile("s_cmp_gt_u32 %7, 3\n\ts_cbranch_scc1 Lpre%=\n\tglobal_load_dwordx4 v[236:239], %6, %3 offset:0\n\tglobal_load_dwordx4 v[240:243], %6, %3 offset:1024\n\tglobal_load_dwordx4 v[244:247], %6, %3 offset:2048\n\tglobal_load_dwordx4 v[248:251], %6, %3 offset:3072\n\tglobal_load_dwordx4 v[220:223], %6, %4 offset:0\n\tglobal_load_dwordx4 v[224:227], %6, %4 offset:1024\n\tglobal_load_dwordx4 v[228:231], %6, %4 offset:2048\n\tglobal_load_dwordx4 v[232:235], %6, %4 offset:3072\n\tglobal_load_dwordx4 v[204:207], %6, %5 offset:0\n\tglobal_load_dwordx4 v[208:211], %6, %5 offset:1024\n\tglobal_load_dwordx4 v[212:215], %6, %5 offset:2048\n\tglobal_load_dwordx4 v[216:219], %6, %5 offset:3072\n\tLpre%=:"
 : "={v[236:251]}"(pre0), "={v[220:235]}"(pre1), "={v[204:219]}"(pre2)
 : "s"(bB + 0), "s"(bB + 4096), "s"(bB + 8192), "v"(lane * 16), "s"(wave) : "memory", "scc");
    if (wave < 4) {
#pragma unroll
      for (int i = 0; i < NPF; ++i)
        asm volatile("global_load_dword %0, %1, %2" : "+v"(pfv) : "v"((unsigned)(PF0 * 6400 + i * 8192 + (idx * 4 + wave) * 64 + lane) * 128u), "s"(ws + W_PK) : "memory");
    }
    tie(qa0); tie(qa1); tie(qb0v); tie(qb1v);
    if (g != 0) { qa1 = u32x2{g == 1 ? 0xFF00u : 0u, 0u}; qb1v = qa1; }
    const bf16x8 qA = __builtin_bit_cast(bf16x8, u32x4{qa0[0], qa0[1], qa1[0], qa1[1]});
    const bf16x8 qB = __builtin_bit_cast(bf16x8, u32x4{qb0v[0], qb0v[1], qb1v[0], qb1v[1]});

    bf16x8 kf[18];
    float mA = NEG, mB = NEG;
    const int kvo = hl * 12800 + half * 1280;
    {
      const unsigned ka0 = lds0 + kvo + g * 128 + rsw * 8;
      const unsigned ka1 = lds0 + (g == 1 ? A_C + half * 1280 + 16 : kvo + 512 + l15 * 8);
      const f32x4 z = {0.f, 0.f, 0.f, 0.f};
      u32x2 k0[18], k1[18];
      f32x4 sA[3], sB[3];
      auto rdk = [&](auto IC_) {
        constexpr int i = decltype(IC_)::v;
        dsr8x2<i * 640>(k0[i], k1[i], ka0, ka1);
      };
      auto mx = [&](float& m, f32x4& v) {
        asm volatile("v_max3_f32 %0, %0, %1, %2\n\tv_max3_f32 %0, %0, %3, %4" : "+v"(m) : "v"(v[0]), "v"(v[1]), "v"(v[2]), "v"(v[3]));
      };
      sfor<0, 6>(rdk);
      sfor<0, 20>([&](auto IC_) {
        constexpr int i = decltype(IC_)::v;
        if constexpr (i < 18) {
          wait_lgkm<2 * ((i + 5 < 17 ? i + 5 : 17) - i)>();
          tie(k0[i]); tie(k1[i]);
          kf[i] = __builtin_bit_cast(bf16x8, u32x4{k0[i][0], k0[i][1], k1[i][0], k1[i][1]});
          if constexpr (i < 17) { sA[i % 3] = mfma16(kf[i], qA, i == 0 ? cm0 : i == 16 ? cm16 : z); tie(sA[i % 3]); }
          if constexpr (i > 0) { sB[i % 3] = mfma16(kf[i], qB, i == 1 ? cm0 : i == 17 ? cm16 : z); tie(sB[i % 3]); }
          if constexpr (i + 6 < 18) rdk(IC<i + 6>{});
        }
        if constexpr (i == 2 || i == 18) asm volatile("s_nop 7\n\ts_nop 7");
        if constexpr (i >= 2 && i - 2 < 17) mx(mA, sA[(i - 2) % 3]);
        if constexpr (i >= 3) mx(mB, sB[(i - 2) % 3]);
      });
    }
    mA = -gmax(mA); mB = -gmax(mB);
    STAMP(2, 3);
    if (wave >= 4) wait_vm<(FILLA == 1 ? 12 : FILLA == 3 ? 8 : 0)>(); else wait_vm<NPF + (PREPOS != 1 ? 4 * NP1 : 0)>();
    tie(cwv);
    bar();
    STAMP(2, 4);
    if (wave >= 4) {
      load_x();
    }
    const f32x4 cA = {mA, mA, mA, mA}, cB = {mB, mB, mB, mB};
    const f32x4 cA0 = cm0 + cA, cA16 = cm16 + cA, cB0 = cm0 + cB, cB16 = cm16 + cB;

    f32x4 oA0 = {0.f, 0.f, 0.f, 0.f}, oA1 = oA0, oB0 = oA0, oB1 = oA0;
    {
      const int q = l15 >> 2, p = l15 & 3;
      const char* vp0 = lds + A_V + kvo + p * 128 + (((4 * g + q) ^ ((p & 2) << 2)) << 3);
      const char* vp1 = lds + (p == 0 ? A_V + kvo + 512 + (4 * g + q) * 8 : A_C + (p == 1 ? 0 : 8));
#pragma unroll
      for (int t = 0; t < 9; ++t) {
        if (t == PRET) { asm volatile("s_cmp_gt_u32 %7, 3\n\ts_cbranch_scc1 Lpre%=\n\tglobal_load_dwordx4 v[188:191], %6, %3 offset:0\n\tglobal_load_dwordx4 v[192:195], %6, %3 offset:1024\n\tglobal_load_dwordx4 v[196:199], %6, %3 offset:2048\n\tglobal_load_dwordx4 v[200:203], %6, %3 offset:3072\n\tglobal_load_dwordx4 v[172:175], %6, %4 offset:0\n\tglobal_load_dwordx4 v[176:179], %6, %4 offset:1024\n\tglobal_load_dwordx4 v[180:183], %6, %4 offset:2048\n\tglobal_load_dwordx4 v[184:187], %6, %4 offset:3072\n\tglobal_load_dwordx4 v[156:159], %6, %5 offset:0\n\tglobal_load_dwordx4 v[160:163], %6, %5 offset:1024\n\tglobal_load_dwordx4 v[164:167], %6, %5 offset:2048\n\tglobal_load_dwordx4 v[168:171], %6, %5 offset:3072\n\tLpre%=:"
 : "={v[188:203]}"(pre3), "={v[172:187]}"(pre4), "={v[156:171]}"(pre5)
 : "s"(bB + 12288), "s"(bB + 16384), "s"(bB + 20480), "v"(lane * 16), "s"(wave) : "memory", "scc"); }
        const bf16x8 v0 = tr_frag(vp0 + t * 1280, vp0 + t * 1280 + 640);
        const bf16x8 v1 = tr_frag(vp1 + t * 1280, vp1 + t * 1280 + 640);
        const u32x2 a0 = exp_pack(mfma16(kf[2 * t], qA, t == 0 ? cA0 : t == 8 ? cA16 : cA));
        const u32x2 a1 = t < 8 ? exp_pack(mfma16(kf[2 * t + 1], qA, cA)) : u32x2{0u, 0u};
        const u32x2 b0 = t > 0 ? exp_pack(mfma16(kf[2 * t], qB, cB)) : u32x2{0u, 0u};
        const u32x2 b1 = exp_pack(mfma16(kf[2 * t + 1], qB, t == 0 ? cB0 : t == 8 ? cB16 : cB));
        const bf16x8 fA = __builtin_bit_cast(bf16x8, u32x4{a0[0], a0[1], a1[0], a1[1]});
        const bf16x8 fB = __builtin_bit_cast(bf16x8, u32x4{b0[0], b0[1], b1[0], b1[1]});
        oA0 = mfma16(v0, fA, oA0); oA1 = mfma16(v1, fA, oA1);
        oB0 = mfma16(v0, fB, oB0); oB1 = mfma16(v1, fB, oB1);
      }
    }
    STAMP(2, 5);
    if (wave < 4) TOUCH_AT(0);
    lds_done(lds0 + L_DN + 20);
    const float dA = __builtin_amdgcn_rcpf(__shfl(oA1[0], l15 + 16));
    const float dB = __builtin_amdgcn_rcpf(__shfl(oB1[0], l15 + 16));
    {
      char* op = ws + W_O + (size_t)qb0 * (DM * 32) + (head >> 3) * 5120;
      const u32x2 r0 = pack4s(oA0, dA), r1 = pack4s(oB0, dB), r2 = pack4s(oA1, dA), r3 = pack4s(oB1, dB);
      const auto s0 = __builtin_amdgcn_permlane16_swap(r0[0], r1[0], false, false);
      const auto s1 = __builtin_amdgcn_permlane16_swap(r0[1], r1[1], false, false);
      const auto s2 = __builtin_amdgcn_permlane16_swap(r2[0], r3[0], false, false);
      const auto s3 = __builtin_amdgcn_permlane16_swap(r2[1], r3[1], false, false);
      if (wave >= 4) { wait_vm<(FILLA ? 9 : 0)>(); tie(xa); tie(xb); tie(xc); }
      gst16s((unsigned)((g & 1) * (DM * 32) + ((head & 7) * 2 + (g >> 1)) * 256 + l15 * 16), u32x4{s0[0], s1[0], s0[1], s1[1]}, op);
      if (g < 2) gst8s((unsigned)(g * (DM * 32) + 4096 + (head & 7) * 128 + l15 * 8), u32x2{s2[0], s3[0]}, op);
    }
    STAMP(2, 6);
    STAMP_FLUSH(2);
  }

  if (wave < 4) {
    const int w = wave;
    const char* bA = ws + (size_t)(rt * 4) * (DM * 32) + w * 5120;
    const char* bG = ws + W_G + (size_t)(rt * 4) * (FF * 32) + w * 12288;
    const char* bF = ws + W_PK + (size_t)3 * (DM * DM * 2) + (ct * 4 + w) * 61440;
    const unsigned xw = lds0 + w * 1024 + lane * 16;
    const int sr = l15 + 16 + (w == 0 ? -1 : w == 1 ? 1 : w == 2 ? -2 : 2);
    unsigned stv;
    asm volatile(
"s_mov_b32 s94, 0x10000\ns_mov_b32 s95, 0x100000\ns_mov_b32 s90, 0xf0000f\ns_mov_b32 s91, 0xf0000f00\ns_mov_b32 s96, 0x22222222\ns_mov_b32 s97, 0x22222222\ns_mov_b32 s98, 0x44444444\n"
"s_mov_b32 s99, 0x44444444\ns_mov_b32 s100, 0x88888888\ns_mov_b32 s101, 0x88888888\ns_add_u32 s88, s80, 0x7000\ns_addc_u32 s89, s81, 0\nglobal_load_dwordx4 v[12:15], v0, s[88:89] offset:-4096\n"
"s_add_u32 s88, s80, 0xc9000\ns_addc_u32 s89, s81, 0\nglobal_load_dwordx4 v[16:19], v0, s[88:89] offset:-4096\nglobal_load_dwordx4 v[20:23], v0, s[88:89] offset:-3072\n"
"global_load_dwordx4 v[24:27], v0, s[88:89] offset:-2048\nglobal_load_dwordx4 v[28:31], v0, s[88:89] offset:-1024\nglobal_load_dwordx4 v[32:35], v0, s[88:89] offset:0\n"
"global_load_dwordx4 v[36:39], v0, s[88:89] offset:1024\nLmc1_%=:\nds_read_b32 v10, v9 offset:20\ns_waitcnt lgkmcnt(0)\nv_cmp_gt_u32 vcc, 5, v10\ns_cbranch_vccz Lmb1_%=\ns_sleep 2\n"
"s_sub_u32 s95, s95, 1\ns_cbranch_scc0 Lmc1_%=\ns_mov_b32 s95, 0\nLmb1_%=:\ns_add_u32 s86, s76, 0x781000\ns_addc_u32 s87, s77, 0\nglobal_load_dwordx4 v[40:43], v0, s[86:87] offset:-4096 sc1\n"
"global_load_dwordx4 v[44:47], v0, s[86:87] offset:-3072 sc1\nglobal_load_dwordx4 v[48:51], v0, s[86:87] offset:-2048 sc1\nglobal_load_dwordx4 v[52:55], v0, s[86:87] offset:-1024 sc1\n"
"global_load_dwordx2 v[56:57], v252, s[86:87] offset:0 sc1\nglobal_load_dwordx2 v[58:59], v252, s[86:87] offset:128 sc1\ns_waitcnt vmcnt(0)\nv_max3_u32 v8, v40, v41, v42\nv_max3_u32 v8, v8, v43, v44\n"
"v_max3_u32 v8, v8, v45, v46\nv_max3_u32 v8, v8, v47, v48\nv_max3_u32 v8, v8, v49, v50\nv_max3_u32 v8, v8, v51, v52\nv_max3_u32 v8, v8, v53, v54\nv_max3_u32 v8, v8, v55, v56\n"
"v_max3_u32 v8, v8, v57, v58\nv_max_u32 v8, v8, v59\nv_cmp_eq_u32 vcc, -1, v8\ns_cbranch_vccnz Lms2_%=\nLmb2_%=:\nv_mfma_f32_16x16x32_bf16 v[60:63], v[236:239], v[40:43], 0\n"
"s_add_u32 s86, s76, 0x786000\ns_addc_u32 s87, s77, 0\nglobal_load_dwordx4 v[64:67], v0, s[86:87] offset:-4096 sc1\nglobal_load_dwordx4 v[68:71], v0, s[86:87] offset:-3072 sc1\n"
"global_load_dwordx4 v[72:75], v0, s[86:87] offset:-2048 sc1\nglobal_load_dwordx4 v[76:79], v0, s[86:87] offset:-1024 sc1\nglobal_load_dwordx2 v[80:81], v252, s[86:87] offset:0 sc1\n"
"global_load_dwordx2 v[82:83], v252, s[86:87] offset:128 sc1\nv_mfma_f32_16x16x32_bf16 v[84:87], v[240:243], v[40:43], 0\nv_mfma_f32_16x16x32_bf16 v[60:63], v[244:247], v[44:47], v[60:63]\n"
"v_mfma_f32_16x16x32_bf16 v[84:87], v[248:251], v[44:47], v[84:87]\nv_mfma_f32_16x16x32_bf16 v[60:63], v[220:223], v[48:51], v[60:63]\nv_mfma_f32_16x16x32_bf16 v[84:87], v[224:227], v[48:51], v[84:87]\n"
"v_mfma_f32_16x16x32_bf16 v[60:63], v[228:231], v[52:55], v[60:63]\nv_mfma_f32_16x16x32_bf16 v[84:87], v[232:235], v[52:55], v[84:87]\nv_mfma_f32_16x16x32_bf16 v[60:63], v[204:207], v[56:59], v[60:63]\n"
"v_mfma_f32_16x16x32_bf16 v[84:87], v[208:211], v[56:59], v[84:87]\nv_mfma_f32_16x16x32_bf16 v[88:91], v[212:215], v[40:43], 0\nv_mfma_f32_16x16x32_bf16 v[92:95], v[216:219], v[40:43], 0\n"
"v_mfma_f32_16x16x32_bf16 v[96:99], v[188:191], v[40:43], 0\nds_write_b128 v1, v[60:63] offset:0\nv_mfma_f32_16x16x32_bf16 v[88:91], v[192:195], v[44:47], v[88:91]\nv_mov_b32 v5, 1\n"
"ds_write_b128 v1, v[84:87] offset:4096\ns_mov_b64 exec, 1\nds_write_b32 v4, v5 offset:0\ns_mov_b64 exec, -1\nv_mfma_f32_16x16x32_bf16 v[92:95], v[196:199], v[44:47], v[92:95]\n"
"v_mfma_f32_16x16x32_bf16 v[96:99], v[200:203], v[44:47], v[96:99]\nv_mfma_f32_16x16x32_bf16 v[88:91], v[172:175], v[48:51], v[88:91]\nv_mfma_f32_16x16x32_bf16 v[92:95], v[176:179], v[48:51], v[92:95]\n"
"v_mfma_f32_16x16x32_bf16 v[96:99], v[180:183], v[48:51], v[96:99]\nv_mfma_f32_16x16x32_bf16 v[88:91], v[184:187], v[52:55], v[88:91]\nv_mfma_f32_16x16x32_bf16 v[92:95], v[156:159], v[52:55], v[92:95]\n"
"v_mfma_f32_16x16x32_bf16 v[96:99], v[160:163], v[52:55], v[96:99]\nv_mfma_f32_16x16x32_bf16 v[88:91], v[164:167], v[56:59], v[88:91]\nv_mfma_f32_16x16x32_bf16 v[92:95], v[168:171], v[56:59], v[92:95]\n"
"v_mfma_f32_16x16x32_bf16 v[96:99], v[12:15], v[56:59], v[96:99]\ns_nop 2\nds_write_b128 v1, v[88:91] offset:8192\ns_nop 3\nds_write_b128 v1, v[92:95] offset:12288\ns_mov_b64 exec, 1\n"
"ds_write_b32 v4, v5 offset:16\ns_mov_b64 exec, -1\ns_nop 1\nds_write_b128 v1, v[96:99] offset:16384\ns_mov_b64 exec, 1\nds_write_b32 v4, v5 offset:32\ns_mov_b64 exec, -1\n"
"global_load_dwordx4 v[100:103], v0, s[88:89] offset:2048\nglobal_load_dwordx4 v[104:107], v0, s[88:89] offset:3072\ns_add_u32 s88, s80, 0xcb000\ns_addc_u32 s89, s81, 0\n"
"global_load_dwordx4 v[108:111], v0, s[88:89] offset:-4096\ns_waitcnt vmcnt(8)\nglobal_load_dwordx4 v[112:115], v0, s[88:89] offset:-3072\ns_waitcnt vmcnt(8)\n"
"global_load_dwordx4 v[116:119], v0, s[88:89] offset:-2048\ns_waitcnt vmcnt(5)\nv_max3_u32 v8, v64, v65, v66\nv_max3_u32 v8, v8, v67, v68\nv_max3_u32 v8, v8, v69, v70\nv_max3_u32 v8, v8, v71, v72\n"
"v_max3_u32 v8, v8, v73, v74\nv_max3_u32 v8, v8, v75, v76\nv_max3_u32 v8, v8, v77, v78\nv_max3_u32 v8, v8, v79, v80\nv_max3_u32 v8, v8, v81, v82\nv_max_u32 v8, v8, v83\nv_cmp_eq_u32 vcc, -1, v8\n"
"s_cbranch_vccnz Lms3_%=\nLmb3_%=:\nv_mfma_f32_16x16x32_bf16 v[120:123], v[236:239], v[64:67], 0\nv_mfma_f32_16x16x32_bf16 v[124:127], v[240:243], v[64:67], 0\n"
"v_mfma_f32_16x16x32_bf16 v[120:123], v[244:247], v[68:71], v[120:123]\nv_mfma_f32_16x16x32_bf16 v[124:127], v[248:251], v[68:71], v[124:127]\n"
"v_mfma_f32_16x16x32_bf16 v[120:123], v[220:223], v[72:75], v[120:123]\nv_mfma_f32_16x16x32_bf16 v[124:127], v[224:227], v[72:75], v[124:127]\n"
"v_mfma_f32_16x16x32_bf16 v[120:123], v[228:231], v[76:79], v[120:123]\nv_mfma_f32_16x16x32_bf16 v[124:127], v[232:235], v[76:79], v[124:127]\n"
"v_mfma_f32_16x16x32_bf16 v[120:123], v[204:207], v[80:83], v[120:123]\nv_mfma_f32_16x16x32_bf16 v[124:127], v[208:211], v[80:83], v[124:127]\n"
"v_mfma_f32_16x16x32_bf16 v[128:131], v[212:215], v[64:67], 0\nv_mfma_f32_16x16x32_bf16 v[132:135], v[216:219], v[64:67], 0\nv_mfma_f32_16x16x32_bf16 v[136:139], v[188:191], v[64:67], 0\n"
"ds_write_b128 v1, v[120:123] offset:20480\nv_mfma_f32_16x16x32_bf16 v[128:131], v[192:195], v[68:71], v[128:131]\nds_write_b128 v1, v[124:127] offset:24576\ns_mov_b64 exec, 1\n"
"ds_write_b32 v4, v5 offset:48\ns_mov_b64 exec, -1\nv_mfma_f32_16x16x32_bf16 v[132:135], v[196:199], v[68:71], v[132:135]\nv_mfma_f32_16x16x32_bf16 v[136:139], v[200:203], v[68:71], v[136:139]\n"
"v_mfma_f32_16x16x32_bf16 v[128:131], v[172:175], v[72:75], v[128:131]\nv_mfma_f32_16x16x32_bf16 v[132:135], v[176:179], v[72:75], v[132:135]\n"
"v_mfma_f32_16x16x32_bf16 v[136:139], v[180:183], v[72:75], v[136:139]\nv_mfma_f32_16x16x32_bf16 v[128:131], v[184:187], v[76:79], v[128:131]\n"
"v_mfma_f32_16x16x32_bf16 v[132:135], v[156:159], v[76:79], v[132:135]\nv_mfma_f32_16x16x32_bf16 v[136:139], v[160:163], v[76:79], v[136:139]\n"
"v_mfma_f32_16x16x32_bf16 v[128:131], v[164:167], v[80:83], v[128:131]\nv_mfma_f32_16x16x32_bf16 v[132:135], v[168:171], v[80:83], v[132:135]\n"
"v_mfma_f32_16x16x32_bf16 v[136:139], v[12:15], v[80:83], v[136:139]\ns_nop 2\nds_write_b128 v1, v[128:131] offset:28672\ns_nop 3\nds_write_b128 v1, v[132:135] offset:32768\ns_mov_b64 exec, 1\n"
"ds_write_b32 v4, v5 offset:64\ns_mov_b64 exec, -1\ns_nop 1\nds_write_b128 v1, v[136:139] offset:36864\ns_mov_b64 exec, 1\nds_write_b32 v4, v5 offset:80\ns_mov_b64 exec, -1\n"
"s_add_u32 s86, s76, 0x78b000\ns_addc_u32 s87, s77, 0\nglobal_load_dwordx4 v[140:143], v0, s[86:87] offset:-4096 sc1\nglobal_load_dwordx4 v[144:147], v0, s[86:87] offset:-3072 sc1\n"
"global_load_dwordx4 v[148:151], v0, s[86:87] offset:-2048 sc1\nglobal_load_dwordx4 v[152:155], v0, s[86:87] offset:-1024 sc1\nglobal_load_dwordx2 v[40:41], v252, s[86:87] offset:0 sc1\n"
"global_load_dwordx2 v[42:43], v252, s[86:87] offset:128 sc1\ns_waitcnt vmcnt(8)\nglobal_load_dwordx4 v[60:63], v0, s[88:89] offset:-1024\ns_waitcnt vmcnt(8)\n"
"global_load_dwordx4 v[84:87], v0, s[88:89] offset:0\ns_waitcnt vmcnt(8)\nglobal_load_dwordx4 v[44:47], v0, s[88:89] offset:1024\ns_waitcnt vmcnt(8)\n"
"global_load_dwordx4 v[48:51], v0, s[88:89] offset:2048\ns_waitcnt vmcnt(8)\nglobal_load_dwordx4 v[52:55], v0, s[88:89] offset:3072\ns_waitcnt vmcnt(5)\nv_max3_u32 v8, v140, v141, v142\n"
"v_max3_u32 v8, v8, v143, v144\nv_max3_u32 v8, v8, v145, v146\nv_max3_u32 v8, v8, v147, v148\nv_max3_u32 v8, v8, v149, v150\nv_max3_u32 v8, v8, v151, v152\nv_max3_u32 v8, v8, v153, v154\n"
"v_max3_u32 v8, v8, v155, v40\nv_max3_u32 v8, v8, v41, v42\nv_max_u32 v8, v8, v43\nv_cmp_eq_u32 vcc, -1, v8\ns_cbranch_vccnz Lms4_%=\nLmb4_%=:\n"
"v_mfma_f32_16x16x32_bf16 v[56:59], v[236:239], v[140:143], 0\nv_mfma_f32_16x16x32_bf16 v[88:91], v[240:243], v[140:143], 0\nv_mfma_f32_16x16x32_bf16 v[56:59], v[244:247], v[144:147], v[56:59]\n"
"v_mfma_f32_16x16x32_bf16 v[88:91], v[248:251], v[144:147], v[88:91]\nv_mfma_f32_16x16x32_bf16 v[56:59], v[220:223], v[148:151], v[56:59]\n"
"v_mfma_f32_16x16x32_bf16 v[88:91], v[224:227], v[148:151], v[88:91]\nv_mfma_f32_16x16x32_bf16 v[56:59], v[228:231], v[152:155], v[56:59]\n"
"v_mfma_f32_16x16x32_bf16 v[88:91], v[232:235], v[152:155], v[88:91]\nv_mfma_f32_16x16x32_bf16 v[56:59], v[204:207], v[40:43], v[56:59]\n"
"v_mfma_f32_16x16x32_bf16 v[88:91], v[208:211], v[40:43], v[88:91]\nv_mfma_f32_16x16x32_bf16 v[92:95], v[212:215], v[140:143], 0\nv_mfma_f32_16x16x32_bf16 v[96:99], v[216:219], v[140:143], 0\n"
"v_mfma_f32_16x16x32_bf16 v[64:67], v[188:191], v[140:143], 0\nds_write_b128 v1, v[56:59] offset:40960\nv_mfma_f32_16x16x32_bf16 v[92:95], v[192:195], v[144:147], v[92:95]\n"
"ds_write_b128 v1, v[88:91] offset:45056\ns_mov_b64 exec, 1\nds_write_b32 v4, v5 offset:96\ns_mov_b64 exec, -1\nv_mfma_f32_16x16x32_bf16 v[96:99], v[196:199], v[144:147], v[96:99]\n"
"v_mfma_f32_16x16x32_bf16 v[64:67], v[200:203], v[144:147], v[64:67]\nv_mfma_f32_16x16x32_bf16 v[92:95], v[172:175], v[148:151], v[92:95]\n"
"v_mfma_f32_16x16x32_bf16 v[96:99], v[176:179], v[148:151], v[96:99]\nv_mfma_f32_16x16x32_bf16 v[64:67], v[180:183], v[148:151], v[64:67]\n"
"v_mfma_f32_16x16x32_bf16 v[92:95], v[184:187], v[152:155], v[92:95]\nv_mfma_f32_16x16x32_bf16 v[96:99], v[156:159], v[152:155], v[96:99]\n"
"v_mfma_f32_16x16x32_bf16 v[64:67], v[160:163], v[152:155], v[64:67]\nds_read_b32 v10, v9 offset:20\nv_mfma_f32_16x16x32_bf16 v[92:95], v[164:167], v[40:43], v[92:95]\n"
"v_mfma_f32_16x16x32_bf16 v[96:99], v[168:171], v[40:43], v[96:99]\nv_mfma_f32_16x16x32_bf16 v[64:67], v[12:15], v[40:43], v[64:67]\ns_waitcnt lgkmcnt(0)\nv_cmp_gt_u32 vcc, 8, v10\n"
"s_cbranch_vccnz Lms5_%=\nLmb5_%=:\nds_write_b128 v1, v[92:95] offset:49152\ns_nop 0\nds_write_b128 v1, v[96:99] offset:53248\ns_mov_b64 exec, 1\nds_write_b32 v4, v5 offset:112\ns_mov_b64 exec, -1\n"
"ds_write_b128 v1, v[64:67] offset:57344\ns_mov_b64 exec, 1\nds_write_b32 v4, v5 offset:128\ns_mov_b64 exec, -1\ns_add_u32 s86, s76, 0x790000\ns_addc_u32 s87, s77, 0\n"
"global_load_dwordx4 v[120:123], v0, s[86:87] offset:-4096 sc1\nglobal_load_dwordx4 v[124:127], v0, s[86:87] offset:-3072 sc1\nglobal_load_dwordx4 v[68:71], v0, s[86:87] offset:-2048 sc1\n"
"global_load_dwordx4 v[72:75], v0, s[86:87] offset:-1024 sc1\nglobal_load_dwordx2 v[76:77], v252, s[86:87] offset:0 sc1\nglobal_load_dwordx2 v[78:79], v252, s[86:87] offset:128 sc1\ns_waitcnt vmcnt(8)\n"
"s_add_u32 s88, s80, 0xcd000\ns_addc_u32 s89, s81, 0\nglobal_load_dwordx4 v[80:83], v0, s[88:89] offset:-4096\ns_waitcnt vmcnt(8)\nglobal_load_dwordx4 v[128:131], v0, s[88:89] offset:-3072\n"
"s_waitcnt vmcnt(8)\nglobal_load_dwordx4 v[132:135], v0, s[88:89] offset:-2048\ns_waitcnt vmcnt(8)\nglobal_load_dwordx4 v[136:139], v0, s[88:89] offset:-1024\ns_waitcnt vmcnt(8)\n"
"global_load_dwordx4 v[140:143], v0, s[88:89] offset:0\ns_waitcnt vmcnt(5)\nv_max3_u32 v8, v120, v121, v122\nv_max3_u32 v8, v8, v123, v124\nv_max3_u32 v8, v8, v125, v126\nv_max3_u32 v8, v8, v127, v68\n"
"v_max3_u32 v8, v8, v69, v70\nv_max3_u32 v8, v8, v71, v72\nv_max3_u32 v8, v8, v73, v74\nv_max3_u32 v8, v8, v75, v76\nv_max3_u32 v8, v8, v77, v78\nv_max_u32 v8, v8, v79\nv_cmp_eq_u32 vcc, -1, v8\n"
"s_cbranch_vccnz Lms6_%=\nLmb6_%=:\nv_mfma_f32_16x16x32_bf16 v[56:59], v[236:239], v[120:123], 0\nv_mfma_f32_16x16x32_bf16 v[88:91], v[240:243], v[120:123], 0\n"
"v_mfma_f32_16x16x32_bf16 v[56:59], v[244:247], v[124:127], v[56:59]\nv_mfma_f32_16x16x32_bf16 v[88:91], v[248:251], v[124:127], v[88:91]\ns_add_u32 s86, s76, 0xc8b000\ns_addc_u32 s87, s77, 0\n"
"global_load_dwordx4 v[144:147], v0, s[86:87] offset:-4096 sc1\nglobal_load_dwordx4 v[148:151], v0, s[86:87] offset:-3072 sc1\nglobal_load_dwordx4 v[152:155], v0, s[86:87] offset:-2048 sc1\n"
"global_load_dwordx4 v[40:43], v0, s[86:87] offset:-1024 sc1\nglobal_load_dwordx4 v[92:95], v0, s[86:87] offset:0 sc1\nv_mfma_f32_16x16x32_bf16 v[56:59], v[220:223], v[68:71], v[56:59]\n"
"v_mfma_f32_16x16x32_bf16 v[88:91], v[224:227], v[68:71], v[88:91]\nv_mfma_f32_16x16x32_bf16 v[56:59], v[228:231], v[72:75], v[56:59]\nv_mfma_f32_16x16x32_bf16 v[88:91], v[232:235], v[72:75], v[88:91]\n"
"v_mfma_f32_16x16x32_bf16 v[56:59], v[204:207], v[76:79], v[56:59]\nv_mfma_f32_16x16x32_bf16 v[88:91], v[208:211], v[76:79], v[88:91]\nv_mfma_f32_16x16x32_bf16 v[96:99], v[212:215], v[120:123], 0\n"
"v_mfma_f32_16x16x32_bf16 v[64:67], v[216:219], v[120:123], 0\nv_mfma_f32_16x16x32_bf16 v[236:239], v[188:191], v[120:123], 0\nds_write_b128 v1, v[56:59] offset:61440\n"
"v_mfma_f32_16x16x32_bf16 v[96:99], v[192:195], v[124:127], v[96:99]\nds_write_b128 v2, v[88:91] offset:0\ns_mov_b64 exec, 1\nds_write_b32 v4, v5 offset:144\ns_mov_b64 exec, -1\n"
"v_mfma_f32_16x16x32_bf16 v[64:67], v[196:199], v[124:127], v[64:67]\nv_mfma_f32_16x16x32_bf16 v[236:239], v[200:203], v[124:127], v[236:239]\n"
"v_mfma_f32_16x16x32_bf16 v[96:99], v[172:175], v[68:71], v[96:99]\nv_mfma_f32_16x16x32_bf16 v[64:67], v[176:179], v[68:71], v[64:67]\n"
"v_mfma_f32_16x16x32_bf16 v[236:239], v[180:183], v[68:71], v[236:239]\nv_mfma_f32_16x16x32_bf16 v[96:99], v[184:187], v[72:75], v[96:99]\n"
"v_mfma_f32_16x16x32_bf16 v[64:67], v[156:159], v[72:75], v[64:67]\nv_mfma_f32_16x16x32_bf16 v[236:239], v[160:163], v[72:75], v[236:239]\n"
"v_mfma_f32_16x16x32_bf16 v[96:99], v[164:167], v[76:79], v[96:99]\nv_mfma_f32_16x16x32_bf16 v[64:67], v[168:171], v[76:79], v[64:67]\n"
"v_mfma_f32_16x16x32_bf16 v[236:239], v[12:15], v[76:79], v[236:239]\ns_nop 2\nds_write_b128 v2, v[96:99] offset:4096\ns_nop 3\nds_write_b128 v2, v[64:67] offset:8192\ns_mov_b64 exec, 1\n"
"ds_write_b32 v4, v5 offset:160\ns_mov_b64 exec, -1\ns_nop 1\nds_write_b128 v2, v[236:239] offset:12288\ns_mov_b64 exec, 1\nds_write_b32 v4, v5 offset:176\ns_mov_b64 exec, -1\ns_waitcnt vmcnt(8)\n"
"global_load_dwordx4 v[240:243], v0, s[88:89] offset:1024\ns_waitcnt vmcnt(8)\nglobal_load_dwordx4 v[244:247], v0, s[88:89] offset:2048\ns_waitcnt vmcnt(8)\n"
"global_load_dwordx4 v[248:251], v0, s[88:89] offset:3072\ns_waitcnt vmcnt(8)\ns_add_u32 s88, s80, 0xcf000\ns_addc_u32 s89, s81, 0\nglobal_load_dwordx4 v[220:223], v0, s[88:89] offset:-4096\n"
"s_waitcnt vmcnt(8)\ns_add_u32 s88, s80, 0x191000\ns_addc_u32 s89, s81, 0\nglobal_load_dwordx4 v[224:227], v0, s[88:89] offset:-4096\ns_waitcnt vmcnt(8)\n"
"global_load_dwordx4 v[228:231], v0, s[88:89] offset:-3072\ns_waitcnt vmcnt(8)\nglobal_load_dwordx4 v[232:235], v0, s[88:89] offset:-2048\ns_waitcnt vmcnt(8)\n"
"global_load_dwordx4 v[204:207], v0, s[88:89] offset:-1024\ns_waitcnt vmcnt(8)\nglobal_load_dwordx4 v[208:211], v0, s[88:89] offset:0\ns_waitcnt vmcnt(8)\n"
"global_load_dwordx4 v[212:215], v0, s[88:89] offset:1024\ns_waitcnt vmcnt(8)\nglobal_load_dwordx4 v[216:219], v0, s[88:89] offset:2048\ns_waitcnt vmcnt(8)\n"
"global_load_dwordx4 v[120:123], v0, s[88:89] offset:3072\ns_waitcnt vmcnt(8)\ns_add_u32 s88, s80, 0x193000\ns_addc_u32 s89, s81, 0\nglobal_load_dwordx4 v[188:191], v0, s[88:89] offset:-4096\n"
"s_waitcnt vmcnt(8)\nglobal_load_dwordx4 v[56:59], v0, s[88:89] offset:-3072\ns_waitcnt vmcnt(8)\nglobal_load_dwordx4 v[192:195], v0, s[88:89] offset:-2048\ns_waitcnt vmcnt(8)\n"
"global_load_dwordx4 v[88:91], v0, s[88:89] offset:-1024\nv_max3_u32 v8, v144, v145, v146\nv_max3_u32 v8, v8, v147, v148\nv_max3_u32 v8, v8, v149, v150\nv_max3_u32 v8, v8, v151, v152\n"
"v_max3_u32 v8, v8, v153, v154\nv_max3_u32 v8, v8, v155, v40\nv_max3_u32 v8, v8, v41, v42\nv_max3_u32 v8, v8, v43, v92\nv_max3_u32 v8, v8, v93, v94\nv_max_u32 v8, v8, v95\nv_cmp_eq_u32 vcc, -1, v8\n"
"s_cbranch_vccnz Lms7_%=\nLmb7_%=:\nv_mov_b32 v196, 0x3f803f80\nv_mov_b32 v197, 0x3f803f80\nv_mov_b32 v198, 0x3f803f80\nv_mov_b32 v199, 0x3f803f80\ns_nop 1\n"
"v_mfma_f32_16x16x32_bf16 v[124:127], v[196:199], v[144:147], 0\nv_mfma_f32_16x16x32_bf16 v[200:203], v[144:147], v[144:147], 0\nv_mfma_f32_16x16x32_bf16 v[172:175], v[16:19], v[144:147], 0\n"
"v_mfma_f32_16x16x32_bf16 v[176:179], v[20:23], v[144:147], 0\nv_mfma_f32_16x16x32_bf16 v[124:127], v[196:199], v[148:151], v[124:127]\n"
"v_mfma_f32_16x16x32_bf16 v[200:203], v[148:151], v[148:151], v[200:203]\nv_mfma_f32_16x16x32_bf16 v[172:175], v[24:27], v[148:151], v[172:175]\n"
"v_mfma_f32_16x16x32_bf16 v[176:179], v[28:31], v[148:151], v[176:179]\nv_mfma_f32_16x16x32_bf16 v[124:127], v[196:199], v[152:155], v[124:127]\n"
"v_mfma_f32_16x16x32_bf16 v[200:203], v[152:155], v[152:155], v[200:203]\nv_mfma_f32_16x16x32_bf16 v[172:175], v[32:35], v[152:155], v[172:175]\n"
"v_mfma_f32_16x16x32_bf16 v[176:179], v[36:39], v[152:155], v[176:179]\nv_mfma_f32_16x16x32_bf16 v[124:127], v[196:199], v[40:43], v[124:127]\n"
"v_mfma_f32_16x16x32_bf16 v[200:203], v[40:43], v[40:43], v[200:203]\ns_add_u32 s86, s76, 0xc90000\ns_addc_u32 s87, s77, 0\nglobal_load_dwordx4 v[68:71], v0, s[86:87] offset:-4096 sc1\n"
"global_load_dwordx4 v[180:183], v0, s[86:87] offset:-3072 sc1\nglobal_load_dwordx4 v[184:187], v0, s[86:87] offset:-2048 sc1\nglobal_load_dwordx4 v[156:159], v0, s[86:87] offset:-1024 sc1\n"
"global_load_dwordx4 v[72:75], v0, s[86:87] offset:0 sc1\nv_mfma_f32_16x16x32_bf16 v[172:175], v[100:103], v[40:43], v[172:175]\nv_mfma_f32_16x16x32_bf16 v[176:179], v[104:107], v[40:43], v[176:179]\n"
"v_mfma_f32_16x16x32_bf16 v[124:127], v[196:199], v[92:95], v[124:127]\nv_mfma_f32_16x16x32_bf16 v[200:203], v[92:95], v[92:95], v[200:203]\n"
"v_mfma_f32_16x16x32_bf16 v[172:175], v[108:111], v[92:95], v[172:175]\nv_mfma_f32_16x16x32_bf16 v[176:179], v[112:115], v[92:95], v[176:179]\nds_read_b32 v10, v9 offset:0\n"
"v_mfma_f32_16x16x32_bf16 v[160:163], v[116:119], v[144:147], 0\nv_mfma_f32_16x16x32_bf16 v[164:167], v[60:63], v[144:147], 0\nv_mfma_f32_16x16x32_bf16 v[168:171], v[84:87], v[144:147], 0\n"
"s_waitcnt lgkmcnt(0)\nv_cmp_gt_u32 vcc, 3, v10\ns_cbranch_vccnz Lms8_%=\nLmb8_%=:\nv_cndmask_b32 v125, v200, v201, s[96:97]\nv_cndmask_b32 v125, v125, v202, s[98:99]\n"
"v_cndmask_b32 v125, v125, v203, s[100:101]\ns_mov_b64 exec, s[90:91]\nds_write_b64 v7, v[124:125] offset:0\ns_mov_b64 exec, -1\nv_mfma_f32_16x16x32_bf16 v[160:163], v[44:47], v[148:151], v[160:163]\n"
"ds_write_b128 v1, v[172:175] offset:0\nv_mfma_f32_16x16x32_bf16 v[164:167], v[48:51], v[148:151], v[164:167]\nv_mov_b32 v5, 2\nds_write_b128 v1, v[176:179] offset:4096\ns_mov_b64 exec, 1\n"
"ds_write_b32 v4, v5 offset:0\ns_mov_b64 exec, -1\nv_mfma_f32_16x16x32_bf16 v[168:171], v[52:55], v[148:151], v[168:171]\nv_mfma_f32_16x16x32_bf16 v[160:163], v[80:83], v[152:155], v[160:163]\n"
"v_mfma_f32_16x16x32_bf16 v[164:167], v[128:131], v[152:155], v[164:167]\nv_mfma_f32_16x16x32_bf16 v[168:171], v[132:135], v[152:155], v[168:171]\n"
"v_mfma_f32_16x16x32_bf16 v[160:163], v[136:139], v[40:43], v[160:163]\nv_mfma_f32_16x16x32_bf16 v[164:167], v[140:143], v[40:43], v[164:167]\n"
"v_mfma_f32_16x16x32_bf16 v[168:171], v[240:243], v[40:43], v[168:171]\nv_mfma_f32_16x16x32_bf16 v[160:163], v[244:247], v[92:95], v[160:163]\n"
"v_mfma_f32_16x16x32_bf16 v[164:167], v[248:251], v[92:95], v[164:167]\nv_mfma_f32_16x16x32_bf16 v[168:171], v[220:223], v[92:95], v[168:171]\ns_nop 2\nds_write_b128 v1, v[160:163] offset:8192\ns_nop 3\n"
"ds_write_b128 v1, v[164:167] offset:12288\ns_mov_b64 exec, 1\nds_write_b32 v4, v5 offset:16\ns_mov_b64 exec, -1\ns_nop 1\nds_write_b128 v1, v[168:171] offset:16384\ns_mov_b64 exec, 1\n"
"ds_write_b32 v4, v5 offset:32\ns_mov_b64 exec, -1\ns_waitcnt vmcnt(8)\nglobal_load_dwordx4 v[76:79], v0, s[88:89] offset:0\ns_waitcnt vmcnt(8)\nglobal_load_dwordx4 v[12:15], v0, s[88:89] offset:1024\n"
"s_waitcnt vmcnt(8)\nglobal_load_dwordx4 v[96:99], v0, s[88:89] offset:2048\ns_waitcnt vmcnt(8)\nglobal_load_dwordx4 v[64:67], v0, s[88:89] offset:3072\ns_waitcnt vmcnt(8)\ns_add_u32 s88, s80, 0x195000\n"
"s_addc_u32 s89, s81, 0\nglobal_load_dwordx4 v[236:239], v0, s[88:89] offset:-4096\ns_waitcnt vmcnt(5)\nv_max3_u32 v8, v68, v69, v70\nv_max3_u32 v8, v8, v71, v180\nv_max3_u32 v8, v8, v181, v182\n"
"v_max3_u32 v8, v8, v183, v184\nv_max3_u32 v8, v8, v185, v186\nv_max3_u32 v8, v8, v187, v156\nv_max3_u32 v8, v8, v157, v158\nv_max3_u32 v8, v8, v159, v72\nv_max3_u32 v8, v8, v73, v74\n"
"v_max_u32 v8, v8, v75\nv_cmp_eq_u32 vcc, -1, v8\ns_cbranch_vccnz Lms9_%=\nLmb9_%=:\nv_mfma_f32_16x16x32_bf16 v[144:147], v[196:199], v[68:71], 0\n"
"v_mfma_f32_16x16x32_bf16 v[124:127], v[68:71], v[68:71], 0\nv_mfma_f32_16x16x32_bf16 v[200:203], v[16:19], v[68:71], 0\nv_mfma_f32_16x16x32_bf16 v[172:175], v[20:23], v[68:71], 0\n"
"v_mfma_f32_16x16x32_bf16 v[144:147], v[196:199], v[180:183], v[144:147]\nv_mfma_f32_16x16x32_bf16 v[124:127], v[180:183], v[180:183], v[124:127]\n"
"v_mfma_f32_16x16x32_bf16 v[200:203], v[24:27], v[180:183], v[200:203]\nv_mfma_f32_16x16x32_bf16 v[172:175], v[28:31], v[180:183], v[172:175]\n"
"v_mfma_f32_16x16x32_bf16 v[144:147], v[196:199], v[184:187], v[144:147]\nv_mfma_f32_16x16x32_bf16 v[124:127], v[184:187], v[184:187], v[124:127]\n"
"v_mfma_f32_16x16x32_bf16 v[200:203], v[32:35], v[184:187], v[200:203]\nv_mfma_f32_16x16x32_bf16 v[172:175], v[36:39], v[184:187], v[172:175]\n"
"v_mfma_f32_16x16x32_bf16 v[144:147], v[196:199], v[156:159], v[144:147]\nv_mfma_f32_16x16x32_bf16 v[124:127], v[156:159], v[156:159], v[124:127]\ns_add_u32 s86, s76, 0xc95000\ns_addc_u32 s87, s77, 0\n"
"global_load_dwordx4 v[176:179], v0, s[86:87] offset:-4096 sc1\nglobal_load_dwordx4 v[148:151], v0, s[86:87] offset:-3072 sc1\nglobal_load_dwordx4 v[152:155], v0, s[86:87] offset:-2048 sc1\n"
"global_load_dwordx4 v[40:43], v0, s[86:87] offset:-1024 sc1\nglobal_load_dwordx4 v[92:95], v0, s[86:87] offset:0 sc1\nv_mfma_f32_16x16x32_bf16 v[200:203], v[100:103], v[156:159], v[200:203]\n"
"v_mfma_f32_16x16x32_bf16 v[172:175], v[104:107], v[156:159], v[172:175]\nv_mfma_f32_16x16x32_bf16 v[144:147], v[196:199], v[72:75], v[144:147]\n"
"v_mfma_f32_16x16x32_bf16 v[124:127], v[72:75], v[72:75], v[124:127]\nv_mfma_f32_16x16x32_bf16 v[200:203], v[108:111], v[72:75], v[200:203]\n"
"v_mfma_f32_16x16x32_bf16 v[172:175], v[112:115], v[72:75], v[172:175]\nds_read_b32 v10, v9 offset:4\nv_mfma_f32_16x16x32_bf16 v[160:163], v[116:119], v[68:71], 0\n"
"v_mfma_f32_16x16x32_bf16 v[164:167], v[60:63], v[68:71], 0\nv_mfma_f32_16x16x32_bf16 v[168:171], v[84:87], v[68:71], 0\ns_waitcnt lgkmcnt(0)\nv_cmp_gt_u32 vcc, 3, v10\ns_cbranch_vccnz Lms10_%=\n"
"Lmb10_%=:\nv_cndmask_b32 v145, v124, v125, s[96:97]\nv_cndmask_b32 v145, v145, v126, s[98:99]\nv_cndmask_b32 v145, v145, v127, s[100:101]\ns_mov_b64 exec, s[90:91]\n"
"ds_write_b64 v7, v[144:145] offset:512\ns_mov_b64 exec, -1\nv_mfma_f32_16x16x32_bf16 v[160:163], v[44:47], v[180:183], v[160:163]\nds_write_b128 v1, v[200:203] offset:20480\n"
"v_mfma_f32_16x16x32_bf16 v[164:167], v[48:51], v[180:183], v[164:167]\nds_write_b128 v1, v[172:175] offset:24576\ns_mov_b64 exec, 1\nds_write_b32 v4, v5 offset:48\ns_mov_b64 exec, -1\n"
"v_mfma_f32_16x16x32_bf16 v[168:171], v[52:55], v[180:183], v[168:171]\nv_mfma_f32_16x16x32_bf16 v[160:163], v[80:83], v[184:187], v[160:163]\n"
"v_mfma_f32_16x16x32_bf16 v[164:167], v[128:131], v[184:187], v[164:167]\nv_mfma_f32_16x16x32_bf16 v[168:171], v[132:135], v[184:187], v[168:171]\n"
"v_mfma_f32_16x16x32_bf16 v[160:163], v[136:139], v[156:159], v[160:163]\nv_mfma_f32_16x16x32_bf16 v[164:167], v[140:143], v[156:159], v[164:167]\n"
"v_mfma_f32_16x16x32_bf16 v[168:171], v[240:243], v[156:159], v[168:171]\nv_mfma_f32_16x16x32_bf16 v[160:163], v[244:247], v[72:75], v[160:163]\n"
"v_mfma_f32_16x16x32_bf16 v[164:167], v[248:251], v[72:75], v[164:167]\nv_mfma_f32_16x16x32_bf16 v[168:171], v[220:223], v[72:75], v[168:171]\ns_nop 2\nds_write_b128 v1, v[160:163] offset:28672\n"
"s_nop 3\nds_write_b128 v1, v[164:167] offset:32768\ns_mov_b64 exec, 1\nds_write_b32 v4, v5 offset:64\ns_mov_b64 exec, -1\ns_nop 1\nds_write_b128 v1, v[168:171] offset:36864\ns_mov_b64 exec, 1\n"
"ds_write_b32 v4, v5 offset:80\ns_mov_b64 exec, -1\ns_waitcnt vmcnt(0)\nv_max3_u32 v8, v176, v177, v178\nv_max3_u32 v8, v8, v179, v148\nv_max3_u32 v8, v8, v149, v150\nv_max3_u32 v8, v8, v151, v152\n"
"v_max3_u32 v8, v8, v153, v154\nv_max3_u32 v8, v8, v155, v40\nv_max3_u32 v8, v8, v41, v42\nv_max3_u32 v8, v8, v43, v92\nv_max3_u32 v8, v8, v93, v94\nv_max_u32 v8, v8, v95\nv_cmp_eq_u32 vcc, -1, v8\n"
"s_cbranch_vccnz Lms11_%=\nLmb11_%=:\nv_mfma_f32_16x16x32_bf16 v[68:71], v[196:199], v[176:179], 0\nv_mfma_f32_16x16x32_bf16 v[144:147], v[176:179], v[176:179], 0\n"
"v_mfma_f32_16x16x32_bf16 v[124:127], v[16:19], v[176:179], 0\nv_mfma_f32_16x16x32_bf16 v[200:203], v[20:23], v[176:179], 0\nv_mfma_f32_16x16x32_bf16 v[68:71], v[196:199], v[148:151], v[68:71]\n"
"v_mfma_f32_16x16x32_bf16 v[144:147], v[148:151], v[148:151], v[144:147]\nv_mfma_f32_16x16x32_bf16 v[124:127], v[24:27], v[148:151], v[124:127]\n"
"v_mfma_f32_16x16x32_bf16 v[200:203], v[28:31], v[148:151], v[200:203]\nv_mfma_f32_16x16x32_bf16 v[68:71], v[196:199], v[152:155], v[68:71]\n"
"v_mfma_f32_16x16x32_bf16 v[144:147], v[152:155], v[152:155], v[144:147]\nv_mfma_f32_16x16x32_bf16 v[124:127], v[32:35], v[152:155], v[124:127]\n"
"v_mfma_f32_16x16x32_bf16 v[200:203], v[36:39], v[152:155], v[200:203]\nv_mfma_f32_16x16x32_bf16 v[68:71], v[196:199], v[40:43], v[68:71]\n"
"v_mfma_f32_16x16x32_bf16 v[144:147], v[40:43], v[40:43], v[144:147]\ns_add_u32 s86, s76, 0xc9a000\ns_addc_u32 s87, s77, 0\nglobal_load_dwordx4 v[172:175], v0, s[86:87] offset:-4096 sc1\n"
"global_load_dwordx4 v[180:183], v0, s[86:87] offset:-3072 sc1\nglobal_load_dwordx4 v[184:187], v0, s[86:87] offset:-2048 sc1\nglobal_load_dwordx4 v[156:159], v0, s[86:87] offset:-1024 sc1\n"
"global_load_dwordx4 v[72:75], v0, s[86:87] offset:0 sc1\nv_mfma_f32_16x16x32_bf16 v[124:127], v[100:103], v[40:43], v[124:127]\nv_mfma_f32_16x16x32_bf16 v[200:203], v[104:107], v[40:43], v[200:203]\n"
"v_mfma_f32_16x16x32_bf16 v[68:71], v[196:199], v[92:95], v[68:71]\nv_mfma_f32_16x16x32_bf16 v[144:147], v[92:95], v[92:95], v[144:147]\n"
"v_mfma_f32_16x16x32_bf16 v[124:127], v[108:111], v[92:95], v[124:127]\nv_mfma_f32_16x16x32_bf16 v[200:203], v[112:115], v[92:95], v[200:203]\nds_read_b32 v10, v9 offset:8\n"
"v_mfma_f32_16x16x32_bf16 v[160:163], v[116:119], v[176:179], 0\nv_mfma_f32_16x16x32_bf16 v[164:167], v[60:63], v[176:179], 0\nv_mfma_f32_16x16x32_bf16 v[168:171], v[84:87], v[176:179], 0\n"
"s_waitcnt lgkmcnt(0)\nv_cmp_gt_u32 vcc, 3, v10\ns_cbranch_vccnz Lms12_%=\nLmb12_%=:\nv_cndmask_b32 v69, v144, v145, s[96:97]\nv_cndmask_b32 v69, v69, v146, s[98:99]\n"
"v_cndmask_b32 v69, v69, v147, s[100:101]\ns_mov_b64 exec, s[90:91]\nds_write_b64 v7, v[68:69] offset:1024\ns_mov_b64 exec, -1\nv_mfma_f32_16x16x32_bf16 v[160:163], v[44:47], v[148:151], v[160:163]\n"
"ds_write_b128 v1, v[124:127] offset:40960\nv_mfma_f32_16x16x32_bf16 v[164:167], v[48:51], v[148:151], v[164:167]\nds_write_b128 v1, v[200:203] offset:45056\ns_mov_b64 exec, 1\n"
"ds_write_b32 v4, v5 offset:96\ns_mov_b64 exec, -1\nv_mfma_f32_16x16x32_bf16 v[168:171], v[52:55], v[148:151], v[168:171]\nv_mfma_f32_16x16x32_bf16 v[160:163], v[80:83], v[152:155], v[160:163]\n"
"v_mfma_f32_16x16x32_bf16 v[164:167], v[128:131], v[152:155], v[164:167]\nv_mfma_f32_16x16x32_bf16 v[168:171], v[132:135], v[152:155], v[168:171]\n"
"v_mfma_f32_16x16x32_bf16 v[160:163], v[136:139], v[40:43], v[160:163]\nv_mfma_f32_16x16x32_bf16 v[164:167], v[140:143], v[40:43], v[164:167]\n"
"v_mfma_f32_16x16x32_bf16 v[168:171], v[240:243], v[40:43], v[168:171]\nv_mfma_f32_16x16x32_bf16 v[160:163], v[244:247], v[92:95], v[160:163]\n"
"v_mfma_f32_16x16x32_bf16 v[164:167], v[248:251], v[92:95], v[164:167]\nv_mfma_f32_16x16x32_bf16 v[168:171], v[220:223], v[92:95], v[168:171]\ns_nop 2\nds_write_b128 v1, v[160:163] offset:49152\n"
"s_nop 3\nds_write_b128 v1, v[164:167] offset:53248\ns_mov_b64 exec, 1\nds_write_b32 v4, v5 offset:112\ns_mov_b64 exec, -1\ns_nop 1\nds_write_b128 v1, v[168:171] offset:57344\ns_mov_b64 exec, 1\n"
"ds_write_b32 v4, v5 offset:128\ns_mov_b64 exec, -1\nglobal_load_dwordx4 v[176:179], v0, s[88:89] offset:-3072\nglobal_load_dwordx4 v[68:71], v0, s[88:89] offset:-2048\n"
"global_load_dwordx4 v[144:147], v0, s[88:89] offset:-1024\nglobal_load_dwordx4 v[124:127], v0, s[88:89] offset:0\ns_waitcnt vmcnt(8)\nglobal_load_dwordx4 v[200:203], v0, s[88:89] offset:1024\n"
"s_waitcnt vmcnt(5)\nv_max3_u32 v8, v172, v173, v174\nv_max3_u32 v8, v8, v175, v180\nv_max3_u32 v8, v8, v181, v182\nv_max3_u32 v8, v8, v183, v184\nv_max3_u32 v8, v8, v185, v186\n"
"v_max3_u32 v8, v8, v187, v156\nv_max3_u32 v8, v8, v157, v158\nv_max3_u32 v8, v8, v159, v72\nv_max3_u32 v8, v8, v73, v74\nv_max_u32 v8, v8, v75\nv_cmp_eq_u32 vcc, -1, v8\ns_cbranch_vccnz Lms13_%=\n"
"Lmb13_%=:\nv_mfma_f32_16x16x32_bf16 v[148:151], v[196:199], v[172:175], 0\nv_mfma_f32_16x16x32_bf16 v[152:155], v[172:175], v[172:175], 0\nv_mfma_f32_16x16x32_bf16 v[40:43], v[16:19], v[172:175], 0\n"
"v_mfma_f32_16x16x32_bf16 v[92:95], v[20:23], v[172:175], 0\nv_mfma_f32_16x16x32_bf16 v[148:151], v[196:199], v[180:183], v[148:151]\n"
"v_mfma_f32_16x16x32_bf16 v[152:155], v[180:183], v[180:183], v[152:155]\nv_mfma_f32_16x16x32_bf16 v[40:43], v[24:27], v[180:183], v[40:43]\n"
"v_mfma_f32_16x16x32_bf16 v[92:95], v[28:31], v[180:183], v[92:95]\nv_mfma_f32_16x16x32_bf16 v[148:151], v[196:199], v[184:187], v[148:151]\n"
"v_mfma_f32_16x16x32_bf16 v[152:155], v[184:187], v[184:187], v[152:155]\nv_mfma_f32_16x16x32_bf16 v[40:43], v[32:35], v[184:187], v[40:43]\n"
"v_mfma_f32_16x16x32_bf16 v[92:95], v[36:39], v[184:187], v[92:95]\nv_mfma_f32_16x16x32_bf16 v[148:151], v[196:199], v[156:159], v[148:151]\n"
"v_mfma_f32_16x16x32_bf16 v[152:155], v[156:159], v[156:159], v[152:155]\nv_mfma_f32_16x16x32_bf16 v[40:43], v[100:103], v[156:159], v[40:43]\n"
"v_mfma_f32_16x16x32_bf16 v[92:95], v[104:107], v[156:159], v[92:95]\nv_mfma_f32_16x16x32_bf16 v[148:151], v[196:199], v[72:75], v[148:151]\n"
"v_mfma_f32_16x16x32_bf16 v[152:155], v[72:75], v[72:75], v[152:155]\nv_mfma_f32_16x16x32_bf16 v[40:43], v[108:111], v[72:75], v[40:43]\n"
"v_mfma_f32_16x16x32_bf16 v[92:95], v[112:115], v[72:75], v[92:95]\nds_read_b32 v10, v9 offset:12\nv_mfma_f32_16x16x32_bf16 v[160:163], v[116:119], v[172:175], 0\n"
"v_mfma_f32_16x16x32_bf16 v[164:167], v[60:63], v[172:175], 0\nv_mfma_f32_16x16x32_bf16 v[168:171], v[84:87], v[172:175], 0\ns_waitcnt lgkmcnt(0)\nv_cmp_gt_u32 vcc, 3, v10\ns_cbranch_vccnz Lms14_%=\n"
"Lmb14_%=:\nv_cndmask_b32 v149, v152, v153, s[96:97]\nv_cndmask_b32 v149, v149, v154, s[98:99]\nv_cndmask_b32 v149, v149, v155, s[100:101]\ns_mov_b64 exec, s[90:91]\n"
"ds_write_b64 v7, v[148:149] offset:1536\ns_mov_b64 exec, -1\nv_mfma_f32_16x16x32_bf16 v[160:163], v[44:47], v[180:183], v[160:163]\nds_write_b128 v1, v[40:43] offset:61440\n"
"v_mfma_f32_16x16x32_bf16 v[164:167], v[48:51], v[180:183], v[164:167]\nds_write_b128 v2, v[92:95] offset:0\ns_mov_b64 exec, 1\nds_write_b32 v4, v5 offset:144\ns_mov_b64 exec, -1\n"
"v_mfma_f32_16x16x32_bf16 v[168:171], v[52:55], v[180:183], v[168:171]\nv_mfma_f32_16x16x32_bf16 v[160:163], v[80:83], v[184:187], v[160:163]\n"
"v_mfma_f32_16x16x32_bf16 v[164:167], v[128:131], v[184:187], v[164:167]\nv_mfma_f32_16x16x32_bf16 v[168:171], v[132:135], v[184:187], v[168:171]\n"
"v_mfma_f32_16x16x32_bf16 v[160:163], v[136:139], v[156:159], v[160:163]\nv_mfma_f32_16x16x32_bf16 v[164:167], v[140:143], v[156:159], v[164:167]\n"
"v_mfma_f32_16x16x32_bf16 v[168:171], v[240:243], v[156:159], v[168:171]\nv_mfma_f32_16x16x32_bf16 v[160:163], v[244:247], v[72:75], v[160:163]\n"
"v_mfma_f32_16x16x32_bf16 v[164:167], v[248:251], v[72:75], v[164:167]\nv_mfma_f32_16x16x32_bf16 v[168:171], v[220:223], v[72:75], v[168:171]\ns_nop 2\nds_write_b128 v2, v[160:163] offset:4096\ns_nop 3\n"
"ds_write_b128 v2, v[164:167] offset:8192\ns_mov_b64 exec, 1\nds_write_b32 v4, v5 offset:160\ns_mov_b64 exec, -1\ns_nop 1\nds_write_b128 v2, v[168:171] offset:12288\ns_mov_b64 exec, 1\n"
"ds_write_b32 v4, v5 offset:176\ns_mov_b64 exec, -1\nglobal_load_dwordx4 v[16:19], v0, s[88:89] offset:2048\nglobal_load_dwordx4 v[20:23], v0, s[88:89] offset:3072\ns_add_u32 s88, s80, 0x197000\n"
"s_addc_u32 s89, s81, 0\nglobal_load_dwordx4 v[24:27], v0, s[88:89] offset:-4096\ns_add_u32 s88, s82, 0x1000\ns_addc_u32 s89, s83, 0\nglobal_load_dwordx4 v[28:31], v0, s[88:89] offset:-4096\n"
"global_load_dwordx4 v[32:35], v0, s[88:89] offset:-3072\nglobal_load_dwordx4 v[36:39], v0, s[88:89] offset:-2048\nglobal_load_dwordx4 v[100:103], v0, s[88:89] offset:-1024\n"
"global_load_dwordx4 v[104:107], v0, s[88:89] offset:0\nglobal_load_dwordx4 v[196:199], v0, s[88:89] offset:1024\nglobal_load_dwordx4 v[108:111], v0, s[88:89] offset:2048\n"
"global_load_dwordx4 v[112:115], v0, s[88:89] offset:3072\ns_add_u32 s88, s82, 0x3000\ns_addc_u32 s89, s83, 0\nglobal_load_dwordx4 v[116:119], v0, s[88:89] offset:-4096\n"
"global_load_dwordx4 v[60:63], v0, s[88:89] offset:-3072\nglobal_load_dwordx4 v[172:175], v0, s[88:89] offset:-2048\nglobal_load_dwordx4 v[84:87], v0, s[88:89] offset:-1024\n"
"global_load_dwordx4 v[148:151], v0, s[88:89] offset:0\nLmc15_%=:\nds_read_b32 v10, v9 offset:12\ns_waitcnt lgkmcnt(0)\nv_cmp_gt_u32 vcc, 6, v10\ns_cbranch_vccz Lmb15_%=\ns_sleep 2\n"
"s_sub_u32 s95, s95, 1\ns_cbranch_scc0 Lmc15_%=\ns_mov_b32 s95, 0\nLmb15_%=:\ns_add_u32 s86, s76, 0xa06000\ns_addc_u32 s87, s77, 0\nglobal_load_dwordx4 v[152:155], v253, s[86:87] offset:-4096 sc1\n"
"global_load_dwordx4 v[44:47], v253, s[86:87] offset:-3072 sc1\nglobal_load_dwordx4 v[40:43], v253, s[86:87] offset:-2048 sc1\nglobal_load_dwordx4 v[48:51], v253, s[86:87] offset:-1024 sc1\n"
"global_load_dwordx4 v[92:95], v253, s[86:87] offset:0 sc1\ns_waitcnt vmcnt(0)\nv_max3_u32 v8, v152, v153, v154\nv_max3_u32 v8, v8, v155, v44\nv_max3_u32 v8, v8, v45, v46\nv_max3_u32 v8, v8, v47, v40\n"
"v_max3_u32 v8, v8, v41, v42\nv_max3_u32 v8, v8, v43, v48\nv_max3_u32 v8, v8, v49, v50\nv_max3_u32 v8, v8, v51, v92\nv_max3_u32 v8, v8, v93, v94\nv_max_u32 v8, v8, v95\nv_cmp_eq_u32 vcc, -1, v8\n"
"s_cbranch_vccnz Lms16_%=\nLmb16_%=:\nv_mfma_f32_16x16x32_bf16 v[180:183], v[224:227], v[152:155], 0\nv_mfma_f32_16x16x32_bf16 v[52:55], v[228:231], v[152:155], 0\n"
"v_mfma_f32_16x16x32_bf16 v[180:183], v[232:235], v[44:47], v[180:183]\nv_mfma_f32_16x16x32_bf16 v[52:55], v[204:207], v[44:47], v[52:55]\ns_add_u32 s86, s76, 0xa01000\ns_addc_u32 s87, s77, 0\n"
"global_load_dwordx4 v[80:83], v253, s[86:87] offset:-4096 sc1\nglobal_load_dwordx4 v[128:131], v253, s[86:87] offset:-3072 sc1\nglobal_load_dwordx4 v[184:187], v253, s[86:87] offset:-2048 sc1\n"
"global_load_dwordx4 v[132:135], v253, s[86:87] offset:-1024 sc1\nglobal_load_dwordx4 v[136:139], v253, s[86:87] offset:0 sc1\nv_mfma_f32_16x16x32_bf16 v[180:183], v[208:211], v[40:43], v[180:183]\n"
"v_mfma_f32_16x16x32_bf16 v[52:55], v[212:215], v[40:43], v[52:55]\nv_mfma_f32_16x16x32_bf16 v[180:183], v[216:219], v[48:51], v[180:183]\n"
"v_mfma_f32_16x16x32_bf16 v[52:55], v[120:123], v[48:51], v[52:55]\nv_mfma_f32_16x16x32_bf16 v[180:183], v[188:191], v[92:95], v[180:183]\n"
"v_mfma_f32_16x16x32_bf16 v[52:55], v[56:59], v[92:95], v[52:55]\nds_read_b32 v10, v9 offset:4\nv_mfma_f32_16x16x32_bf16 v[140:143], v[192:195], v[152:155], 0\n"
"v_mfma_f32_16x16x32_bf16 v[156:159], v[88:91], v[152:155], 0\nv_mfma_f32_16x16x32_bf16 v[240:243], v[76:79], v[152:155], 0\ns_waitcnt lgkmcnt(0)\nv_cmp_gt_u32 vcc, 6, v10\ns_cbranch_vccnz Lms17_%=\n"
"Lmb17_%=:\nds_write_b128 v1, v[180:183] offset:20480\nv_mfma_f32_16x16x32_bf16 v[140:143], v[12:15], v[44:47], v[140:143]\nv_mov_b32 v5, 3\nds_write_b128 v1, v[52:55] offset:24576\ns_mov_b64 exec, 1\n"
"ds_write_b32 v4, v5 offset:48\ns_mov_b64 exec, -1\nv_mfma_f32_16x16x32_bf16 v[156:159], v[96:99], v[44:47], v[156:159]\nv_mfma_f32_16x16x32_bf16 v[240:243], v[64:67], v[44:47], v[240:243]\n"
"v_mfma_f32_16x16x32_bf16 v[140:143], v[236:239], v[40:43], v[140:143]\nv_mfma_f32_16x16x32_bf16 v[156:159], v[176:179], v[40:43], v[156:159]\n"
"v_mfma_f32_16x16x32_bf16 v[240:243], v[68:71], v[40:43], v[240:243]\nv_mfma_f32_16x16x32_bf16 v[140:143], v[144:147], v[48:51], v[140:143]\n"
"v_mfma_f32_16x16x32_bf16 v[156:159], v[124:127], v[48:51], v[156:159]\nv_mfma_f32_16x16x32_bf16 v[240:243], v[200:203], v[48:51], v[240:243]\n"
"v_mfma_f32_16x16x32_bf16 v[140:143], v[16:19], v[92:95], v[140:143]\nv_mfma_f32_16x16x32_bf16 v[156:159], v[20:23], v[92:95], v[156:159]\n"
"v_mfma_f32_16x16x32_bf16 v[240:243], v[24:27], v[92:95], v[240:243]\ns_nop 2\nds_write_b128 v1, v[140:143] offset:28672\ns_nop 3\nds_write_b128 v1, v[156:159] offset:32768\ns_mov_b64 exec, 1\n"
"ds_write_b32 v4, v5 offset:64\ns_mov_b64 exec, -1\ns_nop 1\nds_write_b128 v1, v[240:243] offset:36864\ns_mov_b64 exec, 1\nds_write_b32 v4, v5 offset:80\ns_mov_b64 exec, -1\n"
"global_load_dwordx4 v[244:247], v0, s[88:89] offset:1024\nglobal_load_dwordx4 v[248:251], v0, s[88:89] offset:2048\nglobal_load_dwordx4 v[72:75], v0, s[88:89] offset:3072\ns_add_u32 s88, s82, 0x5000\n"
"s_addc_u32 s89, s83, 0\nglobal_load_dwordx4 v[220:223], v0, s[88:89] offset:-4096\ns_waitcnt vmcnt(8)\nglobal_load_dwordx4 v[160:163], v0, s[88:89] offset:-3072\ns_waitcnt vmcnt(5)\n"
"v_max3_u32 v8, v80, v81, v82\nv_max3_u32 v8, v8, v83, v128\nv_max3_u32 v8, v8, v129, v130\nv_max3_u32 v8, v8, v131, v184\nv_max3_u32 v8, v8, v185, v186\nv_max3_u32 v8, v8, v187, v132\n"
"v_max3_u32 v8, v8, v133, v134\nv_max3_u32 v8, v8, v135, v136\nv_max3_u32 v8, v8, v137, v138\nv_max_u32 v8, v8, v139\nv_cmp_eq_u32 vcc, -1, v8\ns_cbranch_vccnz Lms18_%=\nLmb18_%=:\n"
"v_mfma_f32_16x16x32_bf16 v[164:167], v[224:227], v[80:83], 0\nv_mfma_f32_16x16x32_bf16 v[168:171], v[228:231], v[80:83], 0\nv_mfma_f32_16x16x32_bf16 v[164:167], v[232:235], v[128:131], v[164:167]\n"
"v_mfma_f32_16x16x32_bf16 v[168:171], v[204:207], v[128:131], v[168:171]\ns_add_u32 s86, s76, 0xa0b000\ns_addc_u32 s87, s77, 0\nglobal_load_dwordx4 v[152:155], v253, s[86:87] offset:-4096 sc1\n"
"global_load_dwordx4 v[180:183], v253, s[86:87] offset:-3072 sc1\nglobal_load_dwordx4 v[52:55], v253, s[86:87] offset:-2048 sc1\nglobal_load_dwordx4 v[44:47], v253, s[86:87] offset:-1024 sc1\n"
"global_load_dwordx4 v[40:43], v253, s[86:87] offset:0 sc1\nv_mfma_f32_16x16x32_bf16 v[164:167], v[208:211], v[184:187], v[164:167]\n"
"v_mfma_f32_16x16x32_bf16 v[168:171], v[212:215], v[184:187], v[168:171]\nv_mfma_f32_16x16x32_bf16 v[164:167], v[216:219], v[132:135], v[164:167]\n"
"v_mfma_f32_16x16x32_bf16 v[168:171], v[120:123], v[132:135], v[168:171]\nv_mfma_f32_16x16x32_bf16 v[164:167], v[188:191], v[136:139], v[164:167]\n"
"v_mfma_f32_16x16x32_bf16 v[168:171], v[56:59], v[136:139], v[168:171]\nds_read_b32 v10, v9 offset:0\nv_mfma_f32_16x16x32_bf16 v[48:51], v[192:195], v[80:83], 0\n"
"v_mfma_f32_16x16x32_bf16 v[92:95], v[88:91], v[80:83], 0\nv_mfma_f32_16x16x32_bf16 v[140:143], v[76:79], v[80:83], 0\ns_waitcnt lgkmcnt(0)\nv_cmp_gt_u32 vcc, 6, v10\ns_cbranch_vccnz Lms19_%=\n"
"Lmb19_%=:\nds_write_b128 v1, v[164:167] offset:0\nv_mfma_f32_16x16x32_bf16 v[48:51], v[12:15], v[128:131], v[48:51]\nds_write_b128 v1, v[168:171] offset:4096\ns_mov_b64 exec, 1\n"
"ds_write_b32 v4, v5 offset:0\ns_mov_b64 exec, -1\nv_mfma_f32_16x16x32_bf16 v[92:95], v[96:99], v[128:131], v[92:95]\nv_mfma_f32_16x16x32_bf16 v[140:143], v[64:67], v[128:131], v[140:143]\n"
"v_mfma_f32_16x16x32_bf16 v[48:51], v[236:239], v[184:187], v[48:51]\nv_mfma_f32_16x16x32_bf16 v[92:95], v[176:179], v[184:187], v[92:95]\n"
"v_mfma_f32_16x16x32_bf16 v[140:143], v[68:71], v[184:187], v[140:143]\nv_mfma_f32_16x16x32_bf16 v[48:51], v[144:147], v[132:135], v[48:51]\n"
"v_mfma_f32_16x16x32_bf16 v[92:95], v[124:127], v[132:135], v[92:95]\nv_mfma_f32_16x16x32_bf16 v[140:143], v[200:203], v[132:135], v[140:143]\n"
"v_mfma_f32_16x16x32_bf16 v[48:51], v[16:19], v[136:139], v[48:51]\nv_mfma_f32_16x16x32_bf16 v[92:95], v[20:23], v[136:139], v[92:95]\n"
"v_mfma_f32_16x16x32_bf16 v[140:143], v[24:27], v[136:139], v[140:143]\ns_nop 2\nds_write_b128 v1, v[48:51] offset:8192\ns_nop 3\nds_write_b128 v1, v[92:95] offset:12288\ns_mov_b64 exec, 1\n"
"ds_write_b32 v4, v5 offset:16\ns_mov_b64 exec, -1\ns_nop 1\nds_write_b128 v1, v[140:143] offset:16384\ns_mov_b64 exec, 1\nds_write_b32 v4, v5 offset:32\ns_mov_b64 exec, -1\ns_waitcnt vmcnt(8)\n"
"global_load_dwordx4 v[156:159], v0, s[88:89] offset:-2048\ns_waitcnt vmcnt(8)\nglobal_load_dwordx4 v[240:243], v0, s[88:89] offset:-1024\ns_waitcnt vmcnt(2)\nv_max3_u32 v8, v152, v153, v154\n"
"v_max3_u32 v8, v8, v155, v180\nv_max3_u32 v8, v8, v181, v182\nv_max3_u32 v8, v8, v183, v52\nv_max3_u32 v8, v8, v53, v54\nv_max3_u32 v8, v8, v55, v44\nv_max3_u32 v8, v8, v45, v46\n"
"v_max3_u32 v8, v8, v47, v40\nv_max3_u32 v8, v8, v41, v42\nv_max_u32 v8, v8, v43\nv_cmp_eq_u32 vcc, -1, v8\ns_cbranch_vccnz Lms20_%=\nLmb20_%=:\n"
"v_mfma_f32_16x16x32_bf16 v[80:83], v[224:227], v[152:155], 0\nv_mfma_f32_16x16x32_bf16 v[164:167], v[228:231], v[152:155], 0\nv_mfma_f32_16x16x32_bf16 v[80:83], v[232:235], v[180:183], v[80:83]\n"
"v_mfma_f32_16x16x32_bf16 v[164:167], v[204:207], v[180:183], v[164:167]\ns_add_u32 s86, s76, 0xa10000\ns_addc_u32 s87, s77, 0\nglobal_load_dwordx4 v[168:171], v253, s[86:87] offset:-4096 sc1\n"
"global_load_dwordx4 v[128:131], v253, s[86:87] offset:-3072 sc1\nglobal_load_dwordx4 v[184:187], v253, s[86:87] offset:-2048 sc1\nglobal_load_dwordx4 v[132:135], v253, s[86:87] offset:-1024 sc1\n"
"global_load_dwordx4 v[136:139], v253, s[86:87] offset:0 sc1\nv_mfma_f32_16x16x32_bf16 v[80:83], v[208:211], v[52:55], v[80:83]\nv_mfma_f32_16x16x32_bf16 v[164:167], v[212:215], v[52:55], v[164:167]\n"
"v_mfma_f32_16x16x32_bf16 v[80:83], v[216:219], v[44:47], v[80:83]\nv_mfma_f32_16x16x32_bf16 v[164:167], v[120:123], v[44:47], v[164:167]\n"
"v_mfma_f32_16x16x32_bf16 v[80:83], v[188:191], v[40:43], v[80:83]\nv_mfma_f32_16x16x32_bf16 v[164:167], v[56:59], v[40:43], v[164:167]\nds_read_b32 v10, v9 offset:8\n"
"v_mfma_f32_16x16x32_bf16 v[48:51], v[192:195], v[152:155], 0\nv_mfma_f32_16x16x32_bf16 v[92:95], v[88:91], v[152:155], 0\nv_mfma_f32_16x16x32_bf16 v[140:143], v[76:79], v[152:155], 0\n"
"s_waitcnt lgkmcnt(0)\nv_cmp_gt_u32 vcc, 6, v10\ns_cbranch_vccnz Lms21_%=\nLmb21_%=:\nds_write_b128 v1, v[80:83] offset:40960\nv_mfma_f32_16x16x32_bf16 v[48:51], v[12:15], v[180:183], v[48:51]\n"
"ds_write_b128 v1, v[164:167] offset:45056\ns_mov_b64 exec, 1\nds_write_b32 v4, v5 offset:96\ns_mov_b64 exec, -1\nv_mfma_f32_16x16x32_bf16 v[92:95], v[96:99], v[180:183], v[92:95]\n"
"v_mfma_f32_16x16x32_bf16 v[140:143], v[64:67], v[180:183], v[140:143]\nv_mfma_f32_16x16x32_bf16 v[48:51], v[236:239], v[52:55], v[48:51]\n"
"v_mfma_f32_16x16x32_bf16 v[92:95], v[176:179], v[52:55], v[92:95]\nv_mfma_f32_16x16x32_bf16 v[140:143], v[68:71], v[52:55], v[140:143]\n"
"v_mfma_f32_16x16x32_bf16 v[48:51], v[144:147], v[44:47], v[48:51]\nv_mfma_f32_16x16x32_bf16 v[92:95], v[124:127], v[44:47], v[92:95]\n"
"v_mfma_f32_16x16x32_bf16 v[140:143], v[200:203], v[44:47], v[140:143]\nv_mfma_f32_16x16x32_bf16 v[48:51], v[16:19], v[40:43], v[48:51]\nv_mfma_f32_16x16x32_bf16 v[92:95], v[20:23], v[40:43], v[92:95]\n"
"v_mfma_f32_16x16x32_bf16 v[140:143], v[24:27], v[40:43], v[140:143]\ns_nop 2\nds_write_b128 v1, v[48:51] offset:49152\ns_nop 3\nds_write_b128 v1, v[92:95] offset:53248\ns_mov_b64 exec, 1\n"
"ds_write_b32 v4, v5 offset:112\ns_mov_b64 exec, -1\ns_nop 1\nds_write_b128 v1, v[140:143] offset:57344\ns_mov_b64 exec, 1\nds_write_b32 v4, v5 offset:128\ns_mov_b64 exec, -1\n"
"global_load_dwordx4 v[152:155], v0, s[88:89] offset:0\nglobal_load_dwordx4 v[80:83], v0, s[88:89] offset:1024\ns_waitcnt vmcnt(8)\nglobal_load_dwordx4 v[164:167], v0, s[88:89] offset:2048\n"
"s_waitcnt vmcnt(8)\nglobal_load_dwordx4 v[180:183], v0, s[88:89] offset:3072\ns_waitcnt vmcnt(8)\ns_add_u32 s88, s82, 0x7000\ns_addc_u32 s89, s83, 0\n"
"global_load_dwordx4 v[52:55], v0, s[88:89] offset:-4096\ns_waitcnt vmcnt(5)\nv_max3_u32 v8, v168, v169, v170\nv_max3_u32 v8, v8, v171, v128\nv_max3_u32 v8, v8, v129, v130\n"
"v_max3_u32 v8, v8, v131, v184\nv_max3_u32 v8, v8, v185, v186\nv_max3_u32 v8, v8, v187, v132\nv_max3_u32 v8, v8, v133, v134\nv_max3_u32 v8, v8, v135, v136\nv_max3_u32 v8, v8, v137, v138\n"
"v_max_u32 v8, v8, v139\nv_cmp_eq_u32 vcc, -1, v8\ns_cbranch_vccnz Lms22_%=\nLmb22_%=:\nv_mfma_f32_16x16x32_bf16 v[44:47], v[224:227], v[168:171], 0\n"
"v_mfma_f32_16x16x32_bf16 v[40:43], v[228:231], v[168:171], 0\nv_mfma_f32_16x16x32_bf16 v[44:47], v[232:235], v[128:131], v[44:47]\nv_mfma_f32_16x16x32_bf16 v[40:43], v[204:207], v[128:131], v[40:43]\n"
"s_add_u32 s86, s76, 0xf10000\ns_addc_u32 s87, s77, 0\nglobal_load_dwordx4 v[48:51], v0, s[86:87] offset:-4096 sc1\nglobal_load_dwordx4 v[92:95], v0, s[86:87] offset:-3072 sc1\n"
"global_load_dwordx4 v[140:143], v0, s[86:87] offset:-2048 sc1\nglobal_load_dwordx4 v[224:227], v0, s[86:87] offset:-1024 sc1\nglobal_load_dwordx4 v[228:231], v0, s[86:87] offset:0 sc1\n"
"v_mfma_f32_16x16x32_bf16 v[44:47], v[208:211], v[184:187], v[44:47]\nv_mfma_f32_16x16x32_bf16 v[40:43], v[212:215], v[184:187], v[40:43]\n"
"v_mfma_f32_16x16x32_bf16 v[44:47], v[216:219], v[132:135], v[44:47]\nv_mfma_f32_16x16x32_bf16 v[40:43], v[120:123], v[132:135], v[40:43]\n"
"v_mfma_f32_16x16x32_bf16 v[44:47], v[188:191], v[136:139], v[44:47]\nv_mfma_f32_16x16x32_bf16 v[40:43], v[56:59], v[136:139], v[40:43]\nds_read_b32 v10, v9 offset:12\n"
"v_mfma_f32_16x16x32_bf16 v[232:235], v[192:195], v[168:171], 0\nv_mfma_f32_16x16x32_bf16 v[204:207], v[88:91], v[168:171], 0\nv_mfma_f32_16x16x32_bf16 v[208:211], v[76:79], v[168:171], 0\n"
"s_waitcnt lgkmcnt(0)\nv_cmp_gt_u32 vcc, 6, v10\ns_cbranch_vccnz Lms23_%=\nLmb23_%=:\nds_write_b128 v1, v[44:47] offset:61440\nv_mfma_f32_16x16x32_bf16 v[232:235], v[12:15], v[128:131], v[232:235]\n"
"ds_write_b128 v2, v[40:43] offset:0\ns_mov_b64 exec, 1\nds_write_b32 v4, v5 offset:144\ns_mov_b64 exec, -1\nv_mfma_f32_16x16x32_bf16 v[204:207], v[96:99], v[128:131], v[204:207]\n"
"v_mfma_f32_16x16x32_bf16 v[208:211], v[64:67], v[128:131], v[208:211]\nv_mfma_f32_16x16x32_bf16 v[232:235], v[236:239], v[184:187], v[232:235]\n"
"v_mfma_f32_16x16x32_bf16 v[204:207], v[176:179], v[184:187], v[204:207]\nv_mfma_f32_16x16x32_bf16 v[208:211], v[68:71], v[184:187], v[208:211]\n"
"v_mfma_f32_16x16x32_bf16 v[232:235], v[144:147], v[132:135], v[232:235]\nv_mfma_f32_16x16x32_bf16 v[204:207], v[124:127], v[132:135], v[204:207]\n"
"v_mfma_f32_16x16x32_bf16 v[208:211], v[200:203], v[132:135], v[208:211]\nv_mfma_f32_16x16x32_bf16 v[232:235], v[16:19], v[136:139], v[232:235]\n"
"v_mfma_f32_16x16x32_bf16 v[204:207], v[20:23], v[136:139], v[204:207]\nv_mfma_f32_16x16x32_bf16 v[208:211], v[24:27], v[136:139], v[208:211]\ns_nop 2\nds_write_b128 v2, v[232:235] offset:4096\ns_nop 3\n"
"ds_write_b128 v2, v[204:207] offset:8192\ns_mov_b64 exec, 1\nds_write_b32 v4, v5 offset:160\ns_mov_b64 exec, -1\ns_nop 1\nds_write_b128 v2, v[208:211] offset:12288\ns_mov_b64 exec, 1\n"
"ds_write_b32 v4, v5 offset:176\ns_mov_b64 exec, -1\ns_waitcnt vmcnt(8)\nglobal_load_dwordx4 v[212:215], v0, s[88:89] offset:-3072\ns_waitcnt vmcnt(8)\n"
"global_load_dwordx4 v[216:219], v0, s[88:89] offset:-2048\ns_waitcnt vmcnt(8)\nglobal_load_dwordx4 v[120:123], v0, s[88:89] offset:-1024\ns_waitcnt vmcnt(8)\n"
"global_load_dwordx4 v[188:191], v0, s[88:89] offset:0\ns_waitcnt vmcnt(8)\nglobal_load_dwordx4 v[56:59], v0, s[88:89] offset:1024\ns_waitcnt vmcnt(8)\n"
"global_load_dwordx4 v[192:195], v0, s[88:89] offset:2048\ns_waitcnt vmcnt(8)\nglobal_load_dwordx4 v[88:91], v0, s[88:89] offset:3072\ns_waitcnt vmcnt(8)\ns_add_u32 s88, s82, 0x9000\n"
"s_addc_u32 s89, s83, 0\nglobal_load_dwordx4 v[168:171], v0, s[88:89] offset:-4096\ns_waitcnt vmcnt(8)\nv_max3_u32 v8, v48, v49, v50\nv_max3_u32 v8, v8, v51, v92\nv_max3_u32 v8, v8, v93, v94\n"
"v_max3_u32 v8, v8, v95, v140\nv_max3_u32 v8, v8, v141, v142\nv_max3_u32 v8, v8, v143, v224\nv_max3_u32 v8, v8, v225, v226\nv_max3_u32 v8, v8, v227, v228\nv_max3_u32 v8, v8, v229, v230\n"
"v_max_u32 v8, v8, v231\nv_cmp_eq_u32 vcc, -1, v8\ns_cbranch_vccnz Lms24_%=\nLmb24_%=:\nv_mov_b32 v76, 0x3f803f80\nv_mov_b32 v77, 0x3f803f80\nv_mov_b32 v78, 0x3f803f80\nv_mov_b32 v79, 0x3f803f80\n"
"s_nop 1\nv_mfma_f32_16x16x32_bf16 v[44:47], v[76:79], v[48:51], 0\nv_mfma_f32_16x16x32_bf16 v[12:15], v[48:51], v[48:51], 0\nv_mfma_f32_16x16x32_bf16 v[40:43], v[28:31], v[48:51], 0\n"
"v_mfma_f32_16x16x32_bf16 v[96:99], v[32:35], v[48:51], 0\nv_mfma_f32_16x16x32_bf16 v[44:47], v[76:79], v[92:95], v[44:47]\nv_mfma_f32_16x16x32_bf16 v[12:15], v[92:95], v[92:95], v[12:15]\n"
"v_mfma_f32_16x16x32_bf16 v[40:43], v[36:39], v[92:95], v[40:43]\nv_mfma_f32_16x16x32_bf16 v[96:99], v[100:103], v[92:95], v[96:99]\nv_mfma_f32_16x16x32_bf16 v[44:47], v[76:79], v[140:143], v[44:47]\n"
"s_add_u32 s86, s76, 0xf0b000\ns_addc_u32 s87, s77, 0\nglobal_load_dwordx4 v[128:131], v0, s[86:87] offset:-4096 sc1\nglobal_load_dwordx4 v[64:67], v0, s[86:87] offset:-3072 sc1\n"
"global_load_dwordx4 v[236:239], v0, s[86:87] offset:-2048 sc1\nglobal_load_dwordx4 v[176:179], v0, s[86:87] offset:-1024 sc1\nglobal_load_dwordx4 v[184:187], v0, s[86:87] offset:0 sc1\n"
"v_mfma_f32_16x16x32_bf16 v[12:15], v[140:143], v[140:143], v[12:15]\nv_mfma_f32_16x16x32_bf16 v[40:43], v[104:107], v[140:143], v[40:43]\n"
"v_mfma_f32_16x16x32_bf16 v[96:99], v[196:199], v[140:143], v[96:99]\nv_mfma_f32_16x16x32_bf16 v[44:47], v[76:79], v[224:227], v[44:47]\n"
"v_mfma_f32_16x16x32_bf16 v[12:15], v[224:227], v[224:227], v[12:15]\nv_mfma_f32_16x16x32_bf16 v[40:43], v[108:111], v[224:227], v[40:43]\n"
"v_mfma_f32_16x16x32_bf16 v[96:99], v[112:115], v[224:227], v[96:99]\nv_mfma_f32_16x16x32_bf16 v[44:47], v[76:79], v[228:231], v[44:47]\n"
"v_mfma_f32_16x16x32_bf16 v[12:15], v[228:231], v[228:231], v[12:15]\nv_mfma_f32_16x16x32_bf16 v[40:43], v[116:119], v[228:231], v[40:43]\n"
"v_mfma_f32_16x16x32_bf16 v[96:99], v[60:63], v[228:231], v[96:99]\nds_read_b32 v10, v9 offset:4\nv_mfma_f32_16x16x32_bf16 v[68:71], v[172:175], v[48:51], 0\n"
"v_mfma_f32_16x16x32_bf16 v[144:147], v[84:87], v[48:51], 0\nv_mfma_f32_16x16x32_bf16 v[68:71], v[148:151], v[92:95], v[68:71]\ns_waitcnt lgkmcnt(0)\nv_cmp_gt_u32 vcc, 9, v10\ns_cbranch_vccnz Lms25_%=\n"
"Lmb25_%=:\nv_cndmask_b32 v45, v12, v13, s[96:97]\nv_cndmask_b32 v45, v45, v14, s[98:99]\nv_cndmask_b32 v45, v45, v15, s[100:101]\ns_mov_b64 exec, s[90:91]\nds_write_b64 v7, v[44:45] offset:512\n"
"s_mov_b64 exec, -1\nv_mfma_f32_16x16x32_bf16 v[144:147], v[244:247], v[92:95], v[144:147]\nds_write_b128 v1, v[40:43] offset:20480\nv_mfma_f32_16x16x32_bf16 v[68:71], v[248:251], v[140:143], v[68:71]\n"
"v_mov_b32 v5, 4\nds_write_b128 v1, v[96:99] offset:24576\ns_mov_b64 exec, 1\nds_write_b32 v4, v5 offset:48\ns_mov_b64 exec, -1\nv_mfma_f32_16x16x32_bf16 v[144:147], v[72:75], v[140:143], v[144:147]\n"
"v_mfma_f32_16x16x32_bf16 v[68:71], v[220:223], v[224:227], v[68:71]\nv_mfma_f32_16x16x32_bf16 v[144:147], v[160:163], v[224:227], v[144:147]\n"
"v_mfma_f32_16x16x32_bf16 v[68:71], v[156:159], v[228:231], v[68:71]\nv_mfma_f32_16x16x32_bf16 v[144:147], v[240:243], v[228:231], v[144:147]\ns_nop 5\nds_write_b128 v1, v[68:71] offset:28672\ns_nop 6\n"
"ds_write_b128 v1, v[144:147] offset:32768\ns_mov_b64 exec, 1\nds_write_b32 v4, v5 offset:64\ns_mov_b64 exec, -1\ns_waitcnt vmcnt(0)\nv_max3_u32 v8, v128, v129, v130\nv_max3_u32 v8, v8, v131, v64\n"
"v_max3_u32 v8, v8, v65, v66\nv_max3_u32 v8, v8, v67, v236\nv_max3_u32 v8, v8, v237, v238\nv_max3_u32 v8, v8, v239, v176\nv_max3_u32 v8, v8, v177, v178\nv_max3_u32 v8, v8, v179, v184\n"
"v_max3_u32 v8, v8, v185, v186\nv_max_u32 v8, v8, v187\nv_cmp_eq_u32 vcc, -1, v8\ns_cbranch_vccnz Lms26_%=\nLmb26_%=:\nv_mfma_f32_16x16x32_bf16 v[124:127], v[76:79], v[128:131], 0\n"
"v_mfma_f32_16x16x32_bf16 v[132:135], v[128:131], v[128:131], 0\nv_mfma_f32_16x16x32_bf16 v[200:203], v[28:31], v[128:131], 0\nv_mfma_f32_16x16x32_bf16 v[16:19], v[32:35], v[128:131], 0\n"
"v_mfma_f32_16x16x32_bf16 v[124:127], v[76:79], v[64:67], v[124:127]\nv_mfma_f32_16x16x32_bf16 v[132:135], v[64:67], v[64:67], v[132:135]\n"
"v_mfma_f32_16x16x32_bf16 v[200:203], v[36:39], v[64:67], v[200:203]\nv_mfma_f32_16x16x32_bf16 v[16:19], v[100:103], v[64:67], v[16:19]\n"
"v_mfma_f32_16x16x32_bf16 v[124:127], v[76:79], v[236:239], v[124:127]\ns_add_u32 s86, s76, 0xf15000\ns_addc_u32 s87, s77, 0\nglobal_load_dwordx4 v[20:23], v0, s[86:87] offset:-4096 sc1\n"
"global_load_dwordx4 v[136:139], v0, s[86:87] offset:-3072 sc1\nglobal_load_dwordx4 v[24:27], v0, s[86:87] offset:-2048 sc1\nglobal_load_dwordx4 v[232:235], v0, s[86:87] offset:-1024 sc1\n"
"global_load_dwordx4 v[204:207], v0, s[86:87] offset:0 sc1\nv_mfma_f32_16x16x32_bf16 v[132:135], v[236:239], v[236:239], v[132:135]\n"
"v_mfma_f32_16x16x32_bf16 v[200:203], v[104:107], v[236:239], v[200:203]\nv_mfma_f32_16x16x32_bf16 v[16:19], v[196:199], v[236:239], v[16:19]\n"
"v_mfma_f32_16x16x32_bf16 v[124:127], v[76:79], v[176:179], v[124:127]\nv_mfma_f32_16x16x32_bf16 v[132:135], v[176:179], v[176:179], v[132:135]\n"
"v_mfma_f32_16x16x32_bf16 v[200:203], v[108:111], v[176:179], v[200:203]\nv_mfma_f32_16x16x32_bf16 v[16:19], v[112:115], v[176:179], v[16:19]\n"
"v_mfma_f32_16x16x32_bf16 v[124:127], v[76:79], v[184:187], v[124:127]\nv_mfma_f32_16x16x32_bf16 v[132:135], v[184:187], v[184:187], v[132:135]\n"
"v_mfma_f32_16x16x32_bf16 v[200:203], v[116:119], v[184:187], v[200:203]\nv_mfma_f32_16x16x32_bf16 v[16:19], v[60:63], v[184:187], v[16:19]\nds_read_b32 v10, v9 offset:0\n"
"v_mfma_f32_16x16x32_bf16 v[208:211], v[172:175], v[128:131], 0\nv_mfma_f32_16x16x32_bf16 v[44:47], v[84:87], v[128:131], 0\nv_mfma_f32_16x16x32_bf16 v[208:211], v[148:151], v[64:67], v[208:211]\n"
"s_waitcnt lgkmcnt(0)\nv_cmp_gt_u32 vcc, 9, v10\ns_cbranch_vccnz Lms27_%=\nLmb27_%=:\nv_cndmask_b32 v125, v132, v133, s[96:97]\nv_cndmask_b32 v125, v125, v134, s[98:99]\n"
"v_cndmask_b32 v125, v125, v135, s[100:101]\ns_mov_b64 exec, s[90:91]\nds_write_b64 v7, v[124:125] offset:0\ns_mov_b64 exec, -1\nv_mfma_f32_16x16x32_bf16 v[44:47], v[244:247], v[64:67], v[44:47]\n"
"ds_write_b128 v1, v[200:203] offset:0\nv_mfma_f32_16x16x32_bf16 v[208:211], v[248:251], v[236:239], v[208:211]\nds_write_b128 v1, v[16:19] offset:4096\ns_mov_b64 exec, 1\nds_write_b32 v4, v5 offset:0\n"
"s_mov_b64 exec, -1\nv_mfma_f32_16x16x32_bf16 v[44:47], v[72:75], v[236:239], v[44:47]\nv_mfma_f32_16x16x32_bf16 v[208:211], v[220:223], v[176:179], v[208:211]\n"
"v_mfma_f32_16x16x32_bf16 v[44:47], v[160:163], v[176:179], v[44:47]\nv_mfma_f32_16x16x32_bf16 v[208:211], v[156:159], v[184:187], v[208:211]\n"
"v_mfma_f32_16x16x32_bf16 v[44:47], v[240:243], v[184:187], v[44:47]\ns_nop 5\nds_write_b128 v1, v[208:211] offset:8192\ns_nop 6\nds_write_b128 v1, v[44:47] offset:12288\ns_mov_b64 exec, 1\n"
"ds_write_b32 v4, v5 offset:16\ns_mov_b64 exec, -1\ns_waitcnt vmcnt(0)\nv_max3_u32 v8, v20, v21, v22\nv_max3_u32 v8, v8, v23, v136\nv_max3_u32 v8, v8, v137, v138\nv_max3_u32 v8, v8, v139, v24\n"
"v_max3_u32 v8, v8, v25, v26\nv_max3_u32 v8, v8, v27, v232\nv_max3_u32 v8, v8, v233, v234\nv_max3_u32 v8, v8, v235, v204\nv_max3_u32 v8, v8, v205, v206\nv_max_u32 v8, v8, v207\nv_cmp_eq_u32 vcc, -1, v8\n"
"s_cbranch_vccnz Lms28_%=\nLmb28_%=:\nv_mfma_f32_16x16x32_bf16 v[12:15], v[76:79], v[20:23], 0\nv_mfma_f32_16x16x32_bf16 v[40:43], v[20:23], v[20:23], 0\n"
"v_mfma_f32_16x16x32_bf16 v[96:99], v[28:31], v[20:23], 0\nv_mfma_f32_16x16x32_bf16 v[68:71], v[32:35], v[20:23], 0\nv_mfma_f32_16x16x32_bf16 v[12:15], v[76:79], v[136:139], v[12:15]\n"
"v_mfma_f32_16x16x32_bf16 v[40:43], v[136:139], v[136:139], v[40:43]\nv_mfma_f32_16x16x32_bf16 v[96:99], v[36:39], v[136:139], v[96:99]\n"
"v_mfma_f32_16x16x32_bf16 v[68:71], v[100:103], v[136:139], v[68:71]\nv_mfma_f32_16x16x32_bf16 v[12:15], v[76:79], v[24:27], v[12:15]\ns_add_u32 s86, s76, 0xf1a000\ns_addc_u32 s87, s77, 0\n"
"global_load_dwordx4 v[144:147], v0, s[86:87] offset:-4096 sc1\nglobal_load_dwordx4 v[124:127], v0, s[86:87] offset:-3072 sc1\nglobal_load_dwordx4 v[132:135], v0, s[86:87] offset:-2048 sc1\n"
"global_load_dwordx4 v[200:203], v0, s[86:87] offset:-1024 sc1\nglobal_load_dwordx4 v[16:19], v0, s[86:87] offset:0 sc1\nv_mfma_f32_16x16x32_bf16 v[40:43], v[24:27], v[24:27], v[40:43]\n"
"v_mfma_f32_16x16x32_bf16 v[96:99], v[104:107], v[24:27], v[96:99]\nv_mfma_f32_16x16x32_bf16 v[68:71], v[196:199], v[24:27], v[68:71]\nv_mfma_f32_16x16x32_bf16 v[12:15], v[76:79], v[232:235], v[12:15]\n"
"v_mfma_f32_16x16x32_bf16 v[40:43], v[232:235], v[232:235], v[40:43]\nv_mfma_f32_16x16x32_bf16 v[96:99], v[108:111], v[232:235], v[96:99]\n"
"v_mfma_f32_16x16x32_bf16 v[68:71], v[112:115], v[232:235], v[68:71]\nv_mfma_f32_16x16x32_bf16 v[12:15], v[76:79], v[204:207], v[12:15]\n"
"v_mfma_f32_16x16x32_bf16 v[40:43], v[204:207], v[204:207], v[40:43]\nv_mfma_f32_16x16x32_bf16 v[96:99], v[116:119], v[204:207], v[96:99]\n"
"v_mfma_f32_16x16x32_bf16 v[68:71], v[60:63], v[204:207], v[68:71]\nds_read_b32 v10, v9 offset:8\nv_mfma_f32_16x16x32_bf16 v[208:211], v[172:175], v[20:23], 0\n"
"v_mfma_f32_16x16x32_bf16 v[44:47], v[84:87], v[20:23], 0\nv_mfma_f32_16x16x32_bf16 v[208:211], v[148:151], v[136:139], v[208:211]\ns_waitcnt lgkmcnt(0)\nv_cmp_gt_u32 vcc, 9, v10\n"
"s_cbranch_vccnz Lms29_%=\nLmb29_%=:\nv_cndmask_b32 v13, v40, v41, s[96:97]\nv_cndmask_b32 v13, v13, v42, s[98:99]\nv_cndmask_b32 v13, v13, v43, s[100:101]\ns_mov_b64 exec, s[90:91]\n"
"ds_write_b64 v7, v[12:13] offset:1024\ns_mov_b64 exec, -1\nv_mfma_f32_16x16x32_bf16 v[44:47], v[244:247], v[136:139], v[44:47]\nds_write_b128 v1, v[96:99] offset:40960\n"
"v_mfma_f32_16x16x32_bf16 v[208:211], v[248:251], v[24:27], v[208:211]\nds_write_b128 v1, v[68:71] offset:45056\ns_mov_b64 exec, 1\nds_write_b32 v4, v5 offset:96\ns_mov_b64 exec, -1\n"
"v_mfma_f32_16x16x32_bf16 v[44:47], v[72:75], v[24:27], v[44:47]\nv_mfma_f32_16x16x32_bf16 v[208:211], v[220:223], v[232:235], v[208:211]\n"
"v_mfma_f32_16x16x32_bf16 v[44:47], v[160:163], v[232:235], v[44:47]\nv_mfma_f32_16x16x32_bf16 v[208:211], v[156:159], v[204:207], v[208:211]\n"
"v_mfma_f32_16x16x32_bf16 v[44:47], v[240:243], v[204:207], v[44:47]\ns_nop 5\nds_write_b128 v1, v[208:211] offset:49152\ns_nop 6\nds_write_b128 v1, v[44:47] offset:53248\ns_mov_b64 exec, 1\n"
"ds_write_b32 v4, v5 offset:112\ns_mov_b64 exec, -1\nglobal_load_dwordx4 v[12:15], v0, s[88:89] offset:-3072\nglobal_load_dwordx4 v[40:43], v0, s[88:89] offset:-2048\ns_waitcnt vmcnt(2)\n"
"v_max3_u32 v8, v144, v145, v146\nv_max3_u32 v8, v8, v147, v124\nv_max3_u32 v8, v8, v125, v126\nv_max3_u32 v8, v8, v127, v132\nv_max3_u32 v8, v8, v133, v134\nv_max3_u32 v8, v8, v135, v200\n"
"v_max3_u32 v8, v8, v201, v202\nv_max3_u32 v8, v8, v203, v16\nv_max3_u32 v8, v8, v17, v18\nv_max_u32 v8, v8, v19\nv_cmp_eq_u32 vcc, -1, v8\ns_cbranch_vccnz Lms30_%=\nLmb30_%=:\n"
"v_mfma_f32_16x16x32_bf16 v[96:99], v[76:79], v[144:147], 0\nv_mfma_f32_16x16x32_bf16 v[68:71], v[144:147], v[144:147], 0\nv_mfma_f32_16x16x32_bf16 v[208:211], v[28:31], v[144:147], 0\n"
"v_mfma_f32_16x16x32_bf16 v[44:47], v[32:35], v[144:147], 0\nv_mfma_f32_16x16x32_bf16 v[96:99], v[76:79], v[124:127], v[96:99]\nv_mfma_f32_16x16x32_bf16 v[68:71], v[124:127], v[124:127], v[68:71]\n"
"v_mfma_f32_16x16x32_bf16 v[208:211], v[36:39], v[124:127], v[208:211]\nv_mfma_f32_16x16x32_bf16 v[44:47], v[100:103], v[124:127], v[44:47]\n"
"v_mfma_f32_16x16x32_bf16 v[96:99], v[76:79], v[132:135], v[96:99]\nv_mfma_f32_16x16x32_bf16 v[68:71], v[132:135], v[132:135], v[68:71]\n"
"v_mfma_f32_16x16x32_bf16 v[208:211], v[104:107], v[132:135], v[208:211]\nv_mfma_f32_16x16x32_bf16 v[44:47], v[196:199], v[132:135], v[44:47]\n"
"v_mfma_f32_16x16x32_bf16 v[96:99], v[76:79], v[200:203], v[96:99]\nv_mfma_f32_16x16x32_bf16 v[68:71], v[200:203], v[200:203], v[68:71]\n"
"v_mfma_f32_16x16x32_bf16 v[208:211], v[108:111], v[200:203], v[208:211]\nv_mfma_f32_16x16x32_bf16 v[44:47], v[112:115], v[200:203], v[44:47]\n"
"v_mfma_f32_16x16x32_bf16 v[96:99], v[76:79], v[16:19], v[96:99]\nv_mfma_f32_16x16x32_bf16 v[68:71], v[16:19], v[16:19], v[68:71]\nv_mfma_f32_16x16x32_bf16 v[208:211], v[116:119], v[16:19], v[208:211]\n"
"v_mfma_f32_16x16x32_bf16 v[44:47], v[60:63], v[16:19], v[44:47]\nds_read_b32 v10, v9 offset:12\nv_mfma_f32_16x16x32_bf16 v[28:31], v[172:175], v[144:147], 0\n"
"v_mfma_f32_16x16x32_bf16 v[32:35], v[84:87], v[144:147], 0\nv_mfma_f32_16x16x32_bf16 v[28:31], v[148:151], v[124:127], v[28:31]\ns_waitcnt lgkmcnt(0)\nv_cmp_gt_u32 vcc, 9, v10\n"
"s_cbranch_vccnz Lms31_%=\nLmb31_%=:\nv_cndmask_b32 v97, v68, v69, s[96:97]\nv_cndmask_b32 v97, v97, v70, s[98:99]\nv_cndmask_b32 v97, v97, v71, s[100:101]\ns_mov_b64 exec, s[90:91]\n"
"ds_write_b64 v7, v[96:97] offset:1536\ns_mov_b64 exec, -1\nv_mfma_f32_16x16x32_bf16 v[32:35], v[244:247], v[124:127], v[32:35]\nds_write_b128 v1, v[208:211] offset:61440\n"
"v_mfma_f32_16x16x32_bf16 v[28:31], v[248:251], v[132:135], v[28:31]\nds_write_b128 v2, v[44:47] offset:0\ns_mov_b64 exec, 1\nds_write_b32 v4, v5 offset:144\ns_mov_b64 exec, -1\n"
"v_mfma_f32_16x16x32_bf16 v[32:35], v[72:75], v[132:135], v[32:35]\nv_mfma_f32_16x16x32_bf16 v[28:31], v[220:223], v[200:203], v[28:31]\n"
"v_mfma_f32_16x16x32_bf16 v[32:35], v[160:163], v[200:203], v[32:35]\nv_mfma_f32_16x16x32_bf16 v[28:31], v[156:159], v[16:19], v[28:31]\n"
"v_mfma_f32_16x16x32_bf16 v[32:35], v[240:243], v[16:19], v[32:35]\nglobal_load_dwordx4 v[36:39], v0, s[88:89] offset:-1024\nglobal_load_dwordx4 v[100:103], v0, s[88:89] offset:0\n"
"global_load_dwordx4 v[104:107], v0, s[88:89] offset:1024\nglobal_load_dwordx4 v[196:199], v0, s[88:89] offset:2048\nglobal_load_dwordx4 v[108:111], v0, s[88:89] offset:3072\ns_add_u32 s88, s82, 0xb000\n"
"s_addc_u32 s89, s83, 0\nglobal_load_dwordx4 v[112:115], v0, s[88:89] offset:-4096\nglobal_load_dwordx4 v[76:79], v0, s[88:89] offset:-3072\ns_waitcnt vmcnt(8)\n"
"global_load_dwordx4 v[116:119], v0, s[88:89] offset:-2048\nv_mfma_f32_16x16x32_bf16 v[60:63], v[152:155], v[48:51], 0\nv_mfma_f32_16x16x32_bf16 v[172:175], v[80:83], v[48:51], 0\n"
"v_mfma_f32_16x16x32_bf16 v[60:63], v[164:167], v[92:95], v[60:63]\nds_write_b128 v2, v[28:31] offset:4096\nv_mfma_f32_16x16x32_bf16 v[172:175], v[180:183], v[92:95], v[172:175]\n"
"ds_write_b128 v2, v[32:35] offset:8192\ns_mov_b64 exec, 1\nds_write_b32 v4, v5 offset:160\ns_mov_b64 exec, -1\nv_mfma_f32_16x16x32_bf16 v[60:63], v[52:55], v[140:143], v[60:63]\n"
"v_mfma_f32_16x16x32_bf16 v[172:175], v[212:215], v[140:143], v[172:175]\nv_mfma_f32_16x16x32_bf16 v[60:63], v[216:219], v[224:227], v[60:63]\n"
"v_mfma_f32_16x16x32_bf16 v[172:175], v[120:123], v[224:227], v[172:175]\nv_mfma_f32_16x16x32_bf16 v[60:63], v[188:191], v[228:231], v[60:63]\n"
"v_mfma_f32_16x16x32_bf16 v[172:175], v[56:59], v[228:231], v[172:175]\nv_mfma_f32_16x16x32_bf16 v[84:87], v[192:195], v[48:51], 0\nv_mfma_f32_16x16x32_bf16 v[148:151], v[88:91], v[48:51], 0\n"
"v_mfma_f32_16x16x32_bf16 v[84:87], v[168:171], v[92:95], v[84:87]\nds_write_b128 v2, v[60:63] offset:32768\nv_mfma_f32_16x16x32_bf16 v[148:151], v[12:15], v[92:95], v[148:151]\nv_mov_b32 v5, 5\n"
"ds_write_b128 v2, v[172:175] offset:36864\ns_mov_b64 exec, 1\nds_write_b32 v4, v5 offset:48\ns_mov_b64 exec, -1\ns_waitcnt vmcnt(8)\nv_mfma_f32_16x16x32_bf16 v[84:87], v[40:43], v[140:143], v[84:87]\n"
"s_waitcnt vmcnt(7)\nv_mfma_f32_16x16x32_bf16 v[148:151], v[36:39], v[140:143], v[148:151]\ns_waitcnt vmcnt(6)\nv_mfma_f32_16x16x32_bf16 v[84:87], v[100:103], v[224:227], v[84:87]\ns_waitcnt vmcnt(5)\n"
"v_mfma_f32_16x16x32_bf16 v[148:151], v[104:107], v[224:227], v[148:151]\ns_waitcnt vmcnt(4)\nv_mfma_f32_16x16x32_bf16 v[84:87], v[196:199], v[228:231], v[84:87]\ns_waitcnt vmcnt(3)\n"
"v_mfma_f32_16x16x32_bf16 v[148:151], v[108:111], v[228:231], v[148:151]\nglobal_load_dwordx4 v[96:99], v0, s[88:89] offset:-1024\nglobal_load_dwordx4 v[68:71], v0, s[88:89] offset:0\n"
"global_load_dwordx4 v[244:247], v0, s[88:89] offset:1024\nglobal_load_dwordx4 v[208:211], v0, s[88:89] offset:2048\nglobal_load_dwordx4 v[248:251], v0, s[88:89] offset:3072\ns_add_u32 s88, s82, 0xd000\n"
"s_addc_u32 s89, s83, 0\nglobal_load_dwordx4 v[44:47], v0, s[88:89] offset:-4096\ns_waitcnt vmcnt(8)\nglobal_load_dwordx4 v[72:75], v0, s[88:89] offset:-3072\ns_waitcnt vmcnt(8)\n"
"global_load_dwordx4 v[220:223], v0, s[88:89] offset:-2048\nv_mfma_f32_16x16x32_bf16 v[160:163], v[152:155], v[128:131], 0\nv_mfma_f32_16x16x32_bf16 v[156:159], v[80:83], v[128:131], 0\n"
"v_mfma_f32_16x16x32_bf16 v[160:163], v[164:167], v[64:67], v[160:163]\nds_write_b128 v2, v[84:87] offset:40960\nv_mfma_f32_16x16x32_bf16 v[156:159], v[180:183], v[64:67], v[156:159]\n"
"ds_write_b128 v2, v[148:151] offset:45056\ns_mov_b64 exec, 1\nds_write_b32 v4, v5 offset:64\ns_mov_b64 exec, -1\nv_mfma_f32_16x16x32_bf16 v[160:163], v[52:55], v[236:239], v[160:163]\n"
"v_mfma_f32_16x16x32_bf16 v[156:159], v[212:215], v[236:239], v[156:159]\nv_mfma_f32_16x16x32_bf16 v[160:163], v[216:219], v[176:179], v[160:163]\n"
"v_mfma_f32_16x16x32_bf16 v[156:159], v[120:123], v[176:179], v[156:159]\nv_mfma_f32_16x16x32_bf16 v[160:163], v[188:191], v[184:187], v[160:163]\n"
"v_mfma_f32_16x16x32_bf16 v[156:159], v[56:59], v[184:187], v[156:159]\nv_mfma_f32_16x16x32_bf16 v[240:243], v[192:195], v[128:131], 0\nv_mfma_f32_16x16x32_bf16 v[28:31], v[88:91], v[128:131], 0\n"
"v_mfma_f32_16x16x32_bf16 v[240:243], v[168:171], v[64:67], v[240:243]\nds_write_b128 v2, v[160:163] offset:16384\nv_mfma_f32_16x16x32_bf16 v[28:31], v[12:15], v[64:67], v[28:31]\n"
"ds_write_b128 v2, v[156:159] offset:20480\ns_mov_b64 exec, 1\nds_write_b32 v4, v5 offset:0\ns_mov_b64 exec, -1\nv_mfma_f32_16x16x32_bf16 v[240:243], v[40:43], v[236:239], v[240:243]\n"
"v_mfma_f32_16x16x32_bf16 v[28:31], v[36:39], v[236:239], v[28:31]\nv_mfma_f32_16x16x32_bf16 v[240:243], v[100:103], v[176:179], v[240:243]\n"
"v_mfma_f32_16x16x32_bf16 v[28:31], v[104:107], v[176:179], v[28:31]\nv_mfma_f32_16x16x32_bf16 v[240:243], v[196:199], v[184:187], v[240:243]\n"
"v_mfma_f32_16x16x32_bf16 v[28:31], v[108:111], v[184:187], v[28:31]\ns_waitcnt vmcnt(8)\nglobal_load_dwordx4 v[32:35], v0, s[88:89] offset:-1024\ns_waitcnt vmcnt(8)\n"
"global_load_dwordx4 v[60:63], v0, s[88:89] offset:0\ns_waitcnt vmcnt(8)\nglobal_load_dwordx4 v[172:175], v0, s[88:89] offset:1024\ns_waitcnt vmcnt(8)\n"
"global_load_dwordx4 v[84:87], v0, s[88:89] offset:2048\ns_waitcnt vmcnt(8)\nglobal_load_dwordx4 v[148:151], v0, s[88:89] offset:3072\nv_mfma_f32_16x16x32_bf16 v[160:163], v[152:155], v[20:23], 0\n"
"v_mfma_f32_16x16x32_bf16 v[156:159], v[80:83], v[20:23], 0\nv_mfma_f32_16x16x32_bf16 v[160:163], v[164:167], v[136:139], v[160:163]\nds_write_b128 v2, v[240:243] offset:24576\n"
"v_mfma_f32_16x16x32_bf16 v[156:159], v[180:183], v[136:139], v[156:159]\nds_write_b128 v2, v[28:31] offset:28672\ns_mov_b64 exec, 1\nds_write_b32 v4, v5 offset:16\ns_mov_b64 exec, -1\n"
"v_mfma_f32_16x16x32_bf16 v[160:163], v[52:55], v[24:27], v[160:163]\nv_mfma_f32_16x16x32_bf16 v[156:159], v[212:215], v[24:27], v[156:159]\n"
"v_mfma_f32_16x16x32_bf16 v[160:163], v[216:219], v[232:235], v[160:163]\nv_mfma_f32_16x16x32_bf16 v[156:159], v[120:123], v[232:235], v[156:159]\n"
"v_mfma_f32_16x16x32_bf16 v[160:163], v[188:191], v[204:207], v[160:163]\nv_mfma_f32_16x16x32_bf16 v[156:159], v[56:59], v[204:207], v[156:159]\n"
"v_mfma_f32_16x16x32_bf16 v[240:243], v[192:195], v[20:23], 0\nv_mfma_f32_16x16x32_bf16 v[28:31], v[88:91], v[20:23], 0\nv_mfma_f32_16x16x32_bf16 v[240:243], v[168:171], v[136:139], v[240:243]\n"
"ds_write_b128 v2, v[160:163] offset:49152\nv_mfma_f32_16x16x32_bf16 v[28:31], v[12:15], v[136:139], v[28:31]\nds_write_b128 v2, v[156:159] offset:53248\ns_mov_b64 exec, 1\n"
"ds_write_b32 v4, v5 offset:96\ns_mov_b64 exec, -1\nv_mfma_f32_16x16x32_bf16 v[240:243], v[40:43], v[24:27], v[240:243]\nv_mfma_f32_16x16x32_bf16 v[28:31], v[36:39], v[24:27], v[28:31]\n"
"v_mfma_f32_16x16x32_bf16 v[240:243], v[100:103], v[232:235], v[240:243]\nv_mfma_f32_16x16x32_bf16 v[28:31], v[104:107], v[232:235], v[28:31]\n"
"v_mfma_f32_16x16x32_bf16 v[240:243], v[196:199], v[204:207], v[240:243]\nv_mfma_f32_16x16x32_bf16 v[28:31], v[108:111], v[204:207], v[28:31]\n"
"v_mfma_f32_16x16x32_bf16 v[160:163], v[152:155], v[144:147], 0\nv_mfma_f32_16x16x32_bf16 v[156:159], v[80:83], v[144:147], 0\nv_mfma_f32_16x16x32_bf16 v[160:163], v[164:167], v[124:127], v[160:163]\n"
"ds_write_b128 v2, v[240:243] offset:57344\nv_mfma_f32_16x16x32_bf16 v[156:159], v[180:183], v[124:127], v[156:159]\nds_write_b128 v2, v[28:31] offset:61440\ns_mov_b64 exec, 1\n"
"ds_write_b32 v4, v5 offset:112\ns_mov_b64 exec, -1\nv_mfma_f32_16x16x32_bf16 v[160:163], v[52:55], v[132:135], v[160:163]\nv_mfma_f32_16x16x32_bf16 v[156:159], v[212:215], v[132:135], v[156:159]\n"
"v_mfma_f32_16x16x32_bf16 v[160:163], v[216:219], v[200:203], v[160:163]\nv_mfma_f32_16x16x32_bf16 v[156:159], v[120:123], v[200:203], v[156:159]\n"
"v_mfma_f32_16x16x32_bf16 v[160:163], v[188:191], v[16:19], v[160:163]\nv_mfma_f32_16x16x32_bf16 v[156:159], v[56:59], v[16:19], v[156:159]\n"
"v_mfma_f32_16x16x32_bf16 v[152:155], v[192:195], v[144:147], 0\nv_mfma_f32_16x16x32_bf16 v[80:83], v[88:91], v[144:147], 0\nv_mfma_f32_16x16x32_bf16 v[152:155], v[168:171], v[124:127], v[152:155]\n"
"ds_write_b128 v3, v[160:163] offset:0\nv_mfma_f32_16x16x32_bf16 v[80:83], v[12:15], v[124:127], v[80:83]\nds_write_b128 v3, v[156:159] offset:4096\ns_mov_b64 exec, 1\nds_write_b32 v4, v5 offset:144\n"
"s_mov_b64 exec, -1\nv_mfma_f32_16x16x32_bf16 v[152:155], v[40:43], v[132:135], v[152:155]\nv_mfma_f32_16x16x32_bf16 v[80:83], v[36:39], v[132:135], v[80:83]\n"
"v_mfma_f32_16x16x32_bf16 v[152:155], v[100:103], v[200:203], v[152:155]\nv_mfma_f32_16x16x32_bf16 v[80:83], v[104:107], v[200:203], v[80:83]\n"
"v_mfma_f32_16x16x32_bf16 v[152:155], v[196:199], v[16:19], v[152:155]\nv_mfma_f32_16x16x32_bf16 v[80:83], v[108:111], v[16:19], v[80:83]\ns_waitcnt vmcnt(8)\ns_add_u32 s88, s82, 0xf000\n"
"s_addc_u32 s89, s83, 0\nglobal_load_dwordx4 v[164:167], v0, s[88:89] offset:-4096\ns_waitcnt vmcnt(8)\nglobal_load_dwordx4 v[240:243], v0, s[88:89] offset:-3072\ns_waitcnt vmcnt(8)\n"
"global_load_dwordx4 v[180:183], v0, s[88:89] offset:-2048\ns_waitcnt vmcnt(8)\nglobal_load_dwordx4 v[28:31], v0, s[88:89] offset:-1024\ns_waitcnt vmcnt(8)\ns_add_u32 s88, s82, 0x1e1000\n"
"s_addc_u32 s89, s83, 0\nglobal_load_dwordx4 v[52:55], v0, s[88:89] offset:-4096\ns_waitcnt vmcnt(8)\nglobal_load_dwordx4 v[212:215], v0, s[88:89] offset:-3072\ns_waitcnt vmcnt(8)\n"
"global_load_dwordx4 v[216:219], v0, s[88:89] offset:-2048\ns_waitcnt vmcnt(8)\nglobal_load_dwordx4 v[120:123], v0, s[88:89] offset:-1024\nv_mfma_f32_16x16x32_bf16 v[188:191], v[112:115], v[48:51], 0\n"
"v_mfma_f32_16x16x32_bf16 v[56:59], v[76:79], v[48:51], 0\nv_mfma_f32_16x16x32_bf16 v[188:191], v[116:119], v[92:95], v[188:191]\nds_write_b128 v3, v[152:155] offset:8192\n"
"v_mfma_f32_16x16x32_bf16 v[56:59], v[96:99], v[92:95], v[56:59]\nds_write_b128 v3, v[80:83] offset:12288\ns_mov_b64 exec, 1\nds_write_b32 v4, v5 offset:160\ns_mov_b64 exec, -1\n"
"v_mfma_f32_16x16x32_bf16 v[188:191], v[68:71], v[140:143], v[188:191]\nv_mfma_f32_16x16x32_bf16 v[56:59], v[244:247], v[140:143], v[56:59]\n"
"v_mfma_f32_16x16x32_bf16 v[188:191], v[208:211], v[224:227], v[188:191]\nv_mfma_f32_16x16x32_bf16 v[56:59], v[248:251], v[224:227], v[56:59]\n"
"v_mfma_f32_16x16x32_bf16 v[188:191], v[44:47], v[228:231], v[188:191]\nv_mfma_f32_16x16x32_bf16 v[56:59], v[72:75], v[228:231], v[56:59]\nds_read_b32 v10, v9 offset:4\n"
"v_mfma_f32_16x16x32_bf16 v[192:195], v[220:223], v[48:51], 0\nv_mfma_f32_16x16x32_bf16 v[88:91], v[32:35], v[48:51], 0\nv_mfma_f32_16x16x32_bf16 v[192:195], v[60:63], v[92:95], v[192:195]\n"
"s_waitcnt lgkmcnt(0)\nv_cmp_gt_u32 vcc, 11, v10\ns_cbranch_vccnz Lms32_%=\nLmb32_%=:\nds_write_b128 v1, v[188:191] offset:20480\nv_mfma_f32_16x16x32_bf16 v[88:91], v[172:175], v[92:95], v[88:91]\n"
"v_mov_b32 v5, 6\nds_write_b128 v1, v[56:59] offset:24576\ns_mov_b64 exec, 1\nds_write_b32 v4, v5 offset:48\ns_mov_b64 exec, -1\nv_mfma_f32_16x16x32_bf16 v[192:195], v[84:87], v[140:143], v[192:195]\n"
"s_waitcnt vmcnt(8)\nv_mfma_f32_16x16x32_bf16 v[88:91], v[148:151], v[140:143], v[88:91]\ns_waitcnt vmcnt(7)\nv_mfma_f32_16x16x32_bf16 v[192:195], v[164:167], v[224:227], v[192:195]\ns_waitcnt vmcnt(6)\n"
"v_mfma_f32_16x16x32_bf16 v[88:91], v[240:243], v[224:227], v[88:91]\ns_waitcnt vmcnt(5)\nv_mfma_f32_16x16x32_bf16 v[192:195], v[180:183], v[228:231], v[192:195]\ns_waitcnt vmcnt(4)\n"
"v_mfma_f32_16x16x32_bf16 v[88:91], v[28:31], v[228:231], v[88:91]\nglobal_load_dwordx4 v[168:171], v0, s[88:89] offset:0\nglobal_load_dwordx4 v[160:163], v0, s[88:89] offset:1024\n"
"global_load_dwordx4 v[12:15], v0, s[88:89] offset:2048\nglobal_load_dwordx4 v[156:159], v0, s[88:89] offset:3072\ns_add_u32 s88, s82, 0x1e3000\ns_addc_u32 s89, s83, 0\n"
"global_load_dwordx4 v[40:43], v0, s[88:89] offset:-4096\ns_waitcnt vmcnt(8)\nglobal_load_dwordx4 v[36:39], v0, s[88:89] offset:-3072\ns_waitcnt vmcnt(8)\n"
"global_load_dwordx4 v[100:103], v0, s[88:89] offset:-2048\ns_waitcnt vmcnt(8)\nglobal_load_dwordx4 v[104:107], v0, s[88:89] offset:-1024\nv_mfma_f32_16x16x32_bf16 v[196:199], v[112:115], v[128:131], 0\n"
"v_mfma_f32_16x16x32_bf16 v[108:111], v[76:79], v[128:131], 0\nv_mfma_f32_16x16x32_bf16 v[196:199], v[116:119], v[64:67], v[196:199]\nds_write_b128 v1, v[192:195] offset:28672\n"
"v_mfma_f32_16x16x32_bf16 v[108:111], v[96:99], v[64:67], v[108:111]\nds_write_b128 v1, v[88:91] offset:32768\ns_mov_b64 exec, 1\nds_write_b32 v4, v5 offset:64\ns_mov_b64 exec, -1\n"
"v_mfma_f32_16x16x32_bf16 v[196:199], v[68:71], v[236:239], v[196:199]\nv_mfma_f32_16x16x32_bf16 v[108:111], v[244:247], v[236:239], v[108:111]\n"
"v_mfma_f32_16x16x32_bf16 v[196:199], v[208:211], v[176:179], v[196:199]\nv_mfma_f32_16x16x32_bf16 v[108:111], v[248:251], v[176:179], v[108:111]\n"
"v_mfma_f32_16x16x32_bf16 v[196:199], v[44:47], v[184:187], v[196:199]\nv_mfma_f32_16x16x32_bf16 v[108:111], v[72:75], v[184:187], v[108:111]\nds_read_b32 v10, v9 offset:0\n"
"v_mfma_f32_16x16x32_bf16 v[152:155], v[220:223], v[128:131], 0\nv_mfma_f32_16x16x32_bf16 v[80:83], v[32:35], v[128:131], 0\nv_mfma_f32_16x16x32_bf16 v[152:155], v[60:63], v[64:67], v[152:155]\n"
"s_waitcnt lgkmcnt(0)\nv_cmp_gt_u32 vcc, 11, v10\ns_cbranch_vccnz Lms33_%=\nLmb33_%=:\nds_write_b128 v1, v[196:199] offset:0\nv_mfma_f32_16x16x32_bf16 v[80:83], v[172:175], v[64:67], v[80:83]\n"
"ds_write_b128 v1, v[108:111] offset:4096\ns_mov_b64 exec, 1\nds_write_b32 v4, v5 offset:0\ns_mov_b64 exec, -1\nv_mfma_f32_16x16x32_bf16 v[152:155], v[84:87], v[236:239], v[152:155]\n"
"v_mfma_f32_16x16x32_bf16 v[80:83], v[148:151], v[236:239], v[80:83]\nv_mfma_f32_16x16x32_bf16 v[152:155], v[164:167], v[176:179], v[152:155]\n"
"v_mfma_f32_16x16x32_bf16 v[80:83], v[240:243], v[176:179], v[80:83]\nv_mfma_f32_16x16x32_bf16 v[152:155], v[180:183], v[184:187], v[152:155]\n"
"v_mfma_f32_16x16x32_bf16 v[80:83], v[28:31], v[184:187], v[80:83]\ns_waitcnt vmcnt(8)\nglobal_load_dwordx4 v[48:51], v0, s[88:89] offset:0\ns_waitcnt vmcnt(8)\n"
"global_load_dwordx4 v[188:191], v0, s[88:89] offset:1024\ns_waitcnt vmcnt(8)\nglobal_load_dwordx4 v[92:95], v0, s[88:89] offset:2048\ns_waitcnt vmcnt(8)\n"
"global_load_dwordx4 v[56:59], v0, s[88:89] offset:3072\ns_waitcnt vmcnt(8)\ns_add_u32 s88, s82, 0x1e5000\ns_addc_u32 s89, s83, 0\nglobal_load_dwordx4 v[140:143], v0, s[88:89] offset:-4096\n"
"s_waitcnt vmcnt(8)\nglobal_load_dwordx4 v[224:227], v0, s[88:89] offset:-3072\ns_waitcnt vmcnt(8)\nglobal_load_dwordx4 v[228:231], v0, s[88:89] offset:-2048\ns_waitcnt vmcnt(8)\n"
"global_load_dwordx4 v[192:195], v0, s[88:89] offset:-1024\nv_mfma_f32_16x16x32_bf16 v[88:91], v[112:115], v[20:23], 0\ns_add_u32 s86, s78, 0xd000\ns_addc_u32 s87, s79, 0\n"
"global_load_dwordx4 v[128:131], v0, s[86:87] offset:-4096 sc1\ns_add_u32 s86, s78, 0x1000\ns_addc_u32 s87, s79, 0\nglobal_load_dwordx4 v[196:199], v0, s[86:87] offset:-4096 sc1\n"
"s_add_u32 s86, s78, 0x19000\ns_addc_u32 s87, s79, 0\nglobal_load_dwordx4 v[64:67], v0, s[86:87] offset:-4096 sc1\ns_add_u32 s86, s78, 0x25000\ns_addc_u32 s87, s79, 0\n"
"global_load_dwordx4 v[108:111], v0, s[86:87] offset:-4096 sc1\nv_mfma_f32_16x16x32_bf16 v[236:239], v[76:79], v[20:23], 0\nv_mfma_f32_16x16x32_bf16 v[88:91], v[116:119], v[136:139], v[88:91]\n"
"ds_write_b128 v1, v[152:155] offset:8192\nv_mfma_f32_16x16x32_bf16 v[236:239], v[96:99], v[136:139], v[236:239]\nds_write_b128 v1, v[80:83] offset:12288\ns_mov_b64 exec, 1\n"
"ds_write_b32 v4, v5 offset:16\ns_mov_b64 exec, -1\nv_mfma_f32_16x16x32_bf16 v[88:91], v[68:71], v[24:27], v[88:91]\nv_mfma_f32_16x16x32_bf16 v[236:239], v[244:247], v[24:27], v[236:239]\n"
"v_mfma_f32_16x16x32_bf16 v[88:91], v[208:211], v[232:235], v[88:91]\nv_mfma_f32_16x16x32_bf16 v[236:239], v[248:251], v[232:235], v[236:239]\n"
"v_mfma_f32_16x16x32_bf16 v[88:91], v[44:47], v[204:207], v[88:91]\nv_mfma_f32_16x16x32_bf16 v[236:239], v[72:75], v[204:207], v[236:239]\nds_read_b32 v10, v9 offset:8\n"
"v_mfma_f32_16x16x32_bf16 v[176:179], v[220:223], v[20:23], 0\nv_mfma_f32_16x16x32_bf16 v[184:187], v[32:35], v[20:23], 0\nv_mfma_f32_16x16x32_bf16 v[176:179], v[60:63], v[136:139], v[176:179]\n"
"s_waitcnt lgkmcnt(0)\nv_cmp_gt_u32 vcc, 11, v10\ns_cbranch_vccnz Lms34_%=\nLmb34_%=:\nds_write_b128 v1, v[88:91] offset:40960\nv_mfma_f32_16x16x32_bf16 v[184:187], v[172:175], v[136:139], v[184:187]\n"
"ds_write_b128 v1, v[236:239] offset:45056\ns_mov_b64 exec, 1\nds_write_b32 v4, v5 offset:96\ns_mov_b64 exec, -1\nv_mfma_f32_16x16x32_bf16 v[176:179], v[84:87], v[24:27], v[176:179]\n"
"v_mfma_f32_16x16x32_bf16 v[184:187], v[148:151], v[24:27], v[184:187]\nv_mfma_f32_16x16x32_bf16 v[176:179], v[164:167], v[232:235], v[176:179]\n"
"v_mfma_f32_16x16x32_bf16 v[184:187], v[240:243], v[232:235], v[184:187]\nv_mfma_f32_16x16x32_bf16 v[176:179], v[180:183], v[204:207], v[176:179]\n"
"v_mfma_f32_16x16x32_bf16 v[184:187], v[28:31], v[204:207], v[184:187]\ns_waitcnt vmcnt(8)\nglobal_load_dwordx4 v[152:155], v0, s[88:89] offset:0\ns_waitcnt vmcnt(8)\n"
"global_load_dwordx4 v[80:83], v0, s[88:89] offset:1024\ns_waitcnt vmcnt(8)\nglobal_load_dwordx4 v[20:23], v0, s[88:89] offset:2048\nv_mfma_f32_16x16x32_bf16 v[88:91], v[112:115], v[144:147], 0\n"
"s_add_u32 s86, s78, 0xd400\ns_addc_u32 s87, s79, 0\nglobal_load_dwordx4 v[136:139], v0, s[86:87] offset:-4096 sc1\ns_add_u32 s86, s78, 0x1400\ns_addc_u32 s87, s79, 0\n"
"global_load_dwordx4 v[236:239], v0, s[86:87] offset:-4096 sc1\ns_add_u32 s86, s78, 0x19400\ns_addc_u32 s87, s79, 0\nglobal_load_dwordx4 v[24:27], v0, s[86:87] offset:-4096 sc1\n"
"s_add_u32 s86, s78, 0x25400\ns_addc_u32 s87, s79, 0\nglobal_load_dwordx4 v[232:235], v0, s[86:87] offset:-4096 sc1\nv_mfma_f32_16x16x32_bf16 v[204:207], v[76:79], v[144:147], 0\n"
"v_mfma_f32_16x16x32_bf16 v[88:91], v[116:119], v[124:127], v[88:91]\nds_write_b128 v1, v[176:179] offset:49152\nv_mfma_f32_16x16x32_bf16 v[204:207], v[96:99], v[124:127], v[204:207]\n"
"ds_write_b128 v1, v[184:187] offset:53248\ns_mov_b64 exec, 1\nds_write_b32 v4, v5 offset:112\ns_mov_b64 exec, -1\nv_mfma_f32_16x16x32_bf16 v[88:91], v[68:71], v[132:135], v[88:91]\n"
"v_mfma_f32_16x16x32_bf16 v[204:207], v[244:247], v[132:135], v[204:207]\nv_mfma_f32_16x16x32_bf16 v[88:91], v[208:211], v[200:203], v[88:91]\n"
"v_mfma_f32_16x16x32_bf16 v[204:207], v[248:251], v[200:203], v[204:207]\nv_mfma_f32_16x16x32_bf16 v[88:91], v[44:47], v[16:19], v[88:91]\n"
"v_mfma_f32_16x16x32_bf16 v[204:207], v[72:75], v[16:19], v[204:207]\nds_read_b32 v10, v9 offset:12\nv_mfma_f32_16x16x32_bf16 v[112:115], v[220:223], v[144:147], 0\n"
"v_mfma_f32_16x16x32_bf16 v[76:79], v[32:35], v[144:147], 0\nv_mfma_f32_16x16x32_bf16 v[112:115], v[60:63], v[124:127], v[112:115]\ns_waitcnt lgkmcnt(0)\nv_cmp_gt_u32 vcc, 11, v10\n"
"s_cbranch_vccnz Lms35_%=\nLmb35_%=:\nds_write_b128 v1, v[88:91] offset:61440\nv_mfma_f32_16x16x32_bf16 v[76:79], v[172:175], v[124:127], v[76:79]\nds_write_b128 v2, v[204:207] offset:0\n"
"s_mov_b64 exec, 1\nds_write_b32 v4, v5 offset:144\ns_mov_b64 exec, -1\nv_mfma_f32_16x16x32_bf16 v[112:115], v[84:87], v[132:135], v[112:115]\n"
"v_mfma_f32_16x16x32_bf16 v[76:79], v[148:151], v[132:135], v[76:79]\nv_mfma_f32_16x16x32_bf16 v[112:115], v[164:167], v[200:203], v[112:115]\n"
"v_mfma_f32_16x16x32_bf16 v[76:79], v[240:243], v[200:203], v[76:79]\nv_mfma_f32_16x16x32_bf16 v[112:115], v[180:183], v[16:19], v[112:115]\n"
"v_mfma_f32_16x16x32_bf16 v[76:79], v[28:31], v[16:19], v[76:79]\ns_nop 5\nds_write_b128 v2, v[112:115] offset:4096\ns_nop 6\nds_write_b128 v2, v[76:79] offset:8192\ns_mov_b64 exec, 1\n"
"ds_write_b32 v4, v5 offset:160\ns_mov_b64 exec, -1\ns_waitcnt vmcnt(8)\nglobal_load_dwordx4 v[116:119], v0, s[88:89] offset:3072\ns_waitcnt vmcnt(8)\ns_add_u32 s88, s82, 0x1e7000\n"
"s_addc_u32 s89, s83, 0\nglobal_load_dwordx4 v[176:179], v0, s[88:89] offset:-4096\ns_waitcnt vmcnt(8)\nglobal_load_dwordx4 v[96:99], v0, s[88:89] offset:-3072\ns_waitcnt vmcnt(8)\n"
"global_load_dwordx4 v[184:187], v0, s[88:89] offset:-2048\ns_waitcnt vmcnt(8)\nglobal_load_dwordx4 v[68:71], v0, s[88:89] offset:-1024\ns_waitcnt vmcnt(8)\n"
"global_load_dwordx4 v[244:247], v0, s[88:89] offset:0\ns_waitcnt vmcnt(8)\nglobal_load_dwordx4 v[208:211], v0, s[88:89] offset:1024\ns_waitcnt vmcnt(8)\n"
"global_load_dwordx4 v[248:251], v0, s[88:89] offset:2048\nv_max3_u32 v8, v128, v129, v130\nv_max3_u32 v8, v8, v131, v196\nv_max3_u32 v8, v8, v197, v198\nv_max3_u32 v8, v8, v199, v64\n"
"v_max3_u32 v8, v8, v65, v66\nv_max3_u32 v8, v8, v67, v108\nv_max3_u32 v8, v8, v109, v110\nv_max_u32 v8, v8, v111\nv_cmp_eq_u32 vcc, -1, v8\ns_cbranch_vccnz Lms36_%=\nLmb36_%=:\n"
"v_mfma_f32_16x16x32_bf16 v[44:47], v[52:55], v[128:131], 0\ns_add_u32 s86, s78, 0xe800\ns_addc_u32 s87, s79, 0\nglobal_load_dwordx4 v[72:75], v0, s[86:87] offset:-4096 sc1\ns_add_u32 s86, s78, 0x2800\n"
"s_addc_u32 s87, s79, 0\nglobal_load_dwordx4 v[220:223], v0, s[86:87] offset:-4096 sc1\ns_add_u32 s86, s78, 0x1a800\ns_addc_u32 s87, s79, 0\n"
"global_load_dwordx4 v[144:147], v0, s[86:87] offset:-4096 sc1\ns_add_u32 s86, s78, 0x26800\ns_addc_u32 s87, s79, 0\nglobal_load_dwordx4 v[32:35], v0, s[86:87] offset:-4096 sc1\n"
"v_mfma_f32_16x16x32_bf16 v[60:63], v[52:55], v[196:199], 0\nv_mfma_f32_16x16x32_bf16 v[88:91], v[52:55], v[64:67], 0\nv_mfma_f32_16x16x32_bf16 v[124:127], v[52:55], v[108:111], 0\n"
"v_mfma_f32_16x16x32_bf16 v[172:175], v[212:215], v[128:131], 0\nv_mfma_f32_16x16x32_bf16 v[204:207], v[212:215], v[196:199], 0\nv_mfma_f32_16x16x32_bf16 v[84:87], v[212:215], v[64:67], 0\n"
"v_mfma_f32_16x16x32_bf16 v[132:135], v[212:215], v[108:111], 0\nv_mfma_f32_16x16x32_bf16 v[148:151], v[216:219], v[128:131], 0\nv_mfma_f32_16x16x32_bf16 v[164:167], v[216:219], v[196:199], 0\n"
"v_mfma_f32_16x16x32_bf16 v[200:203], v[216:219], v[64:67], 0\nv_mfma_f32_16x16x32_bf16 v[240:243], v[216:219], v[108:111], 0\nv_mfma_f32_16x16x32_bf16 v[180:183], v[120:123], v[128:131], 0\n"
"v_mfma_f32_16x16x32_bf16 v[16:19], v[120:123], v[196:199], 0\nv_mfma_f32_16x16x32_bf16 v[28:31], v[120:123], v[64:67], 0\nv_mfma_f32_16x16x32_bf16 v[112:115], v[120:123], v[108:111], 0\n"
"v_mfma_f32_16x16x32_bf16 v[76:79], v[168:171], v[128:131], 0\nv_mfma_f32_16x16x32_bf16 v[52:55], v[168:171], v[196:199], 0\nv_mfma_f32_16x16x32_bf16 v[212:215], v[168:171], v[64:67], 0\n"
"v_mfma_f32_16x16x32_bf16 v[216:219], v[168:171], v[108:111], 0\ns_waitcnt vmcnt(8)\nglobal_load_dwordx4 v[120:123], v0, s[88:89] offset:3072\ns_waitcnt vmcnt(8)\ns_add_u32 s88, s82, 0x1e9000\n"
"s_addc_u32 s89, s83, 0\nglobal_load_dwordx4 v[128:131], v0, s[88:89] offset:-4096\nv_max3_u32 v8, v136, v137, v138\nv_max3_u32 v8, v8, v139, v236\nv_max3_u32 v8, v8, v237, v238\n"
"v_max3_u32 v8, v8, v239, v24\nv_max3_u32 v8, v8, v25, v26\nv_max3_u32 v8, v8, v27, v232\nv_max3_u32 v8, v8, v233, v234\nv_max_u32 v8, v8, v235\nv_cmp_eq_u32 vcc, -1, v8\ns_cbranch_vccnz Lms37_%=\n"
"Lmb37_%=:\nv_mfma_f32_16x16x32_bf16 v[44:47], v[160:163], v[136:139], v[44:47]\ns_add_u32 s86, s78, 0xec00\ns_addc_u32 s87, s79, 0\nglobal_load_dwordx4 v[196:199], v0, s[86:87] offset:-4096 sc1\n"
"s_add_u32 s86, s78, 0x2c00\ns_addc_u32 s87, s79, 0\nglobal_load_dwordx4 v[64:67], v0, s[86:87] offset:-4096 sc1\ns_add_u32 s86, s78, 0x1ac00\ns_addc_u32 s87, s79, 0\n"
"global_load_dwordx4 v[108:111], v0, s[86:87] offset:-4096 sc1\ns_add_u32 s86, s78, 0x26c00\ns_addc_u32 s87, s79, 0\nglobal_load_dwordx4 v[168:171], v0, s[86:87] offset:-4096 sc1\n"
"v_mfma_f32_16x16x32_bf16 v[60:63], v[160:163], v[236:239], v[60:63]\nv_mfma_f32_16x16x32_bf16 v[88:91], v[160:163], v[24:27], v[88:91]\n"
"v_mfma_f32_16x16x32_bf16 v[124:127], v[160:163], v[232:235], v[124:127]\nv_mfma_f32_16x16x32_bf16 v[172:175], v[12:15], v[136:139], v[172:175]\n"
"v_mfma_f32_16x16x32_bf16 v[204:207], v[12:15], v[236:239], v[204:207]\nv_mfma_f32_16x16x32_bf16 v[84:87], v[12:15], v[24:27], v[84:87]\n"
"v_mfma_f32_16x16x32_bf16 v[132:135], v[12:15], v[232:235], v[132:135]\nv_mfma_f32_16x16x32_bf16 v[148:151], v[156:159], v[136:139], v[148:151]\n"
"v_mfma_f32_16x16x32_bf16 v[164:167], v[156:159], v[236:239], v[164:167]\nv_mfma_f32_16x16x32_bf16 v[200:203], v[156:159], v[24:27], v[200:203]\n"
"v_mfma_f32_16x16x32_bf16 v[240:243], v[156:159], v[232:235], v[240:243]\nv_mfma_f32_16x16x32_bf16 v[180:183], v[40:43], v[136:139], v[180:183]\n"
"v_mfma_f32_16x16x32_bf16 v[16:19], v[40:43], v[236:239], v[16:19]\nv_mfma_f32_16x16x32_bf16 v[28:31], v[40:43], v[24:27], v[28:31]\n"
"v_mfma_f32_16x16x32_bf16 v[112:115], v[40:43], v[232:235], v[112:115]\nv_mfma_f32_16x16x32_bf16 v[76:79], v[36:39], v[136:139], v[76:79]\n"
"v_mfma_f32_16x16x32_bf16 v[52:55], v[36:39], v[236:239], v[52:55]\nv_mfma_f32_16x16x32_bf16 v[212:215], v[36:39], v[24:27], v[212:215]\n"
"v_mfma_f32_16x16x32_bf16 v[216:219], v[36:39], v[232:235], v[216:219]\ns_waitcnt vmcnt(8)\nglobal_load_dwordx4 v[160:163], v0, s[88:89] offset:-3072\ns_waitcnt vmcnt(8)\n"
"global_load_dwordx4 v[12:15], v0, s[88:89] offset:-2048\ns_waitcnt vmcnt(8)\nglobal_load_dwordx4 v[156:159], v0, s[88:89] offset:-1024\ns_waitcnt vmcnt(8)\n"
"global_load_dwordx4 v[40:43], v0, s[88:89] offset:0\ns_waitcnt vmcnt(8)\nglobal_load_dwordx4 v[136:139], v0, s[88:89] offset:1024\nv_max3_u32 v8, v72, v73, v74\nv_max3_u32 v8, v8, v75, v220\n"
"v_max3_u32 v8, v8, v221, v222\nv_max3_u32 v8, v8, v223, v144\nv_max3_u32 v8, v8, v145, v146\nv_max3_u32 v8, v8, v147, v32\nv_max3_u32 v8, v8, v33, v34\nv_max_u32 v8, v8, v35\nv_cmp_eq_u32 vcc, -1, v8\n"
"s_cbranch_vccnz Lms38_%=\nLmb38_%=:\nv_mfma_f32_16x16x32_bf16 v[44:47], v[100:103], v[72:75], v[44:47]\ns_add_u32 s86, s78, 0xd800\ns_addc_u32 s87, s79, 0\n"
"global_load_dwordx4 v[236:239], v0, s[86:87] offset:-4096 sc1\ns_add_u32 s86, s78, 0x1800\ns_addc_u32 s87, s79, 0\nglobal_load_dwordx4 v[24:27], v0, s[86:87] offset:-4096 sc1\n"
"s_add_u32 s86, s78, 0x19800\ns_addc_u32 s87, s79, 0\nglobal_load_dwordx4 v[232:235], v0, s[86:87] offset:-4096 sc1\ns_add_u32 s86, s78, 0x25800\ns_addc_u32 s87, s79, 0\n"
"global_load_dwordx4 v[36:39], v0, s[86:87] offset:-4096 sc1\nv_mfma_f32_16x16x32_bf16 v[60:63], v[100:103], v[220:223], v[60:63]\nv_mfma_f32_16x16x32_bf16 v[88:91], v[100:103], v[144:147], v[88:91]\n"
"v_mfma_f32_16x16x32_bf16 v[124:127], v[100:103], v[32:35], v[124:127]\nv_mfma_f32_16x16x32_bf16 v[172:175], v[104:107], v[72:75], v[172:175]\n"
"v_mfma_f32_16x16x32_bf16 v[204:207], v[104:107], v[220:223], v[204:207]\nv_mfma_f32_16x16x32_bf16 v[84:87], v[104:107], v[144:147], v[84:87]\n"
"v_mfma_f32_16x16x32_bf16 v[132:135], v[104:107], v[32:35], v[132:135]\nv_mfma_f32_16x16x32_bf16 v[148:151], v[48:51], v[72:75], v[148:151]\n"
"v_mfma_f32_16x16x32_bf16 v[164:167], v[48:51], v[220:223], v[164:167]\nv_mfma_f32_16x16x32_bf16 v[200:203], v[48:51], v[144:147], v[200:203]\n"
"v_mfma_f32_16x16x32_bf16 v[240:243], v[48:51], v[32:35], v[240:243]\nv_mfma_f32_16x16x32_bf16 v[180:183], v[188:191], v[72:75], v[180:183]\n"
"v_mfma_f32_16x16x32_bf16 v[16:19], v[188:191], v[220:223], v[16:19]\nv_mfma_f32_16x16x32_bf16 v[28:31], v[188:191], v[144:147], v[28:31]\n"
"v_mfma_f32_16x16x32_bf16 v[112:115], v[188:191], v[32:35], v[112:115]\nv_mfma_f32_16x16x32_bf16 v[76:79], v[92:95], v[72:75], v[76:79]\n"
"v_mfma_f32_16x16x32_bf16 v[52:55], v[92:95], v[220:223], v[52:55]\nv_mfma_f32_16x16x32_bf16 v[212:215], v[92:95], v[144:147], v[212:215]\n"
"v_mfma_f32_16x16x32_bf16 v[216:219], v[92:95], v[32:35], v[216:219]\ns_waitcnt vmcnt(8)\nglobal_load_dwordx4 v[100:103], v0, s[88:89] offset:2048\ns_waitcnt vmcnt(8)\n"
"global_load_dwordx4 v[104:107], v0, s[88:89] offset:3072\ns_waitcnt vmcnt(8)\ns_add_u32 s88, s82, 0x1eb000\ns_addc_u32 s89, s83, 0\nglobal_load_dwordx4 v[48:51], v0, s[88:89] offset:-4096\n"
"s_waitcnt vmcnt(8)\nglobal_load_dwordx4 v[188:191], v0, s[88:89] offset:-3072\ns_waitcnt vmcnt(8)\nglobal_load_dwordx4 v[72:75], v0, s[88:89] offset:-2048\nv_max3_u32 v8, v196, v197, v198\n"
"v_max3_u32 v8, v8, v199, v64\nv_max3_u32 v8, v8, v65, v66\nv_max3_u32 v8, v8, v67, v108\nv_max3_u32 v8, v8, v109, v110\nv_max3_u32 v8, v8, v111, v168\nv_max3_u32 v8, v8, v169, v170\n"
"v_max_u32 v8, v8, v171\nv_cmp_eq_u32 vcc, -1, v8\ns_cbranch_vccnz Lms39_%=\nLmb39_%=:\nv_mfma_f32_16x16x32_bf16 v[44:47], v[56:59], v[196:199], v[44:47]\ns_add_u32 s86, s78, 0xdc00\n"
"s_addc_u32 s87, s79, 0\nglobal_load_dwordx4 v[220:223], v0, s[86:87] offset:-4096 sc1\ns_add_u32 s86, s78, 0x1c00\ns_addc_u32 s87, s79, 0\nglobal_load_dwordx4 v[144:147], v0, s[86:87] offset:-4096 sc1\n"
"s_add_u32 s86, s78, 0x19c00\ns_addc_u32 s87, s79, 0\nglobal_load_dwordx4 v[32:35], v0, s[86:87] offset:-4096 sc1\ns_add_u32 s86, s78, 0x25c00\ns_addc_u32 s87, s79, 0\n"
"global_load_dwordx4 v[92:95], v0, s[86:87] offset:-4096 sc1\nv_mfma_f32_16x16x32_bf16 v[60:63], v[56:59], v[64:67], v[60:63]\nv_mfma_f32_16x16x32_bf16 v[88:91], v[56:59], v[108:111], v[88:91]\n"
"v_mfma_f32_16x16x32_bf16 v[124:127], v[56:59], v[168:171], v[124:127]\nv_mfma_f32_16x16x32_bf16 v[172:175], v[140:143], v[196:199], v[172:175]\n"
"v_mfma_f32_16x16x32_bf16 v[204:207], v[140:143], v[64:67], v[204:207]\nv_mfma_f32_16x16x32_bf16 v[84:87], v[140:143], v[108:111], v[84:87]\n"
"v_mfma_f32_16x16x32_bf16 v[132:135], v[140:143], v[168:171], v[132:135]\nv_mfma_f32_16x16x32_bf16 v[148:151], v[224:227], v[196:199], v[148:151]\n"
"v_mfma_f32_16x16x32_bf16 v[164:167], v[224:227], v[64:67], v[164:167]\nv_mfma_f32_16x16x32_bf16 v[200:203], v[224:227], v[108:111], v[200:203]\n"
"v_mfma_f32_16x16x32_bf16 v[240:243], v[224:227], v[168:171], v[240:243]\nv_mfma_f32_16x16x32_bf16 v[180:183], v[228:231], v[196:199], v[180:183]\n"
"v_mfma_f32_16x16x32_bf16 v[16:19], v[228:231], v[64:67], v[16:19]\nv_mfma_f32_16x16x32_bf16 v[28:31], v[228:231], v[108:111], v[28:31]\n"
"v_mfma_f32_16x16x32_bf16 v[112:115], v[228:231], v[168:171], v[112:115]\nv_mfma_f32_16x16x32_bf16 v[76:79], v[192:195], v[196:199], v[76:79]\n"
"v_mfma_f32_16x16x32_bf16 v[52:55], v[192:195], v[64:67], v[52:55]\nv_mfma_f32_16x16x32_bf16 v[212:215], v[192:195], v[108:111], v[212:215]\n"
"v_mfma_f32_16x16x32_bf16 v[216:219], v[192:195], v[168:171], v[216:219]\ns_waitcnt vmcnt(8)\nglobal_load_dwordx4 v[56:59], v0, s[88:89] offset:-1024\ns_waitcnt vmcnt(8)\n"
"global_load_dwordx4 v[140:143], v0, s[88:89] offset:0\ns_waitcnt vmcnt(8)\nglobal_load_dwordx4 v[224:227], v0, s[88:89] offset:1024\ns_waitcnt vmcnt(8)\n"
"global_load_dwordx4 v[228:231], v0, s[88:89] offset:2048\ns_waitcnt vmcnt(8)\nglobal_load_dwordx4 v[196:199], v0, s[88:89] offset:3072\nv_max3_u32 v8, v236, v237, v238\nv_max3_u32 v8, v8, v239, v24\n"
"v_max3_u32 v8, v8, v25, v26\nv_max3_u32 v8, v8, v27, v232\nv_max3_u32 v8, v8, v233, v234\nv_max3_u32 v8, v8, v235, v36\nv_max3_u32 v8, v8, v37, v38\nv_max_u32 v8, v8, v39\nv_cmp_eq_u32 vcc, -1, v8\n"
"s_cbranch_vccnz Lms40_%=\nLmb40_%=:\nv_mfma_f32_16x16x32_bf16 v[44:47], v[152:155], v[236:239], v[44:47]\ns_add_u32 s86, s78, 0xf000\ns_addc_u32 s87, s79, 0\n"
"global_load_dwordx4 v[64:67], v0, s[86:87] offset:-4096 sc1\ns_add_u32 s86, s78, 0x3000\ns_addc_u32 s87, s79, 0\nglobal_load_dwordx4 v[108:111], v0, s[86:87] offset:-4096 sc1\n"
"s_add_u32 s86, s78, 0x1b000\ns_addc_u32 s87, s79, 0\nglobal_load_dwordx4 v[168:171], v0, s[86:87] offset:-4096 sc1\ns_add_u32 s86, s78, 0x27000\ns_addc_u32 s87, s79, 0\n"
"global_load_dwordx4 v[192:195], v0, s[86:87] offset:-4096 sc1\nv_mfma_f32_16x16x32_bf16 v[60:63], v[152:155], v[24:27], v[60:63]\nv_mfma_f32_16x16x32_bf16 v[88:91], v[152:155], v[232:235], v[88:91]\n"
"v_mfma_f32_16x16x32_bf16 v[124:127], v[152:155], v[36:39], v[124:127]\nv_mfma_f32_16x16x32_bf16 v[172:175], v[80:83], v[236:239], v[172:175]\n"
"v_mfma_f32_16x16x32_bf16 v[204:207], v[80:83], v[24:27], v[204:207]\nv_mfma_f32_16x16x32_bf16 v[84:87], v[80:83], v[232:235], v[84:87]\n"
"v_mfma_f32_16x16x32_bf16 v[132:135], v[80:83], v[36:39], v[132:135]\nv_mfma_f32_16x16x32_bf16 v[148:151], v[20:23], v[236:239], v[148:151]\n"
"v_mfma_f32_16x16x32_bf16 v[164:167], v[20:23], v[24:27], v[164:167]\nv_mfma_f32_16x16x32_bf16 v[200:203], v[20:23], v[232:235], v[200:203]\n"
"v_mfma_f32_16x16x32_bf16 v[240:243], v[20:23], v[36:39], v[240:243]\nv_mfma_f32_16x16x32_bf16 v[180:183], v[116:119], v[236:239], v[180:183]\n"
"v_mfma_f32_16x16x32_bf16 v[16:19], v[116:119], v[24:27], v[16:19]\nv_mfma_f32_16x16x32_bf16 v[28:31], v[116:119], v[232:235], v[28:31]\n"
"v_mfma_f32_16x16x32_bf16 v[112:115], v[116:119], v[36:39], v[112:115]\nv_mfma_f32_16x16x32_bf16 v[76:79], v[176:179], v[236:239], v[76:79]\n"
"v_mfma_f32_16x16x32_bf16 v[52:55], v[176:179], v[24:27], v[52:55]\nv_mfma_f32_16x16x32_bf16 v[212:215], v[176:179], v[232:235], v[212:215]\n"
"v_mfma_f32_16x16x32_bf16 v[216:219], v[176:179], v[36:39], v[216:219]\ns_waitcnt vmcnt(8)\ns_add_u32 s88, s82, 0x1ed000\ns_addc_u32 s89, s83, 0\n"
"global_load_dwordx4 v[152:155], v0, s[88:89] offset:-4096\ns_waitcnt vmcnt(8)\nglobal_load_dwordx4 v[80:83], v0, s[88:89] offset:-3072\ns_waitcnt vmcnt(8)\n"
"global_load_dwordx4 v[20:23], v0, s[88:89] offset:-2048\ns_waitcnt vmcnt(8)\nglobal_load_dwordx4 v[116:119], v0, s[88:89] offset:-1024\ns_waitcnt vmcnt(8)\n"
"global_load_dwordx4 v[236:239], v0, s[88:89] offset:0\nv_max3_u32 v8, v220, v221, v222\nv_max3_u32 v8, v8, v223, v144\nv_max3_u32 v8, v8, v145, v146\nv_max3_u32 v8, v8, v147, v32\n"
"v_max3_u32 v8, v8, v33, v34\nv_max3_u32 v8, v8, v35, v92\nv_max3_u32 v8, v8, v93, v94\nv_max_u32 v8, v8, v95\nv_cmp_eq_u32 vcc, -1, v8\ns_cbranch_vccnz Lms41_%=\nLmb41_%=:\n"
"v_mfma_f32_16x16x32_bf16 v[44:47], v[96:99], v[220:223], v[44:47]\ns_add_u32 s86, s78, 0xf400\ns_addc_u32 s87, s79, 0\nglobal_load_dwordx4 v[24:27], v0, s[86:87] offset:-4096 sc1\n"
"s_add_u32 s86, s78, 0x3400\ns_addc_u32 s87, s79, 0\nglobal_load_dwordx4 v[232:235], v0, s[86:87] offset:-4096 sc1\ns_add_u32 s86, s78, 0x1b400\ns_addc_u32 s87, s79, 0\n"
"global_load_dwordx4 v[36:39], v0, s[86:87] offset:-4096 sc1\ns_add_u32 s86, s78, 0x27400\ns_addc_u32 s87, s79, 0\nglobal_load_dwordx4 v[176:179], v0, s[86:87] offset:-4096 sc1\n"
"v_mfma_f32_16x16x32_bf16 v[60:63], v[96:99], v[144:147], v[60:63]\nv_mfma_f32_16x16x32_bf16 v[88:91], v[96:99], v[32:35], v[88:91]\nv_mfma_f32_16x16x32_bf16 v[124:127], v[96:99], v[92:95], v[124:127]\n"
"v_mfma_f32_16x16x32_bf16 v[172:175], v[184:187], v[220:223], v[172:175]\nv_mfma_f32_16x16x32_bf16 v[204:207], v[184:187], v[144:147], v[204:207]\n"
"v_mfma_f32_16x16x32_bf16 v[84:87], v[184:187], v[32:35], v[84:87]\nv_mfma_f32_16x16x32_bf16 v[132:135], v[184:187], v[92:95], v[132:135]\n"
"v_mfma_f32_16x16x32_bf16 v[148:151], v[68:71], v[220:223], v[148:151]\nv_mfma_f32_16x16x32_bf16 v[164:167], v[68:71], v[144:147], v[164:167]\n"
"v_mfma_f32_16x16x32_bf16 v[200:203], v[68:71], v[32:35], v[200:203]\nv_mfma_f32_16x16x32_bf16 v[240:243], v[68:71], v[92:95], v[240:243]\n"
"v_mfma_f32_16x16x32_bf16 v[180:183], v[244:247], v[220:223], v[180:183]\nv_mfma_f32_16x16x32_bf16 v[16:19], v[244:247], v[144:147], v[16:19]\n"
"v_mfma_f32_16x16x32_bf16 v[28:31], v[244:247], v[32:35], v[28:31]\nv_mfma_f32_16x16x32_bf16 v[112:115], v[244:247], v[92:95], v[112:115]\n"
"v_mfma_f32_16x16x32_bf16 v[76:79], v[208:211], v[220:223], v[76:79]\nv_mfma_f32_16x16x32_bf16 v[52:55], v[208:211], v[144:147], v[52:55]\n"
"v_mfma_f32_16x16x32_bf16 v[212:215], v[208:211], v[32:35], v[212:215]\nv_mfma_f32_16x16x32_bf16 v[216:219], v[208:211], v[92:95], v[216:219]\ns_waitcnt vmcnt(8)\n"
"global_load_dwordx4 v[96:99], v0, s[88:89] offset:1024\ns_waitcnt vmcnt(8)\nglobal_load_dwordx4 v[184:187], v0, s[88:89] offset:2048\ns_waitcnt vmcnt(8)\n"
"global_load_dwordx4 v[68:71], v0, s[88:89] offset:3072\ns_waitcnt vmcnt(8)\ns_add_u32 s88, s82, 0x1ef000\ns_addc_u32 s89, s83, 0\nglobal_load_dwordx4 v[244:247], v0, s[88:89] offset:-4096\n"
"s_waitcnt vmcnt(8)\nglobal_load_dwordx4 v[220:223], v0, s[88:89] offset:-3072\nv_max3_u32 v8, v64, v65, v66\nv_max3_u32 v8, v8, v67, v108\nv_max3_u32 v8, v8, v109, v110\nv_max3_u32 v8, v8, v111, v168\n"
"v_max3_u32 v8, v8, v169, v170\nv_max3_u32 v8, v8, v171, v192\nv_max3_u32 v8, v8, v193, v194\nv_max_u32 v8, v8, v195\nv_cmp_eq_u32 vcc, -1, v8\ns_cbranch_vccnz Lms42_%=\nLmb42_%=:\n"
"v_mfma_f32_16x16x32_bf16 v[44:47], v[248:251], v[64:67], v[44:47]\ns_add_u32 s86, s78, 0xe000\ns_addc_u32 s87, s79, 0\nglobal_load_dwordx4 v[144:147], v0, s[86:87] offset:-4096 sc1\n"
"global_load_dwordx4 v[32:35], v0, s[86:87] offset:-3072 sc1\nglobal_load_dwordx4 v[92:95], v0, s[86:87] offset:2048 sc1\nglobal_load_dwordx4 v[208:211], v0, s[86:87] offset:3072 sc1\n"
"v_mfma_f32_16x16x32_bf16 v[60:63], v[248:251], v[108:111], v[60:63]\nv_mfma_f32_16x16x32_bf16 v[88:91], v[248:251], v[168:171], v[88:91]\n"
"v_mfma_f32_16x16x32_bf16 v[124:127], v[248:251], v[192:195], v[124:127]\nv_mfma_f32_16x16x32_bf16 v[172:175], v[120:123], v[64:67], v[172:175]\n"
"v_mfma_f32_16x16x32_bf16 v[204:207], v[120:123], v[108:111], v[204:207]\nv_mfma_f32_16x16x32_bf16 v[84:87], v[120:123], v[168:171], v[84:87]\n"
"v_mfma_f32_16x16x32_bf16 v[132:135], v[120:123], v[192:195], v[132:135]\nv_mfma_f32_16x16x32_bf16 v[148:151], v[128:131], v[64:67], v[148:151]\n"
"v_mfma_f32_16x16x32_bf16 v[164:167], v[128:131], v[108:111], v[164:167]\nv_mfma_f32_16x16x32_bf16 v[200:203], v[128:131], v[168:171], v[200:203]\n"
"v_mfma_f32_16x16x32_bf16 v[240:243], v[128:131], v[192:195], v[240:243]\nv_mfma_f32_16x16x32_bf16 v[180:183], v[160:163], v[64:67], v[180:183]\n"
"v_mfma_f32_16x16x32_bf16 v[16:19], v[160:163], v[108:111], v[16:19]\nv_mfma_f32_16x16x32_bf16 v[28:31], v[160:163], v[168:171], v[28:31]\n"
"v_mfma_f32_16x16x32_bf16 v[112:115], v[160:163], v[192:195], v[112:115]\nv_mfma_f32_16x16x32_bf16 v[76:79], v[12:15], v[64:67], v[76:79]\n"
"v_mfma_f32_16x16x32_bf16 v[52:55], v[12:15], v[108:111], v[52:55]\nv_mfma_f32_16x16x32_bf16 v[212:215], v[12:15], v[168:171], v[212:215]\n"
"v_mfma_f32_16x16x32_bf16 v[216:219], v[12:15], v[192:195], v[216:219]\ns_waitcnt vmcnt(8)\nglobal_load_dwordx4 v[248:251], v0, s[88:89] offset:-2048\ns_waitcnt vmcnt(8)\n"
"global_load_dwordx4 v[120:123], v0, s[88:89] offset:-1024\nv_max3_u32 v8, v24, v25, v26\nv_max3_u32 v8, v8, v27, v232\nv_max3_u32 v8, v8, v233, v234\nv_max3_u32 v8, v8, v235, v36\n"
"v_max3_u32 v8, v8, v37, v38\nv_max3_u32 v8, v8, v39, v176\nv_max3_u32 v8, v8, v177, v178\nv_max_u32 v8, v8, v179\nv_cmp_eq_u32 vcc, -1, v8\ns_cbranch_vccnz Lms43_%=\nLmb43_%=:\n"
"v_mfma_f32_16x16x32_bf16 v[44:47], v[156:159], v[24:27], v[44:47]\ns_add_u32 s86, s78, 0x2000\ns_addc_u32 s87, s79, 0\nglobal_load_dwordx4 v[128:131], v0, s[86:87] offset:-4096 sc1\n"
"global_load_dwordx4 v[160:163], v0, s[86:87] offset:-3072 sc1\nglobal_load_dwordx4 v[64:67], v0, s[86:87] offset:2048 sc1\nglobal_load_dwordx4 v[108:111], v0, s[86:87] offset:3072 sc1\n"
"v_mfma_f32_16x16x32_bf16 v[60:63], v[156:159], v[232:235], v[60:63]\nv_mfma_f32_16x16x32_bf16 v[88:91], v[156:159], v[36:39], v[88:91]\n"
"v_mfma_f32_16x16x32_bf16 v[124:127], v[156:159], v[176:179], v[124:127]\nv_mfma_f32_16x16x32_bf16 v[172:175], v[40:43], v[24:27], v[172:175]\n"
"v_mfma_f32_16x16x32_bf16 v[204:207], v[40:43], v[232:235], v[204:207]\nv_mfma_f32_16x16x32_bf16 v[84:87], v[40:43], v[36:39], v[84:87]\n"
"v_mfma_f32_16x16x32_bf16 v[132:135], v[40:43], v[176:179], v[132:135]\nv_mfma_f32_16x16x32_bf16 v[148:151], v[136:139], v[24:27], v[148:151]\n"
"v_mfma_f32_16x16x32_bf16 v[164:167], v[136:139], v[232:235], v[164:167]\nv_mfma_f32_16x16x32_bf16 v[200:203], v[136:139], v[36:39], v[200:203]\n"
"v_mfma_f32_16x16x32_bf16 v[240:243], v[136:139], v[176:179], v[240:243]\nv_mfma_f32_16x16x32_bf16 v[180:183], v[100:103], v[24:27], v[180:183]\n"
"v_mfma_f32_16x16x32_bf16 v[16:19], v[100:103], v[232:235], v[16:19]\nv_mfma_f32_16x16x32_bf16 v[28:31], v[100:103], v[36:39], v[28:31]\n"
"v_mfma_f32_16x16x32_bf16 v[112:115], v[100:103], v[176:179], v[112:115]\nv_mfma_f32_16x16x32_bf16 v[76:79], v[104:107], v[24:27], v[76:79]\n"
"v_mfma_f32_16x16x32_bf16 v[52:55], v[104:107], v[232:235], v[52:55]\nv_mfma_f32_16x16x32_bf16 v[212:215], v[104:107], v[36:39], v[212:215]\n"
"v_mfma_f32_16x16x32_bf16 v[216:219], v[104:107], v[176:179], v[216:219]\ns_waitcnt vmcnt(6)\nv_max3_u32 v8, v144, v145, v146\nv_max3_u32 v8, v8, v147, v32\nv_max3_u32 v8, v8, v33, v34\n"
"v_max3_u32 v8, v8, v35, v92\nv_max3_u32 v8, v8, v93, v94\nv_max3_u32 v8, v8, v95, v208\nv_max3_u32 v8, v8, v209, v210\nv_max_u32 v8, v8, v211\nv_cmp_eq_u32 vcc, -1, v8\ns_cbranch_vccnz Lms44_%=\n"
"Lmb44_%=:\nv_mfma_f32_16x16x32_bf16 v[44:47], v[48:51], v[144:147], v[44:47]\ns_add_u32 s86, s78, 0x1a000\ns_addc_u32 s87, s79, 0\nglobal_load_dwordx4 v[168:171], v0, s[86:87] offset:-4096 sc1\n"
"global_load_dwordx4 v[192:195], v0, s[86:87] offset:-3072 sc1\nglobal_load_dwordx4 v[12:15], v0, s[86:87] offset:2048 sc1\nglobal_load_dwordx4 v[156:159], v0, s[86:87] offset:3072 sc1\n"
"v_mfma_f32_16x16x32_bf16 v[172:175], v[188:191], v[144:147], v[172:175]\nv_mfma_f32_16x16x32_bf16 v[148:151], v[152:155], v[144:147], v[148:151]\n"
"v_mfma_f32_16x16x32_bf16 v[180:183], v[80:83], v[144:147], v[180:183]\nv_mfma_f32_16x16x32_bf16 v[76:79], v[20:23], v[144:147], v[76:79]\n"
"v_mfma_f32_16x16x32_bf16 v[44:47], v[72:75], v[32:35], v[44:47]\nv_mfma_f32_16x16x32_bf16 v[172:175], v[56:59], v[32:35], v[172:175]\n"
"v_mfma_f32_16x16x32_bf16 v[148:151], v[116:119], v[32:35], v[148:151]\nv_mfma_f32_16x16x32_bf16 v[180:183], v[236:239], v[32:35], v[180:183]\n"
"v_mfma_f32_16x16x32_bf16 v[76:79], v[96:99], v[32:35], v[76:79]\nv_mfma_f32_16x16x32_bf16 v[44:47], v[140:143], v[92:95], v[44:47]\n"
"v_mfma_f32_16x16x32_bf16 v[172:175], v[224:227], v[92:95], v[172:175]\nv_mfma_f32_16x16x32_bf16 v[148:151], v[184:187], v[92:95], v[148:151]\n"
"v_mfma_f32_16x16x32_bf16 v[180:183], v[68:71], v[92:95], v[180:183]\nv_mfma_f32_16x16x32_bf16 v[76:79], v[244:247], v[92:95], v[76:79]\n"
"v_mfma_f32_16x16x32_bf16 v[44:47], v[228:231], v[208:211], v[44:47]\nds_read_b32 v10, v9 offset:4\nv_mfma_f32_16x16x32_bf16 v[172:175], v[196:199], v[208:211], v[172:175]\n"
"v_mfma_f32_16x16x32_bf16 v[148:151], v[220:223], v[208:211], v[148:151]\ns_waitcnt vmcnt(9)\nv_mfma_f32_16x16x32_bf16 v[180:183], v[248:251], v[208:211], v[180:183]\ns_waitcnt lgkmcnt(0)\n"
"v_cmp_gt_u32 vcc, 13, v10\ns_cbranch_vccnz Lms45_%=\nLmb45_%=:\nds_write_b128 v1, v[44:47] offset:20480\ns_waitcnt vmcnt(8)\nv_mfma_f32_16x16x32_bf16 v[76:79], v[120:123], v[208:211], v[76:79]\n"
"v_mov_b32 v5, 7\nds_write_b128 v1, v[172:175] offset:24576\ns_mov_b64 exec, 1\nds_write_b32 v4, v5 offset:48\ns_mov_b64 exec, -1\ns_waitcnt vmcnt(4)\nv_max3_u32 v8, v128, v129, v130\n"
"v_max3_u32 v8, v8, v131, v160\nv_max3_u32 v8, v8, v161, v162\nv_max3_u32 v8, v8, v163, v64\nv_max3_u32 v8, v8, v65, v66\nv_max3_u32 v8, v8, v67, v108\nv_max3_u32 v8, v8, v109, v110\n"
"v_max_u32 v8, v8, v111\nv_cmp_eq_u32 vcc, -1, v8\ns_cbranch_vccnz Lms46_%=\nLmb46_%=:\nv_mfma_f32_16x16x32_bf16 v[60:63], v[48:51], v[128:131], v[60:63]\ns_add_u32 s86, s78, 0x26000\n"
"s_addc_u32 s87, s79, 0\nglobal_load_dwordx4 v[40:43], v0, s[86:87] offset:-4096 sc1\nglobal_load_dwordx4 v[136:139], v0, s[86:87] offset:-3072 sc1\n"
"global_load_dwordx4 v[100:103], v0, s[86:87] offset:2048 sc1\nglobal_load_dwordx4 v[24:27], v0, s[86:87] offset:3072 sc1\nv_mfma_f32_16x16x32_bf16 v[204:207], v[188:191], v[128:131], v[204:207]\n"
"v_mfma_f32_16x16x32_bf16 v[164:167], v[152:155], v[128:131], v[164:167]\nds_write_b128 v1, v[148:151] offset:28672\nv_mfma_f32_16x16x32_bf16 v[16:19], v[80:83], v[128:131], v[16:19]\n"
"ds_write_b128 v1, v[180:183] offset:32768\ns_mov_b64 exec, 1\nds_write_b32 v4, v5 offset:64\ns_mov_b64 exec, -1\nv_mfma_f32_16x16x32_bf16 v[52:55], v[20:23], v[128:131], v[52:55]\n"
"ds_write_b128 v1, v[76:79] offset:36864\ns_mov_b64 exec, 1\nds_write_b32 v4, v5 offset:80\ns_mov_b64 exec, -1\nv_mfma_f32_16x16x32_bf16 v[60:63], v[72:75], v[160:163], v[60:63]\n"
"v_mfma_f32_16x16x32_bf16 v[204:207], v[56:59], v[160:163], v[204:207]\nv_mfma_f32_16x16x32_bf16 v[164:167], v[116:119], v[160:163], v[164:167]\n"
"v_mfma_f32_16x16x32_bf16 v[16:19], v[236:239], v[160:163], v[16:19]\nv_mfma_f32_16x16x32_bf16 v[52:55], v[96:99], v[160:163], v[52:55]\n"
"v_mfma_f32_16x16x32_bf16 v[60:63], v[140:143], v[64:67], v[60:63]\nv_mfma_f32_16x16x32_bf16 v[204:207], v[224:227], v[64:67], v[204:207]\n"
"v_mfma_f32_16x16x32_bf16 v[164:167], v[184:187], v[64:67], v[164:167]\nv_mfma_f32_16x16x32_bf16 v[16:19], v[68:71], v[64:67], v[16:19]\n"
"v_mfma_f32_16x16x32_bf16 v[52:55], v[244:247], v[64:67], v[52:55]\nv_mfma_f32_16x16x32_bf16 v[60:63], v[228:231], v[108:111], v[60:63]\nds_read_b32 v10, v9 offset:0\n"
"v_mfma_f32_16x16x32_bf16 v[204:207], v[196:199], v[108:111], v[204:207]\nv_mfma_f32_16x16x32_bf16 v[164:167], v[220:223], v[108:111], v[164:167]\n"
"v_mfma_f32_16x16x32_bf16 v[16:19], v[248:251], v[108:111], v[16:19]\ns_waitcnt lgkmcnt(0)\nv_cmp_gt_u32 vcc, 13, v10\ns_cbranch_vccnz Lms47_%=\nLmb47_%=:\nds_write_b128 v1, v[60:63] offset:0\n"
"v_mfma_f32_16x16x32_bf16 v[52:55], v[120:123], v[108:111], v[52:55]\nds_write_b128 v1, v[204:207] offset:4096\ns_mov_b64 exec, 1\nds_write_b32 v4, v5 offset:0\ns_mov_b64 exec, -1\ns_waitcnt vmcnt(4)\n"
"v_max3_u32 v8, v168, v169, v170\nv_max3_u32 v8, v8, v171, v192\nv_max3_u32 v8, v8, v193, v194\nv_max3_u32 v8, v8, v195, v12\nv_max3_u32 v8, v8, v13, v14\nv_max3_u32 v8, v8, v15, v156\n"
"v_max3_u32 v8, v8, v157, v158\nv_max_u32 v8, v8, v159\nv_cmp_eq_u32 vcc, -1, v8\ns_cbranch_vccnz Lms48_%=\nLmb48_%=:\nv_mfma_f32_16x16x32_bf16 v[88:91], v[48:51], v[168:171], v[88:91]\n"
"v_mfma_f32_16x16x32_bf16 v[84:87], v[188:191], v[168:171], v[84:87]\nv_mfma_f32_16x16x32_bf16 v[200:203], v[152:155], v[168:171], v[200:203]\nds_write_b128 v1, v[164:167] offset:8192\n"
"v_mfma_f32_16x16x32_bf16 v[28:31], v[80:83], v[168:171], v[28:31]\nds_write_b128 v1, v[16:19] offset:12288\ns_mov_b64 exec, 1\nds_write_b32 v4, v5 offset:16\ns_mov_b64 exec, -1\n"
"v_mfma_f32_16x16x32_bf16 v[212:215], v[20:23], v[168:171], v[212:215]\nds_write_b128 v1, v[52:55] offset:16384\ns_mov_b64 exec, 1\nds_write_b32 v4, v5 offset:32\ns_mov_b64 exec, -1\n"
"v_mfma_f32_16x16x32_bf16 v[88:91], v[72:75], v[192:195], v[88:91]\nv_mfma_f32_16x16x32_bf16 v[84:87], v[56:59], v[192:195], v[84:87]\n"
"v_mfma_f32_16x16x32_bf16 v[200:203], v[116:119], v[192:195], v[200:203]\nv_mfma_f32_16x16x32_bf16 v[28:31], v[236:239], v[192:195], v[28:31]\n"
"v_mfma_f32_16x16x32_bf16 v[212:215], v[96:99], v[192:195], v[212:215]\nv_mfma_f32_16x16x32_bf16 v[88:91], v[140:143], v[12:15], v[88:91]\n"
"v_mfma_f32_16x16x32_bf16 v[84:87], v[224:227], v[12:15], v[84:87]\nv_mfma_f32_16x16x32_bf16 v[200:203], v[184:187], v[12:15], v[200:203]\n"
"v_mfma_f32_16x16x32_bf16 v[28:31], v[68:71], v[12:15], v[28:31]\nv_mfma_f32_16x16x32_bf16 v[212:215], v[244:247], v[12:15], v[212:215]\n"
"v_mfma_f32_16x16x32_bf16 v[88:91], v[228:231], v[156:159], v[88:91]\nds_read_b32 v10, v9 offset:8\nv_mfma_f32_16x16x32_bf16 v[84:87], v[196:199], v[156:159], v[84:87]\n"
"v_mfma_f32_16x16x32_bf16 v[200:203], v[220:223], v[156:159], v[200:203]\nv_mfma_f32_16x16x32_bf16 v[28:31], v[248:251], v[156:159], v[28:31]\ns_waitcnt lgkmcnt(0)\nv_cmp_gt_u32 vcc, 13, v10\n"
"s_cbranch_vccnz Lms49_%=\nLmb49_%=:\nds_write_b128 v1, v[88:91] offset:40960\nv_mfma_f32_16x16x32_bf16 v[212:215], v[120:123], v[156:159], v[212:215]\nds_write_b128 v1, v[84:87] offset:45056\n"
"s_mov_b64 exec, 1\nds_write_b32 v4, v5 offset:96\ns_mov_b64 exec, -1\ns_waitcnt vmcnt(0)\nv_max3_u32 v8, v40, v41, v42\nv_max3_u32 v8, v8, v43, v136\nv_max3_u32 v8, v8, v137, v138\n"
"v_max3_u32 v8, v8, v139, v100\nv_max3_u32 v8, v8, v101, v102\nv_max3_u32 v8, v8, v103, v24\nv_max3_u32 v8, v8, v25, v26\nv_max_u32 v8, v8, v27\nv_cmp_eq_u32 vcc, -1, v8\ns_cbranch_vccnz Lms50_%=\n"
"Lmb50_%=:\nv_mfma_f32_16x16x32_bf16 v[124:127], v[48:51], v[40:43], v[124:127]\nv_mfma_f32_16x16x32_bf16 v[132:135], v[188:191], v[40:43], v[132:135]\n"
"v_mfma_f32_16x16x32_bf16 v[240:243], v[152:155], v[40:43], v[240:243]\nds_write_b128 v1, v[200:203] offset:49152\nv_mfma_f32_16x16x32_bf16 v[112:115], v[80:83], v[40:43], v[112:115]\n"
"ds_write_b128 v1, v[28:31] offset:53248\ns_mov_b64 exec, 1\nds_write_b32 v4, v5 offset:112\ns_mov_b64 exec, -1\nv_mfma_f32_16x16x32_bf16 v[216:219], v[20:23], v[40:43], v[216:219]\n"
"ds_write_b128 v1, v[212:215] offset:57344\ns_mov_b64 exec, 1\nds_write_b32 v4, v5 offset:128\ns_mov_b64 exec, -1\nv_mfma_f32_16x16x32_bf16 v[124:127], v[72:75], v[136:139], v[124:127]\n"
"v_mfma_f32_16x16x32_bf16 v[132:135], v[56:59], v[136:139], v[132:135]\nv_mfma_f32_16x16x32_bf16 v[240:243], v[116:119], v[136:139], v[240:243]\n"
"v_mfma_f32_16x16x32_bf16 v[112:115], v[236:239], v[136:139], v[112:115]\nv_mfma_f32_16x16x32_bf16 v[216:219], v[96:99], v[136:139], v[216:219]\n"
"v_mfma_f32_16x16x32_bf16 v[124:127], v[140:143], v[100:103], v[124:127]\nv_mfma_f32_16x16x32_bf16 v[132:135], v[224:227], v[100:103], v[132:135]\n"
"v_mfma_f32_16x16x32_bf16 v[240:243], v[184:187], v[100:103], v[240:243]\nv_mfma_f32_16x16x32_bf16 v[112:115], v[68:71], v[100:103], v[112:115]\n"
"v_mfma_f32_16x16x32_bf16 v[216:219], v[244:247], v[100:103], v[216:219]\nv_mfma_f32_16x16x32_bf16 v[124:127], v[228:231], v[24:27], v[124:127]\nds_read_b32 v10, v9 offset:12\n"
"v_mfma_f32_16x16x32_bf16 v[132:135], v[196:199], v[24:27], v[132:135]\nv_mfma_f32_16x16x32_bf16 v[240:243], v[220:223], v[24:27], v[240:243]\n"
"v_mfma_f32_16x16x32_bf16 v[112:115], v[248:251], v[24:27], v[112:115]\ns_waitcnt lgkmcnt(0)\nv_cmp_gt_u32 vcc, 13, v10\ns_cbranch_vccnz Lms51_%=\nLmb51_%=:\nds_write_b128 v1, v[124:127] offset:61440\n"
"v_mfma_f32_16x16x32_bf16 v[216:219], v[120:123], v[24:27], v[216:219]\nds_write_b128 v2, v[132:135] offset:0\ns_mov_b64 exec, 1\nds_write_b32 v4, v5 offset:144\ns_mov_b64 exec, -1\n"
"ds_write_b128 v2, v[240:243] offset:4096\nds_write_b128 v2, v[112:115] offset:8192\ns_mov_b64 exec, 1\nds_write_b32 v4, v5 offset:160\ns_mov_b64 exec, -1\nds_write_b128 v2, v[216:219] offset:12288\n"
"s_mov_b64 exec, 1\nds_write_b32 v4, v5 offset:176\ns_mov_b64 exec, -1\ns_branch Lmend_%=\nLms2_%=:\ns_sub_u32 s94, s94, 1\ns_cbranch_scc1 Lmg2_%=\ns_add_u32 s92, s76, 0x781000\ns_addc_u32 s93, s77, 0\n"
"global_load_dwordx4 v[40:43], v0, s[92:93] offset:-4096 sc1\nglobal_load_dwordx4 v[44:47], v0, s[92:93] offset:-3072 sc1\nglobal_load_dwordx4 v[48:51], v0, s[92:93] offset:-2048 sc1\n"
"global_load_dwordx4 v[52:55], v0, s[92:93] offset:-1024 sc1\nglobal_load_dwordx2 v[56:57], v252, s[92:93] offset:0 sc1\nglobal_load_dwordx2 v[58:59], v252, s[92:93] offset:128 sc1\ns_waitcnt vmcnt(0)\n"
"v_max3_u32 v8, v40, v41, v42\nv_max3_u32 v8, v8, v43, v44\nv_max3_u32 v8, v8, v45, v46\nv_max3_u32 v8, v8, v47, v48\nv_max3_u32 v8, v8, v49, v50\nv_max3_u32 v8, v8, v51, v52\n"
"v_max3_u32 v8, v8, v53, v54\nv_max3_u32 v8, v8, v55, v56\nv_max3_u32 v8, v8, v57, v58\nv_max_u32 v8, v8, v59\nv_cmp_eq_u32 vcc, -1, v8\ns_cbranch_vccnz Lms2_%=\ns_branch Lmb2_%=\nLmg2_%=:\n"
"s_mov_b32 s94, 0\ns_branch Lmb2_%=\nLms3_%=:\ns_sub_u32 s94, s94, 1\ns_cbranch_scc1 Lmg3_%=\ns_add_u32 s92, s76, 0x786000\ns_addc_u32 s93, s77, 0\n"
"global_load_dwordx4 v[64:67], v0, s[92:93] offset:-4096 sc1\nglobal_load_dwordx4 v[68:71], v0, s[92:93] offset:-3072 sc1\nglobal_load_dwordx4 v[72:75], v0, s[92:93] offset:-2048 sc1\n"
"global_load_dwordx4 v[76:79], v0, s[92:93] offset:-1024 sc1\nglobal_load_dwordx2 v[80:81], v252, s[92:93] offset:0 sc1\nglobal_load_dwordx2 v[82:83], v252, s[92:93] offset:128 sc1\ns_waitcnt vmcnt(0)\n"
"v_max3_u32 v8, v64, v65, v66\nv_max3_u32 v8, v8, v67, v68\nv_max3_u32 v8, v8, v69, v70\nv_max3_u32 v8, v8, v71, v72\nv_max3_u32 v8, v8, v73, v74\nv_max3_u32 v8, v8, v75, v76\n"
"v_max3_u32 v8, v8, v77, v78\nv_max3_u32 v8, v8, v79, v80\nv_max3_u32 v8, v8, v81, v82\nv_max_u32 v8, v8, v83\nv_cmp_eq_u32 vcc, -1, v8\ns_cbranch_vccnz Lms3_%=\ns_branch Lmb3_%=\nLmg3_%=:\n"
"s_mov_b32 s94, 0\ns_branch Lmb3_%=\nLms4_%=:\ns_sub_u32 s94, s94, 1\ns_cbranch_scc1 Lmg4_%=\ns_add_u32 s92, s76, 0x78b000\ns_addc_u32 s93, s77, 0\n"
"global_load_dwordx4 v[140:143], v0, s[92:93] offset:-4096 sc1\nglobal_load_dwordx4 v[144:147], v0, s[92:93] offset:-3072 sc1\nglobal_load_dwordx4 v[148:151], v0, s[92:93] offset:-2048 sc1\n"
"global_load_dwordx4 v[152:155], v0, s[92:93] offset:-1024 sc1\nglobal_load_dwordx2 v[40:41], v252, s[92:93] offset:0 sc1\nglobal_load_dwordx2 v[42:43], v252, s[92:93] offset:128 sc1\n"
"s_waitcnt vmcnt(0)\nv_max3_u32 v8, v140, v141, v142\nv_max3_u32 v8, v8, v143, v144\nv_max3_u32 v8, v8, v145, v146\nv_max3_u32 v8, v8, v147, v148\nv_max3_u32 v8, v8, v149, v150\n"
"v_max3_u32 v8, v8, v151, v152\nv_max3_u32 v8, v8, v153, v154\nv_max3_u32 v8, v8, v155, v40\nv_max3_u32 v8, v8, v41, v42\nv_max_u32 v8, v8, v43\nv_cmp_eq_u32 vcc, -1, v8\ns_cbranch_vccnz Lms4_%=\n"
"s_branch Lmb4_%=\nLmg4_%=:\ns_mov_b32 s94, 0\ns_branch Lmb4_%=\nLms5_%=:\nds_read_b32 v10, v9 offset:20\ns_waitcnt lgkmcnt(0)\nv_cmp_gt_u32 vcc, 8, v10\ns_cbranch_vccz Lmb5_%=\ns_sub_u32 s95, s95, 1\n"
"s_cbranch_scc0 Lms5_%=\ns_mov_b32 s95, 0\ns_branch Lmb5_%=\nLms6_%=:\ns_sub_u32 s94, s94, 1\ns_cbranch_scc1 Lmg6_%=\ns_add_u32 s92, s76, 0x790000\ns_addc_u32 s93, s77, 0\n"
"global_load_dwordx4 v[120:123], v0, s[92:93] offset:-4096 sc1\nglobal_load_dwordx4 v[124:127], v0, s[92:93] offset:-3072 sc1\nglobal_load_dwordx4 v[68:71], v0, s[92:93] offset:-2048 sc1\n"
"global_load_dwordx4 v[72:75], v0, s[92:93] offset:-1024 sc1\nglobal_load_dwordx2 v[76:77], v252, s[92:93] offset:0 sc1\nglobal_load_dwordx2 v[78:79], v252, s[92:93] offset:128 sc1\ns_waitcnt vmcnt(0)\n"
"v_max3_u32 v8, v120, v121, v122\nv_max3_u32 v8, v8, v123, v124\nv_max3_u32 v8, v8, v125, v126\nv_max3_u32 v8, v8, v127, v68\nv_max3_u32 v8, v8, v69, v70\nv_max3_u32 v8, v8, v71, v72\n"
"v_max3_u32 v8, v8, v73, v74\nv_max3_u32 v8, v8, v75, v76\nv_max3_u32 v8, v8, v77, v78\nv_max_u32 v8, v8, v79\nv_cmp_eq_u32 vcc, -1, v8\ns_cbranch_vccnz Lms6_%=\ns_branch Lmb6_%=\nLmg6_%=:\n"
"s_mov_b32 s94, 0\ns_branch Lmb6_%=\nLms7_%=:\ns_sub_u32 s94, s94, 1\ns_cbranch_scc1 Lmg7_%=\ns_add_u32 s92, s76, 0xc8b000\ns_addc_u32 s93, s77, 0\n"
"global_load_dwordx4 v[144:147], v0, s[92:93] offset:-4096 sc1\nglobal_load_dwordx4 v[148:151], v0, s[92:93] offset:-3072 sc1\nglobal_load_dwordx4 v[152:155], v0, s[92:93] offset:-2048 sc1\n"
"global_load_dwordx4 v[40:43], v0, s[92:93] offset:-1024 sc1\nglobal_load_dwordx4 v[92:95], v0, s[92:93] offset:0 sc1\ns_waitcnt vmcnt(0)\nv_max3_u32 v8, v144, v145, v146\nv_max3_u32 v8, v8, v147, v148\n"
"v_max3_u32 v8, v8, v149, v150\nv_max3_u32 v8, v8, v151, v152\nv_max3_u32 v8, v8, v153, v154\nv_max3_u32 v8, v8, v155, v40\nv_max3_u32 v8, v8, v41, v42\nv_max3_u32 v8, v8, v43, v92\n"
"v_max3_u32 v8, v8, v93, v94\nv_max_u32 v8, v8, v95\nv_cmp_eq_u32 vcc, -1, v8\ns_cbranch_vccnz Lms7_%=\ns_branch Lmb7_%=\nLmg7_%=:\ns_mov_b32 s94, 0\ns_branch Lmb7_%=\nLms8_%=:\n"
"ds_read_b32 v10, v9 offset:0\ns_waitcnt lgkmcnt(0)\nv_cmp_gt_u32 vcc, 3, v10\ns_cbranch_vccz Lmb8_%=\ns_sub_u32 s95, s95, 1\ns_cbranch_scc0 Lms8_%=\ns_mov_b32 s95, 0\ns_branch Lmb8_%=\nLms9_%=:\n"
"s_sub_u32 s94, s94, 1\ns_cbranch_scc1 Lmg9_%=\ns_add_u32 s92, s76, 0xc90000\ns_addc_u32 s93, s77, 0\nglobal_load_dwordx4 v[68:71], v0, s[92:93] offset:-4096 sc1\n"
"global_load_dwordx4 v[180:183], v0, s[92:93] offset:-3072 sc1\nglobal_load_dwordx4 v[184:187], v0, s[92:93] offset:-2048 sc1\nglobal_load_dwordx4 v[156:159], v0, s[92:93] offset:-1024 sc1\n"
"global_load_dwordx4 v[72:75], v0, s[92:93] offset:0 sc1\ns_waitcnt vmcnt(0)\nv_max3_u32 v8, v68, v69, v70\nv_max3_u32 v8, v8, v71, v180\nv_max3_u32 v8, v8, v181, v182\nv_max3_u32 v8, v8, v183, v184\n"
"v_max3_u32 v8, v8, v185, v186\nv_max3_u32 v8, v8, v187, v156\nv_max3_u32 v8, v8, v157, v158\nv_max3_u32 v8, v8, v159, v72\nv_max3_u32 v8, v8, v73, v74\nv_max_u32 v8, v8, v75\nv_cmp_eq_u32 vcc, -1, v8\n"
"s_cbranch_vccnz Lms9_%=\ns_branch Lmb9_%=\nLmg9_%=:\ns_mov_b32 s94, 0\ns_branch Lmb9_%=\nLms10_%=:\nds_read_b32 v10, v9 offset:4\ns_waitcnt lgkmcnt(0)\nv_cmp_gt_u32 vcc, 3, v10\n"
"s_cbranch_vccz Lmb10_%=\ns_sub_u32 s95, s95, 1\ns_cbranch_scc0 Lms10_%=\ns_mov_b32 s95, 0\ns_branch Lmb10_%=\nLms11_%=:\ns_sub_u32 s94, s94, 1\ns_cbranch_scc1 Lmg11_%=\ns_add_u32 s92, s76, 0xc95000\n"
"s_addc_u32 s93, s77, 0\nglobal_load_dwordx4 v[176:179], v0, s[92:93] offset:-4096 sc1\nglobal_load_dwordx4 v[148:151], v0, s[92:93] offset:-3072 sc1\n"
"global_load_dwordx4 v[152:155], v0, s[92:93] offset:-2048 sc1\nglobal_load_dwordx4 v[40:43], v0, s[92:93] offset:-1024 sc1\nglobal_load_dwordx4 v[92:95], v0, s[92:93] offset:0 sc1\ns_waitcnt vmcnt(0)\n"
"v_max3_u32 v8, v176, v177, v178\nv_max3_u32 v8, v8, v179, v148\nv_max3_u32 v8, v8, v149, v150\nv_max3_u32 v8, v8, v151, v152\nv_max3_u32 v8, v8, v153, v154\nv_max3_u32 v8, v8, v155, v40\n"
"v_max3_u32 v8, v8, v41, v42\nv_max3_u32 v8, v8, v43, v92\nv_max3_u32 v8, v8, v93, v94\nv_max_u32 v8, v8, v95\nv_cmp_eq_u32 vcc, -1, v8\ns_cbranch_vccnz Lms11_%=\ns_branch Lmb11_%=\nLmg11_%=:\n"
"s_mov_b32 s94, 0\ns_branch Lmb11_%=\nLms12_%=:\nds_read_b32 v10, v9 offset:8\ns_waitcnt lgkmcnt(0)\nv_cmp_gt_u32 vcc, 3, v10\ns_cbranch_vccz Lmb12_%=\ns_sub_u32 s95, s95, 1\ns_cbranch_scc0 Lms12_%=\n"
"s_mov_b32 s95, 0\ns_branch Lmb12_%=\nLms13_%=:\ns_sub_u32 s94, s94, 1\ns_cbranch_scc1 Lmg13_%=\ns_add_u32 s92, s76, 0xc9a000\ns_addc_u32 s93, s77, 0\n"
"global_load_dwordx4 v[172:175], v0, s[92:93] offset:-4096 sc1\nglobal_load_dwordx4 v[180:183], v0, s[92:93] offset:-3072 sc1\nglobal_load_dwordx4 v[184:187], v0, s[92:93] offset:-2048 sc1\n"
"global_load_dwordx4 v[156:159], v0, s[92:93] offset:-1024 sc1\nglobal_load_dwordx4 v[72:75], v0, s[92:93] offset:0 sc1\ns_waitcnt vmcnt(0)\nv_max3_u32 v8, v172, v173, v174\n"
"v_max3_u32 v8, v8, v175, v180\nv_max3_u32 v8, v8, v181, v182\nv_max3_u32 v8, v8, v183, v184\nv_max3_u32 v8, v8, v185, v186\nv_max3_u32 v8, v8, v187, v156\nv_max3_u32 v8, v8, v157, v158\n"
"v_max3_u32 v8, v8, v159, v72\nv_max3_u32 v8, v8, v73, v74\nv_max_u32 v8, v8, v75\nv_cmp_eq_u32 vcc, -1, v8\ns_cbranch_vccnz Lms13_%=\ns_branch Lmb13_%=\nLmg13_%=:\ns_mov_b32 s94, 0\ns_branch Lmb13_%=\n"
"Lms14_%=:\nds_read_b32 v10, v9 offset:12\ns_waitcnt lgkmcnt(0)\nv_cmp_gt_u32 vcc, 3, v10\ns_cbranch_vccz Lmb14_%=\ns_sub_u32 s95, s95, 1\ns_cbranch_scc0 Lms14_%=\ns_mov_b32 s95, 0\ns_branch Lmb14_%=\n"
"Lms16_%=:\ns_sub_u32 s94, s94, 1\ns_cbranch_scc1 Lmg16_%=\ns_add_u32 s92, s76, 0xa06000\ns_addc_u32 s93, s77, 0\nglobal_load_dwordx4 v[152:155], v253, s[92:93] offset:-4096 sc1\n"
"global_load_dwordx4 v[44:47], v253, s[92:93] offset:-3072 sc1\nglobal_load_dwordx4 v[40:43], v253, s[92:93] offset:-2048 sc1\nglobal_load_dwordx4 v[48:51], v253, s[92:93] offset:-1024 sc1\n"
"global_load_dwordx4 v[92:95], v253, s[92:93] offset:0 sc1\ns_waitcnt vmcnt(0)\nv_max3_u32 v8, v152, v153, v154\nv_max3_u32 v8, v8, v155, v44\nv_max3_u32 v8, v8, v45, v46\nv_max3_u32 v8, v8, v47, v40\n"
"v_max3_u32 v8, v8, v41, v42\nv_max3_u32 v8, v8, v43, v48\nv_max3_u32 v8, v8, v49, v50\nv_max3_u32 v8, v8, v51, v92\nv_max3_u32 v8, v8, v93, v94\nv_max_u32 v8, v8, v95\nv_cmp_eq_u32 vcc, -1, v8\n"
"s_cbranch_vccnz Lms16_%=\ns_branch Lmb16_%=\nLmg16_%=:\ns_mov_b32 s94, 0\ns_branch Lmb16_%=\nLms17_%=:\nds_read_b32 v10, v9 offset:4\ns_waitcnt lgkmcnt(0)\nv_cmp_gt_u32 vcc, 6, v10\n"
"s_cbranch_vccz Lmb17_%=\ns_sub_u32 s95, s95, 1\ns_cbranch_scc0 Lms17_%=\ns_mov_b32 s95, 0\ns_branch Lmb17_%=\nLms18_%=:\ns_sub_u32 s94, s94, 1\ns_cbranch_scc1 Lmg18_%=\ns_add_u32 s92, s76, 0xa01000\n"
"s_addc_u32 s93, s77, 0\nglobal_load_dwordx4 v[80:83], v253, s[92:93] offset:-4096 sc1\nglobal_load_dwordx4 v[128:131], v253, s[92:93] offset:-3072 sc1\n"
"global_load_dwordx4 v[184:187], v253, s[92:93] offset:-2048 sc1\nglobal_load_dwordx4 v[132:135], v253, s[92:93] offset:-1024 sc1\nglobal_load_dwordx4 v[136:139], v253, s[92:93] offset:0 sc1\n"
"s_waitcnt vmcnt(0)\nv_max3_u32 v8, v80, v81, v82\nv_max3_u32 v8, v8, v83, v128\nv_max3_u32 v8, v8, v129, v130\nv_max3_u32 v8, v8, v131, v184\nv_max3_u32 v8, v8, v185, v186\n"
"v_max3_u32 v8, v8, v187, v132\nv_max3_u32 v8, v8, v133, v134\nv_max3_u32 v8, v8, v135, v136\nv_max3_u32 v8, v8, v137, v138\nv_max_u32 v8, v8, v139\nv_cmp_eq_u32 vcc, -1, v8\ns_cbranch_vccnz Lms18_%=\n"
"s_branch Lmb18_%=\nLmg18_%=:\ns_mov_b32 s94, 0\ns_branch Lmb18_%=\nLms19_%=:\nds_read_b32 v10, v9 offset:0\ns_waitcnt lgkmcnt(0)\nv_cmp_gt_u32 vcc, 6, v10\ns_cbranch_vccz Lmb19_%=\n"
"s_sub_u32 s95, s95, 1\ns_cbranch_scc0 Lms19_%=\ns_mov_b32 s95, 0\ns_branch Lmb19_%=\nLms20_%=:\ns_sub_u32 s94, s94, 1\ns_cbranch_scc1 Lmg20_%=\ns_add_u32 s92, s76, 0xa0b000\ns_addc_u32 s93, s77, 0\n"
"global_load_dwordx4 v[152:155], v253, s[92:93] offset:-4096 sc1\nglobal_load_dwordx4 v[180:183], v253, s[92:93] offset:-3072 sc1\nglobal_load_dwordx4 v[52:55], v253, s[92:93] offset:-2048 sc1\n"
"global_load_dwordx4 v[44:47], v253, s[92:93] offset:-1024 sc1\nglobal_load_dwordx4 v[40:43], v253, s[92:93] offset:0 sc1\ns_waitcnt vmcnt(0)\nv_max3_u32 v8, v152, v153, v154\n"
"v_max3_u32 v8, v8, v155, v180\nv_max3_u32 v8, v8, v181, v182\nv_max3_u32 v8, v8, v183, v52\nv_max3_u32 v8, v8, v53, v54\nv_max3_u32 v8, v8, v55, v44\nv_max3_u32 v8, v8, v45, v46\n"
"v_max3_u32 v8, v8, v47, v40\nv_max3_u32 v8, v8, v41, v42\nv_max_u32 v8, v8, v43\nv_cmp_eq_u32 vcc, -1, v8\ns_cbranch_vccnz Lms20_%=\ns_branch Lmb20_%=\nLmg20_%=:\ns_mov_b32 s94, 0\ns_branch Lmb20_%=\n"
"Lms21_%=:\nds_read_b32 v10, v9 offset:8\ns_waitcnt lgkmcnt(0)\nv_cmp_gt_u32 vcc, 6, v10\ns_cbranch_vccz Lmb21_%=\ns_sub_u32 s95, s95, 1\ns_cbranch_scc0 Lms21_%=\ns_mov_b32 s95, 0\ns_branch Lmb21_%=\n"
"Lms22_%=:\ns_sub_u32 s94, s94, 1\ns_cbranch_scc1 Lmg22_%=\ns_add_u32 s92, s76, 0xa10000\ns_addc_u32 s93, s77, 0\nglobal_load_dwordx4 v[168:171], v253, s[92:93] offset:-4096 sc1\n"
"global_load_dwordx4 v[128:131], v253, s[92:93] offset:-3072 sc1\nglobal_load_dwordx4 v[184:187], v253, s[92:93] offset:-2048 sc1\nglobal_load_dwordx4 v[132:135], v253, s[92:93] offset:-1024 sc1\n"
"global_load_dwordx4 v[136:139], v253, s[92:93] offset:0 sc1\ns_waitcnt vmcnt(0)\nv_max3_u32 v8, v168, v169, v170\nv_max3_u32 v8, v8, v171, v128\nv_max3_u32 v8, v8, v129, v130\n"
"v_max3_u32 v8, v8, v131, v184\nv_max3_u32 v8, v8, v185, v186\nv_max3_u32 v8, v8, v187, v132\nv_max3_u32 v8, v8, v133, v134\nv_max3_u32 v8, v8, v135, v136\nv_max3_u32 v8, v8, v137, v138\n"
"v_max_u32 v8, v8, v139\nv_cmp_eq_u32 vcc, -1, v8\ns_cbranch_vccnz Lms22_%=\ns_branch Lmb22_%=\nLmg22_%=:\ns_mov_b32 s94, 0\ns_branch Lmb22_%=\nLms23_%=:\nds_read_b32 v10, v9 offset:12\n"
"s_waitcnt lgkmcnt(0)\nv_cmp_gt_u32 vcc, 6, v10\ns_cbranch_vccz Lmb23_%=\ns_sub_u32 s95, s95, 1\ns_cbranch_scc0 Lms23_%=\ns_mov_b32 s95, 0\ns_branch Lmb23_%=\nLms24_%=:\ns_sub_u32 s94, s94, 1\n"
"s_cbranch_scc1 Lmg24_%=\ns_add_u32 s92, s76, 0xf10000\ns_addc_u32 s93, s77, 0\nglobal_load_dwordx4 v[48:51], v0, s[92:93] offset:-4096 sc1\nglobal_load_dwordx4 v[92:95], v0, s[92:93] offset:-3072 sc1\n"
"global_load_dwordx4 v[140:143], v0, s[92:93] offset:-2048 sc1\nglobal_load_dwordx4 v[224:227], v0, s[92:93] offset:-1024 sc1\nglobal_load_dwordx4 v[228:231], v0, s[92:93] offset:0 sc1\n"
"s_waitcnt vmcnt(0)\nv_max3_u32 v8, v48, v49, v50\nv_max3_u32 v8, v8, v51, v92\nv_max3_u32 v8, v8, v93, v94\nv_max3_u32 v8, v8, v95, v140\nv_max3_u32 v8, v8, v141, v142\nv_max3_u32 v8, v8, v143, v224\n"
"v_max3_u32 v8, v8, v225, v226\nv_max3_u32 v8, v8, v227, v228\nv_max3_u32 v8, v8, v229, v230\nv_max_u32 v8, v8, v231\nv_cmp_eq_u32 vcc, -1, v8\ns_cbranch_vccnz Lms24_%=\ns_branch Lmb24_%=\nLmg24_%=:\n"
"s_mov_b32 s94, 0\ns_branch Lmb24_%=\nLms25_%=:\nds_read_b32 v10, v9 offset:4\ns_waitcnt lgkmcnt(0)\nv_cmp_gt_u32 vcc, 9, v10\ns_cbranch_vccz Lmb25_%=\ns_sub_u32 s95, s95, 1\ns_cbranch_scc0 Lms25_%=\n"
"s_mov_b32 s95, 0\ns_branch Lmb25_%=\nLms26_%=:\ns_sub_u32 s94, s94, 1\ns_cbranch_scc1 Lmg26_%=\ns_add_u32 s92, s76, 0xf0b000\ns_addc_u32 s93, s77, 0\n"
"global_load_dwordx4 v[128:131], v0, s[92:93] offset:-4096 sc1\nglobal_load_dwordx4 v[64:67], v0, s[92:93] offset:-3072 sc1\nglobal_load_dwordx4 v[236:239], v0, s[92:93] offset:-2048 sc1\n"
"global_load_dwordx4 v[176:179], v0, s[92:93] offset:-1024 sc1\nglobal_load_dwordx4 v[184:187], v0, s[92:93] offset:0 sc1\ns_waitcnt vmcnt(0)\nv_max3_u32 v8, v128, v129, v130\n"
"v_max3_u32 v8, v8, v131, v64\nv_max3_u32 v8, v8, v65, v66\nv_max3_u32 v8, v8, v67, v236\nv_max3_u32 v8, v8, v237, v238\nv_max3_u32 v8, v8, v239, v176\nv_max3_u32 v8, v8, v177, v178\n"
"v_max3_u32 v8, v8, v179, v184\nv_max3_u32 v8, v8, v185, v186\nv_max_u32 v8, v8, v187\nv_cmp_eq_u32 vcc, -1, v8\ns_cbranch_vccnz Lms26_%=\ns_branch Lmb26_%=\nLmg26_%=:\ns_mov_b32 s94, 0\n"
"s_branch Lmb26_%=\nLms27_%=:\nds_read_b32 v10, v9 offset:0\ns_waitcnt lgkmcnt(0)\nv_cmp_gt_u32 vcc, 9, v10\ns_cbranch_vccz Lmb27_%=\ns_sub_u32 s95, s95, 1\ns_cbranch_scc0 Lms27_%=\ns_mov_b32 s95, 0\n"
"s_branch Lmb27_%=\nLms28_%=:\ns_sub_u32 s94, s94, 1\ns_cbranch_scc1 Lmg28_%=\ns_add_u32 s92, s76, 0xf15000\ns_addc_u32 s93, s77, 0\nglobal_load_dwordx4 v[20:23], v0, s[92:93] offset:-4096 sc1\n"
"global_load_dwordx4 v[136:139], v0, s[92:93] offset:-3072 sc1\nglobal_load_dwordx4 v[24:27], v0, s[92:93] offset:-2048 sc1\nglobal_load_dwordx4 v[232:235], v0, s[92:93] offset:-1024 sc1\n"
"global_load_dwordx4 v[204:207], v0, s[92:93] offset:0 sc1\ns_waitcnt vmcnt(0)\nv_max3_u32 v8, v20, v21, v22\nv_max3_u32 v8, v8, v23, v136\nv_max3_u32 v8, v8, v137, v138\nv_max3_u32 v8, v8, v139, v24\n"
"v_max3_u32 v8, v8, v25, v26\nv_max3_u32 v8, v8, v27, v232\nv_max3_u32 v8, v8, v233, v234\nv_max3_u32 v8, v8, v235, v204\nv_max3_u32 v8, v8, v205, v206\nv_max_u32 v8, v8, v207\nv_cmp_eq_u32 vcc, -1, v8\n"
"s_cbranch_vccnz Lms28_%=\ns_branch Lmb28_%=\nLmg28_%=:\ns_mov_b32 s94, 0\ns_branch Lmb28_%=\nLms29_%=:\nds_read_b32 v10, v9 offset:8\ns_waitcnt lgkmcnt(0)\nv_cmp_gt_u32 vcc, 9, v10\n"
"s_cbranch_vccz Lmb29_%=\ns_sub_u32 s95, s95, 1\ns_cbranch_scc0 Lms29_%=\ns_mov_b32 s95, 0\ns_branch Lmb29_%=\nLms30_%=:\ns_sub_u32 s94, s94, 1\ns_cbranch_scc1 Lmg30_%=\ns_add_u32 s92, s76, 0xf1a000\n"
"s_addc_u32 s93, s77, 0\nglobal_load_dwordx4 v[144:147], v0, s[92:93] offset:-4096 sc1\nglobal_load_dwordx4 v[124:127], v0, s[92:93] offset:-3072 sc1\n"
"global_load_dwordx4 v[132:135], v0, s[92:93] offset:-2048 sc1\nglobal_load_dwordx4 v[200:203], v0, s[92:93] offset:-1024 sc1\nglobal_load_dwordx4 v[16:19], v0, s[92:93] offset:0 sc1\n"
"s_waitcnt vmcnt(0)\nv_max3_u32 v8, v144, v145, v146\nv_max3_u32 v8, v8, v147, v124\nv_max3_u32 v8, v8, v125, v126\nv_max3_u32 v8, v8, v127, v132\nv_max3_u32 v8, v8, v133, v134\n"
"v_max3_u32 v8, v8, v135, v200\nv_max3_u32 v8, v8, v201, v202\nv_max3_u32 v8, v8, v203, v16\nv_max3_u32 v8, v8, v17, v18\nv_max_u32 v8, v8, v19\nv_cmp_eq_u32 vcc, -1, v8\ns_cbranch_vccnz Lms30_%=\n"
"s_branch Lmb30_%=\nLmg30_%=:\ns_mov_b32 s94, 0\ns_branch Lmb30_%=\nLms31_%=:\nds_read_b32 v10, v9 offset:12\ns_waitcnt lgkmcnt(0)\nv_cmp_gt_u32 vcc, 9, v10\ns_cbranch_vccz Lmb31_%=\n"
"s_sub_u32 s95, s95, 1\ns_cbranch_scc0 Lms31_%=\ns_mov_b32 s95, 0\ns_branch Lmb31_%=\nLms32_%=:\nds_read_b32 v10, v9 offset:4\ns_waitcnt lgkmcnt(0)\nv_cmp_gt_u32 vcc, 11, v10\ns_cbranch_vccz Lmb32_%=\n"
"s_sub_u32 s95, s95, 1\ns_cbranch_scc0 Lms32_%=\ns_mov_b32 s95, 0\ns_branch Lmb32_%=\nLms33_%=:\nds_read_b32 v10, v9 offset:0\ns_waitcnt lgkmcnt(0)\nv_cmp_gt_u32 vcc, 11, v10\ns_cbranch_vccz Lmb33_%=\n"
"s_sub_u32 s95, s95, 1\ns_cbranch_scc0 Lms33_%=\ns_mov_b32 s95, 0\ns_branch Lmb33_%=\nLms34_%=:\nds_read_b32 v10, v9 offset:8\ns_waitcnt lgkmcnt(0)\nv_cmp_gt_u32 vcc, 11, v10\ns_cbranch_vccz Lmb34_%=\n"
"s_sub_u32 s95, s95, 1\ns_cbranch_scc0 Lms34_%=\ns_mov_b32 s95, 0\ns_branch Lmb34_%=\nLms35_%=:\nds_read_b32 v10, v9 offset:12\ns_waitcnt lgkmcnt(0)\nv_cmp_gt_u32 vcc, 11, v10\ns_cbranch_vccz Lmb35_%=\n"
"s_sub_u32 s95, s95, 1\ns_cbranch_scc0 Lms35_%=\ns_mov_b32 s95, 0\ns_branch Lmb35_%=\nLms36_%=:\ns_sub_u32 s94, s94, 1\ns_cbranch_scc1 Lmg36_%=\ns_add_u32 s92, s78, 0xd000\ns_addc_u32 s93, s79, 0\n"
"global_load_dwordx4 v[128:131], v0, s[92:93] offset:-4096 sc1\ns_add_u32 s92, s78, 0x1000\ns_addc_u32 s93, s79, 0\nglobal_load_dwordx4 v[196:199], v0, s[92:93] offset:-4096 sc1\n"
"s_add_u32 s92, s78, 0x19000\ns_addc_u32 s93, s79, 0\nglobal_load_dwordx4 v[64:67], v0, s[92:93] offset:-4096 sc1\ns_add_u32 s92, s78, 0x25000\ns_addc_u32 s93, s79, 0\n"
"global_load_dwordx4 v[108:111], v0, s[92:93] offset:-4096 sc1\ns_waitcnt vmcnt(0)\nv_max3_u32 v8, v128, v129, v130\nv_max3_u32 v8, v8, v131, v196\nv_max3_u32 v8, v8, v197, v198\n"
"v_max3_u32 v8, v8, v199, v64\nv_max3_u32 v8, v8, v65, v66\nv_max3_u32 v8, v8, v67, v108\nv_max3_u32 v8, v8, v109, v110\nv_max_u32 v8, v8, v111\nv_cmp_eq_u32 vcc, -1, v8\ns_cbranch_vccnz Lms36_%=\n"
"s_branch Lmb36_%=\nLmg36_%=:\ns_mov_b32 s94, 0\ns_branch Lmb36_%=\nLms37_%=:\ns_sub_u32 s94, s94, 1\ns_cbranch_scc1 Lmg37_%=\ns_add_u32 s92, s78, 0xd400\ns_addc_u32 s93, s79, 0\n"
"global_load_dwordx4 v[136:139], v0, s[92:93] offset:-4096 sc1\ns_add_u32 s92, s78, 0x1400\ns_addc_u32 s93, s79, 0\nglobal_load_dwordx4 v[236:239], v0, s[92:93] offset:-4096 sc1\n"
"s_add_u32 s92, s78, 0x19400\ns_addc_u32 s93, s79, 0\nglobal_load_dwordx4 v[24:27], v0, s[92:93] offset:-4096 sc1\ns_add_u32 s92, s78, 0x25400\ns_addc_u32 s93, s79, 0\n"
"global_load_dwordx4 v[232:235], v0, s[92:93] offset:-4096 sc1\ns_waitcnt vmcnt(0)\nv_max3_u32 v8, v136, v137, v138\nv_max3_u32 v8, v8, v139, v236\nv_max3_u32 v8, v8, v237, v238\n"
"v_max3_u32 v8, v8, v239, v24\nv_max3_u32 v8, v8, v25, v26\nv_max3_u32 v8, v8, v27, v232\nv_max3_u32 v8, v8, v233, v234\nv_max_u32 v8, v8, v235\nv_cmp_eq_u32 vcc, -1, v8\ns_cbranch_vccnz Lms37_%=\n"
"s_branch Lmb37_%=\nLmg37_%=:\ns_mov_b32 s94, 0\ns_branch Lmb37_%=\nLms38_%=:\ns_sub_u32 s94, s94, 1\ns_cbranch_scc1 Lmg38_%=\ns_add_u32 s92, s78, 0xe800\ns_addc_u32 s93, s79, 0\n"
"global_load_dwordx4 v[72:75], v0, s[92:93] offset:-4096 sc1\ns_add_u32 s92, s78, 0x2800\ns_addc_u32 s93, s79, 0\nglobal_load_dwordx4 v[220:223], v0, s[92:93] offset:-4096 sc1\n"
"s_add_u32 s92, s78, 0x1a800\ns_addc_u32 s93, s79, 0\nglobal_load_dwordx4 v[144:147], v0, s[92:93] offset:-4096 sc1\ns_add_u32 s92, s78, 0x26800\ns_addc_u32 s93, s79, 0\n"
"global_load_dwordx4 v[32:35], v0, s[92:93] offset:-4096 sc1\ns_waitcnt vmcnt(0)\nv_max3_u32 v8, v72, v73, v74\nv_max3_u32 v8, v8, v75, v220\nv_max3_u32 v8, v8, v221, v222\n"
"v_max3_u32 v8, v8, v223, v144\nv_max3_u32 v8, v8, v145, v146\nv_max3_u32 v8, v8, v147, v32\nv_max3_u32 v8, v8, v33, v34\nv_max_u32 v8, v8, v35\nv_cmp_eq_u32 vcc, -1, v8\ns_cbranch_vccnz Lms38_%=\n"
"s_branch Lmb38_%=\nLmg38_%=:\ns_mov_b32 s94, 0\ns_branch Lmb38_%=\nLms39_%=:\ns_sub_u32 s94, s94, 1\ns_cbranch_scc1 Lmg39_%=\ns_add_u32 s92, s78, 0xec00\ns_addc_u32 s93, s79, 0\n"
"global_load_dwordx4 v[196:199], v0, s[92:93] offset:-4096 sc1\ns_add_u32 s92, s78, 0x2c00\ns_addc_u32 s93, s79, 0\nglobal_load_dwordx4 v[64:67], v0, s[92:93] offset:-4096 sc1\n"
"s_add_u32 s92, s78, 0x1ac00\ns_addc_u32 s93, s79, 0\nglobal_load_dwordx4 v[108:111], v0, s[92:93] offset:-4096 sc1\ns_add_u32 s92, s78, 0x26c00\ns_addc_u32 s93, s79, 0\n"
"global_load_dwordx4 v[168:171], v0, s[92:93] offset:-4096 sc1\ns_waitcnt vmcnt(0)\nv_max3_u32 v8, v196, v197, v198\nv_max3_u32 v8, v8, v199, v64\nv_max3_u32 v8, v8, v65, v66\n"
"v_max3_u32 v8, v8, v67, v108\nv_max3_u32 v8, v8, v109, v110\nv_max3_u32 v8, v8, v111, v168\nv_max3_u32 v8, v8, v169, v170\nv_max_u32 v8, v8, v171\nv_cmp_eq_u32 vcc, -1, v8\ns_cbranch_vccnz Lms39_%=\n"
"s_branch Lmb39_%=\nLmg39_%=:\ns_mov_b32 s94, 0\ns_branch Lmb39_%=\nLms40_%=:\ns_sub_u32 s94, s94, 1\ns_cbranch_scc1 Lmg40_%=\ns_add_u32 s92, s78, 0xd800\ns_addc_u32 s93, s79, 0\n"
"global_load_dwordx4 v[236:239], v0, s[92:93] offset:-4096 sc1\ns_add_u32 s92, s78, 0x1800\ns_addc_u32 s93, s79, 0\nglobal_load_dwordx4 v[24:27], v0, s[92:93] offset:-4096 sc1\n"
"s_add_u32 s92, s78, 0x19800\ns_addc_u32 s93, s79, 0\nglobal_load_dwordx4 v[232:235], v0, s[92:93] offset:-4096 sc1\ns_add_u32 s92, s78, 0x25800\ns_addc_u32 s93, s79, 0\n"
"global_load_dwordx4 v[36:39], v0, s[92:93] offset:-4096 sc1\ns_waitcnt vmcnt(0)\nv_max3_u32 v8, v236, v237, v238\nv_max3_u32 v8, v8, v239, v24\nv_max3_u32 v8, v8, v25, v26\n"
"v_max3_u32 v8, v8, v27, v232\nv_max3_u32 v8, v8, v233, v234\nv_max3_u32 v8, v8, v235, v36\nv_max3_u32 v8, v8, v37, v38\nv_max_u32 v8, v8, v39\nv_cmp_eq_u32 vcc, -1, v8\ns_cbranch_vccnz Lms40_%=\n"
"s_branch Lmb40_%=\nLmg40_%=:\ns_mov_b32 s94, 0\ns_branch Lmb40_%=\nLms41_%=:\ns_sub_u32 s94, s94, 1\ns_cbranch_scc1 Lmg41_%=\ns_add_u32 s92, s78, 0xdc00\ns_addc_u32 s93, s79, 0\n"
"global_load_dwordx4 v[220:223], v0, s[92:93] offset:-4096 sc1\ns_add_u32 s92, s78, 0x1c00\ns_addc_u32 s93, s79, 0\nglobal_load_dwordx4 v[144:147], v0, s[92:93] offset:-4096 sc1\n"
"s_add_u32 s92, s78, 0x19c00\ns_addc_u32 s93, s79, 0\nglobal_load_dwordx4 v[32:35], v0, s[92:93] offset:-4096 sc1\ns_add_u32 s92, s78, 0x25c00\ns_addc_u32 s93, s79, 0\n"
"global_load_dwordx4 v[92:95], v0, s[92:93] offset:-4096 sc1\ns_waitcnt vmcnt(0)\nv_max3_u32 v8, v220, v221, v222\nv_max3_u32 v8, v8, v223, v144\nv_max3_u32 v8, v8, v145, v146\n"
"v_max3_u32 v8, v8, v147, v32\nv_max3_u32 v8, v8, v33, v34\nv_max3_u32 v8, v8, v35, v92\nv_max3_u32 v8, v8, v93, v94\nv_max_u32 v8, v8, v95\nv_cmp_eq_u32 vcc, -1, v8\ns_cbranch_vccnz Lms41_%=\n"
"s_branch Lmb41_%=\nLmg41_%=:\ns_mov_b32 s94, 0\ns_branch Lmb41_%=\nLms42_%=:\ns_sub_u32 s94, s94, 1\ns_cbranch_scc1 Lmg42_%=\ns_add_u32 s92, s78, 0xf000\ns_addc_u32 s93, s79, 0\n"
"global_load_dwordx4 v[64:67], v0, s[92:93] offset:-4096 sc1\ns_add_u32 s92, s78, 0x3000\ns_addc_u32 s93, s79, 0\nglobal_load_dwordx4 v[108:111], v0, s[92:93] offset:-4096 sc1\n"
"s_add_u32 s92, s78, 0x1b000\ns_addc_u32 s93, s79, 0\nglobal_load_dwordx4 v[168:171], v0, s[92:93] offset:-4096 sc1\ns_add_u32 s92, s78, 0x27000\ns_addc_u32 s93, s79, 0\n"
"global_load_dwordx4 v[192:195], v0, s[92:93] offset:-4096 sc1\ns_waitcnt vmcnt(0)\nv_max3_u32 v8, v64, v65, v66\nv_max3_u32 v8, v8, v67, v108\nv_max3_u32 v8, v8, v109, v110\n"
"v_max3_u32 v8, v8, v111, v168\nv_max3_u32 v8, v8, v169, v170\nv_max3_u32 v8, v8, v171, v192\nv_max3_u32 v8, v8, v193, v194\nv_max_u32 v8, v8, v195\nv_cmp_eq_u32 vcc, -1, v8\ns_cbranch_vccnz Lms42_%=\n"
"s_branch Lmb42_%=\nLmg42_%=:\ns_mov_b32 s94, 0\ns_branch Lmb42_%=\nLms43_%=:\ns_sub_u32 s94, s94, 1\ns_cbranch_scc1 Lmg43_%=\ns_add_u32 s92, s78, 0xf400\ns_addc_u32 s93, s79, 0\n"
"global_load_dwordx4 v[24:27], v0, s[92:93] offset:-4096 sc1\ns_add_u32 s92, s78, 0x3400\ns_addc_u32 s93, s79, 0\nglobal_load_dwordx4 v[232:235], v0, s[92:93] offset:-4096 sc1\n"
"s_add_u32 s92, s78, 0x1b400\ns_addc_u32 s93, s79, 0\nglobal_load_dwordx4 v[36:39], v0, s[92:93] offset:-4096 sc1\ns_add_u32 s92, s78, 0x27400\ns_addc_u32 s93, s79, 0\n"
"global_load_dwordx4 v[176:179], v0, s[92:93] offset:-4096 sc1\ns_waitcnt vmcnt(0)\nv_max3_u32 v8, v24, v25, v26\nv_max3_u32 v8, v8, v27, v232\nv_max3_u32 v8, v8, v233, v234\n"
"v_max3_u32 v8, v8, v235, v36\nv_max3_u32 v8, v8, v37, v38\nv_max3_u32 v8, v8, v39, v176\nv_max3_u32 v8, v8, v177, v178\nv_max_u32 v8, v8, v179\nv_cmp_eq_u32 vcc, -1, v8\ns_cbranch_vccnz Lms43_%=\n"
"s_branch Lmb43_%=\nLmg43_%=:\ns_mov_b32 s94, 0\ns_branch Lmb43_%=\nLms44_%=:\ns_sub_u32 s94, s94, 1\ns_cbranch_scc1 Lmg44_%=\ns_add_u32 s92, s78, 0xe000\ns_addc_u32 s93, s79, 0\n"
"global_load_dwordx4 v[144:147], v0, s[92:93] offset:-4096 sc1\nglobal_load_dwordx4 v[32:35], v0, s[92:93] offset:-3072 sc1\nglobal_load_dwordx4 v[92:95], v0, s[92:93] offset:2048 sc1\n"
"global_load_dwordx4 v[208:211], v0, s[92:93] offset:3072 sc1\ns_waitcnt vmcnt(0)\nv_max3_u32 v8, v144, v145, v146\nv_max3_u32 v8, v8, v147, v32\nv_max3_u32 v8, v8, v33, v34\n"
"v_max3_u32 v8, v8, v35, v92\nv_max3_u32 v8, v8, v93, v94\nv_max3_u32 v8, v8, v95, v208\nv_max3_u32 v8, v8, v209, v210\nv_max_u32 v8, v8, v211\nv_cmp_eq_u32 vcc, -1, v8\ns_cbranch_vccnz Lms44_%=\n"
"s_branch Lmb44_%=\nLmg44_%=:\ns_mov_b32 s94, 0\ns_branch Lmb44_%=\nLms45_%=:\nds_read_b32 v10, v9 offset:4\ns_waitcnt lgkmcnt(0)\nv_cmp_gt_u32 vcc, 13, v10\ns_cbranch_vccz Lmb45_%=\n"
"s_sub_u32 s95, s95, 1\ns_cbranch_scc0 Lms45_%=\ns_mov_b32 s95, 0\ns_branch Lmb45_%=\nLms46_%=:\ns_sub_u32 s94, s94, 1\ns_cbranch_scc1 Lmg46_%=\ns_add_u32 s92, s78, 0x2000\ns_addc_u32 s93, s79, 0\n"
"global_load_dwordx4 v[128:131], v0, s[92:93] offset:-4096 sc1\nglobal_load_dwordx4 v[160:163], v0, s[92:93] offset:-3072 sc1\nglobal_load_dwordx4 v[64:67], v0, s[92:93] offset:2048 sc1\n"
"global_load_dwordx4 v[108:111], v0, s[92:93] offset:3072 sc1\ns_waitcnt vmcnt(0)\nv_max3_u32 v8, v128, v129, v130\nv_max3_u32 v8, v8, v131, v160\nv_max3_u32 v8, v8, v161, v162\n"
"v_max3_u32 v8, v8, v163, v64\nv_max3_u32 v8, v8, v65, v66\nv_max3_u32 v8, v8, v67, v108\nv_max3_u32 v8, v8, v109, v110\nv_max_u32 v8, v8, v111\nv_cmp_eq_u32 vcc, -1, v8\ns_cbranch_vccnz Lms46_%=\n"
"s_branch Lmb46_%=\nLmg46_%=:\ns_mov_b32 s94, 0\ns_branch Lmb46_%=\nLms47_%=:\nds_read_b32 v10, v9 offset:0\ns_waitcnt lgkmcnt(0)\nv_cmp_gt_u32 vcc, 13, v10\ns_cbranch_vccz Lmb47_%=\n"
"s_sub_u32 s95, s95, 1\ns_cbranch_scc0 Lms47_%=\ns_mov_b32 s95, 0\ns_branch Lmb47_%=\nLms48_%=:\ns_sub_u32 s94, s94, 1\ns_cbranch_scc1 Lmg48_%=\ns_add_u32 s92, s78, 0x1a000\ns_addc_u32 s93, s79, 0\n"
"global_load_dwordx4 v[168:171], v0, s[92:93] offset:-4096 sc1\nglobal_load_dwordx4 v[192:195], v0, s[92:93] offset:-3072 sc1\nglobal_load_dwordx4 v[12:15], v0, s[92:93] offset:2048 sc1\n"
"global_load_dwordx4 v[156:159], v0, s[92:93] offset:3072 sc1\ns_waitcnt vmcnt(0)\nv_max3_u32 v8, v168, v169, v170\nv_max3_u32 v8, v8, v171, v192\nv_max3_u32 v8, v8, v193, v194\n"
"v_max3_u32 v8, v8, v195, v12\nv_max3_u32 v8, v8, v13, v14\nv_max3_u32 v8, v8, v15, v156\nv_max3_u32 v8, v8, v157, v158\nv_max_u32 v8, v8, v159\nv_cmp_eq_u32 vcc, -1, v8\ns_cbranch_vccnz Lms48_%=\n"
"s_branch Lmb48_%=\nLmg48_%=:\ns_mov_b32 s94, 0\ns_branch Lmb48_%=\nLms49_%=:\nds_read_b32 v10, v9 offset:8\ns_waitcnt lgkmcnt(0)\nv_cmp_gt_u32 vcc, 13, v10\ns_cbranch_vccz Lmb49_%=\n"
"s_sub_u32 s95, s95, 1\ns_cbranch_scc0 Lms49_%=\ns_mov_b32 s95, 0\ns_branch Lmb49_%=\nLms50_%=:\ns_sub_u32 s94, s94, 1\ns_cbranch_scc1 Lmg50_%=\ns_add_u32 s92, s78, 0x26000\ns_addc_u32 s93, s79, 0\n"
"global_load_dwordx4 v[40:43], v0, s[92:93] offset:-4096 sc1\nglobal_load_dwordx4 v[136:139], v0, s[92:93] offset:-3072 sc1\nglobal_load_dwordx4 v[100:103], v0, s[92:93] offset:2048 sc1\n"
"global_load_dwordx4 v[24:27], v0, s[92:93] offset:3072 sc1\ns_waitcnt vmcnt(0)\nv_max3_u32 v8, v40, v41, v42\nv_max3_u32 v8, v8, v43, v136\nv_max3_u32 v8, v8, v137, v138\nv_max3_u32 v8, v8, v139, v100\n"
"v_max3_u32 v8, v8, v101, v102\nv_max3_u32 v8, v8, v103, v24\nv_max3_u32 v8, v8, v25, v26\nv_max_u32 v8, v8, v27\nv_cmp_eq_u32 vcc, -1, v8\ns_cbranch_vccnz Lms50_%=\ns_branch Lmb50_%=\nLmg50_%=:\n"
"s_mov_b32 s94, 0\ns_branch Lmb50_%=\nLms51_%=:\nds_read_b32 v10, v9 offset:12\ns_waitcnt lgkmcnt(0)\nv_cmp_gt_u32 vcc, 13, v10\ns_cbranch_vccz Lmb51_%=\ns_sub_u32 s95, s95, 1\ns_cbranch_scc0 Lms51_%=\n"
"s_mov_b32 s95, 0\ns_branch Lmb51_%=\nLmend_%=:\n"

        : "={v11}"(stv), "+{v6}"(pfv), "+{v[236:251]}"(pre0), "+{v[220:235]}"(pre1), "+{v[204:219]}"(pre2), "+{v[188:203]}"(pre3), "+{v[172:187]}"(pre4), "+{v[156:171]}"(pre5)
        : "{v0}"(lane * 16), "{v1}"(xw), "{v2}"(xw + 65536), "{v3}"(xw + 131072), "{v4}"(lds0 + L_IF + w * 4),
          "{v7}"(lds0 + L_ST + l15 * 32 + w * 8), "{v9}"(lds0 + L_DN), "{v252}"(g * 256 + l15 * 8),
          "{v253}"((sr >> 4) * (DM * 32) + g * 256 + (sr & 15) * 16),
          "{s[76:77]}"(bA), "{s[78:79]}"(bG), "{s[80:81]}"(bB), "{s[82:83]}"(bF)
        : "memory", "vcc", "scc", "v5", "v8", "v10", "v12", "v13", "v14", "v15", "v16", "v17", "v18", "v19", "v20", "v21", "v22", "v23", "v24", "v25", "v26", "v27", "v28", "v29", "v30", "v31", "v32", "v33", "v34", "v35", "v36", "v37", "v38", "v39", "v40", "v41", "v42", "v43", "v44", "v45", "v46", "v47", "v48", "v49", "v50", "v51", "v52", "v53", "v54", "v55", "v56", "v57", "v58", "v59", "v60", "v61", "v62", "v63", "v64", "v65", "v66", "v67", "v68", "v69", "v70", "v71", "v72", "v73", "v74", "v75", "v76", "v77", "v78", "v79", "v80", "v81", "v82", "v83", "v84", "v85", "v86", "v87", "v88", "v89", "v90", "v91", "v92", "v93", "v94", "v95", "v96", "v97", "v98", "v99", "v100", "v101", "v102", "v103", "v104", "v105", "v106", "v107", "v108", "v109", "v110", "v111", "v112", "v113", "v114", "v115", "v116", "v117", "v118", "v119", "v120", "v121", "v122", "v123", "v124", "v125", "v126", "v127", "v128", "v129", "v130", "v131", "v132", "v133", "v134", "v135", "v136", "v137", "v138", "v139", "v140", "v141", "v142", "v143", "v144", "v145", "v146", "v147", "v148", "v149", "v150", "v151", "v152", "v153", "v154", "v155", "s86", "s87", "s88", "s89", "s90", "s91", "s92", "s93", "s94", "s95", "s96", "s97", "s98", "s99", "s100", "s101"
);
    MST_FLUSH();
    CODE_MARK(Lf_e2);
    return;
  }

  __builtin_amdgcn_s_setprio(EPRIO);
  const unsigned flb = lds0 + L_IF, dnb = lds0 + L_DN;
  f32x4 pa[5], pb[5];
  {
    const unsigned c0 = (unsigned)(5 * g * CSN + (e == 0 ? ct * 80 + 4 * l15 : DM + ct * 192 + (e - 1) * 64 + 4 * l15)) * 4;
    const unsigned c1 = (unsigned)(5 * g * CSN + ct * 80 + 64 + 4 * (l15 & 3)) * 4;
#pragma unroll
    for (int k = 0; k < 5; ++k) {
      asm volatile("global_load_dwordx4 %0, %1, %2" : "=v"(pa[k]) : "v"(c0 + k * (CSN * 4)), "s"(ws + W_CS) : "memory");
      asm volatile("global_load_dwordx4 %0, %1, %2" : "=v"(pb[k]) : "v"(c1 + k * (CSN * 4)), "s"(ws + W_CS) : "memory");
    }
  }
  fill_x1();
  TOUCH_AT(1);
  const unsigned psrc = lds0 + (g >> 1) * 4096 + (g & 1) * 512 + l15 * 16;
  const unsigned ssrc = lds0 + L_ST + l15 * 32;
  auto stats = [&](float s1, float s2, float& mean, float& rs, float& var) {
    mean = s1 * (1.0f / DM);
    var = fmaxf(s2 * (1.0f / DM) - mean * mean, 0.f);
    rs = __builtin_amdgcn_rsqf(var + LN_EPS);
  };
  float mu[3], rr[3];
  u32x4 ya, yb;
  u32x2 yc;
  auto item = [&](auto MC, auto TC, int rb) {
    constexpr int MODE = decltype(MC)::v, T = decltype(TC)::v, N = T < 2 ? 8 : 4;
    constexpr unsigned tag = MODE == 7 ? 7 : MODE - 2;
    float f[8], cs[8], s1, s2;
    if constexpr (MODE == 4) {
      const f32x4 c0 = *(const f32x4*)(lds + L_CS + (T < 2 ? 32 * T + 8 * g : 64 + 4 * g) * 4);
#pragma unroll
      for (int c = 0; c < 4; ++c) cs[c] = c0[c];
      if constexpr (T < 2) {
        const f32x4 c1 = *(const f32x4*)(lds + L_CS + (32 * T + 8 * g + 4) * 4);
#pragma unroll
        for (int c = 0; c < 4; ++c) cs[4 + c] = c1[c];
      }
    }
    const int row = rt * 64 + rb * 16 + l15;
    const int oct = T < 2 ? 4 * T + g : 8 + (g >> 1);
    unsigned oa = MODE == 7 ? (unsigned)(row * DM + ct * 80 + oct * 8) * 2
                            : (unsigned)((row >> 4) * (DM * 16) + (ct * 10 + oct) * 128 + (row & 15) * 8) * 2;
    tie(oa);
    char* ob = MODE == 7 ? (char*)OUT : ws + (MODE == 3 ? W_H : MODE == 4 ? W_HH : W_X1);
    item_read<(T < 2), MODE == 4>(flb + (rb * 3 + T) * 16, tag, T < 2 ? psrc + rb * 20480 + T * 8192 : lds0 + rb * 20480 + 16384 + lane * 16,
                                  ssrc + rb * 512, f, s1, s2);
    if constexpr (MODE != 7) lds_done(dnb + rb * 4);
    if constexpr (MODE == 4) {
      float var;
      stats(s1, s2, mu[T], rr[T], var);
      const float rs2 = rr[T] * __builtin_amdgcn_rsqf(var * rr[T] * rr[T] + LN_EPS);
#pragma unroll
      for (int c = 0; c < N; ++c) f[c] = gelu_t((f[c] - mu[T] * cs[c]) * rs2);
    } else {
#pragma unroll
      for (int c = 0; c < N; ++c) {
        const unsigned hv = T == 0 ? xa[c >> 1] : T == 1 ? xb[c >> 1] : xc[(c >> 1) & 1];
        const float hf = (c & 1) ? bfhi(hv) : bflo(hv);
        if constexpr (MODE == 3) f[c] += hf;
        if constexpr (MODE == 5) f[c] += (hf - mu[T]) * rr[T];
        if constexpr (MODE == 7) {
          const unsigned yv = T == 0 ? ya[c >> 1] : T == 1 ? yb[c >> 1] : yc[(c >> 1) & 1];
          f[c] = (f[c] + ((c & 1) ? bfhi(yv) : bflo(yv))) + hf;
        }
      }
    }
    u32x4 o;
    if constexpr (T < 2) {
      o = u32x4{pack2(f[0], f[1]), pack2(f[2], f[3]), pack2(f[4], f[5]), pack2(f[6], f[7])};
      if constexpr (MODE != 7) {
#pragma unroll
        for (int c = 0; c < 4; ++c) o[c] = nosen(o[c]);
      }
      if constexpr (MODE == 7) gst16o(oa, o, ob); else gst16s(oa, o, ob);
      if constexpr (MODE == 3) { if constexpr (T == 0) xa = o; else xb = o; }
      if constexpr (MODE == 5) { if constexpr (T == 0) ya = o; else yb = o; }
    } else {
      u32x2 o2 = {pack2(f[0], f[1]), pack2(f[2], f[3])};
      if constexpr (MODE != 7) { o2[0] = nosen(o2[0]); o2[1] = nosen(o2[1]); }
      o = widen(o2);
      if ((g & 1) == 0) {
        if constexpr (MODE == 7) gst16o(oa, o, ob); else gst16s(oa, o, ob);
      }
      if constexpr (MODE == 3) xc = o2;
      if constexpr (MODE == 5) yc = o2;
    }
  };
  auto phase = [&](auto MC) {
    constexpr int MODE = decltype(MC)::v;
#pragma clang loop unroll(disable)
    for (int pos = 0; pos < 4; ++pos) {
      const int rb = (MODE >= 5 && pos < 2) ? 1 - pos : pos;
      const int t = (e - rb) & 3;
      if constexpr (MODE == 3 || (MODE == 5 && FILLA == 0)) { if (pos == 2) wait_vm<1>(); }
      if (t == 0) item(MC, IC<0>{}, rb);
      else if (t == 1) item(MC, IC<1>{}, rb);
      else if (t == 2) item(MC, IC<2>{}, rb);
      MST(1 + (MODE - 3) * 4 + pos);
    }
  };
  phase(IC<3>{});
  {
#pragma unroll
    for (int k = 0; k < 5; ++k) { tie(pa[k]); tie(pb[k]); }
    f32x4 sa = ((pa[0] + pa[1]) + (pa[2] + pa[3])) + pa[4];
#pragma unroll
    for (int c = 0; c < 4; ++c) sa[c] = gsum(sa[c]);
    if (g == 0) *(f32x4*)(lds + L_CS + (e == 0 ? 0 : 64 + e * 256) + l15 * 16) = sa;
    if (e == 0) {
      f32x4 sb = ((pb[0] + pb[1]) + (pb[2] + pb[3])) + pb[4];
#pragma unroll
      for (int c = 0; c < 4; ++c) sb[c] = gsum(sb[c]);
      if (lane < 4) *(f32x4*)(lds + L_CS + 256 + lane * 16) = sb;
    }
    lds_done(dnb + 16);
  }
  TOUCH_AT(2);
  fill_g(0); fill_g(1);
  lds_wait1(dnb + 16, 4);
  phase(IC<4>{});
  TOUCH_AT(3);
  fill_g(2);
  phase(IC<5>{});
  TOUCH_AT(4);
  {
    float m6[2], r6[2];
    const int p = e & 1;
    auto item6 = [&](auto FC, int c, int q) {
      constexpr bool FIRST = decltype(FC)::v;
      const int rb = q ? 2 + (e >> 1) : 1 - (e >> 1);
      const int col = c * 64 + p * 32 + 8 * g;
      const f32x4 c0 = *(const f32x4*)(lds + L_CS + 320 + col * 4), c1 = *(const f32x4*)(lds + L_CS + 320 + col * 4 + 16);
      unsigned oa = (unsigned)((rt * 4 + rb) * (FF * 16) + (ct * 24 + (col >> 3)) * 128 + l15 * 8) * 2;
      tie(oa);
      float f[8], s1, s2;
      item_read<true, FIRST>(flb + (rb * 3 + p) * 16, 4 + c, psrc + (c == 1 ? 20 + rb * 4 + 2 * p : rb * 5 + 2 * p) * 4096, ssrc + rb * 512, f, s1, s2);
      if (c != 1) lds_done(dnb + rb * 4);
      if constexpr (FIRST) { float var; stats(s1, s2, m6[q], r6[q], var); }
#pragma unroll
      for (int k = 0; k < 8; ++k) f[k] = gelu_t((f[k] - m6[q] * (k < 4 ? c0[k & 3] : c1[k & 3])) * r6[q]);
      const u32x4 o = {nosen(pack2(f[0], f[1])), nosen(pack2(f[2], f[3])), nosen(pack2(f[4], f[5])), nosen(pack2(f[6], f[7]))};
      gst16s(oa, o, ws + W_G);
    };
    item6(IC<1>{}, 0, 0); item6(IC<1>{}, 0, 1);
    TOUCH_AT(5);
    MST(13);
#pragma clang loop unroll(disable)
    for (int c = 1; c < 3; ++c) {
      item6(IC<0>{}, c, 0); item6(IC<0>{}, c, 1);
      MST(13 + c);
    }
  }
  phase(IC<7>{});
  tie(pfv);
  MST(27);
  MST_FLUSH();
  CODE_MARK(Lf_e);
}

extern "C" void kernel_launch(void* const* d_in, const int* in_sizes, int n_in, void* d_out, int out_size,
                              void* d_ws, size_t ws_size, hipStream_t stream) {
  const u16* x = (const u16*)d_in[0];
  char* ws = (char*)d_ws;
  qkv_k<<<256, 512, 0, stream>>>(x, (const u16*)d_in[1], ws, (const u16*)d_in[2], (const u16*)d_in[4], (const u16*)d_in[5],
                                 (const u16*)d_in[6], (const u16*)d_in[7]);
  fused_k<<<256, 512, 0, stream>>>(ws, x, (u16*)d_out);
}
```
